```python
import torch

def _softmax(x, axis=-1):
    return torch.softmax(x, dim=axis)
def _gelu(x):
    return torch.nn.functional.gelu(x, approximate="tanh")
def _silu(x):
    return torch.nn.functional.silu(x)
def _sigmoid(x):
    return torch.sigmoid(x)
def _layernorm(x, axis=-1, eps=1e-5):
    m = x.mean(dim=axis, keepdim=True)
    v = x.var(dim=axis, keepdim=True, unbiased=False)
    return (x - m) / torch.sqrt(v + eps)
def _scatter_add(tgt, idx, src, axis=0):
    return tgt.clone().index_add_(axis, idx, src)
def _segment_sum(src, seg, n):
    out = torch.zeros((n,) + src.shape[1:], dtype=src.dtype)
    return out.index_add_(0, seg, src)
def _pad(x, pw):
    flat = tuple(v for pair in reversed(pw) for v in pair)
    return torch.nn.functional.pad(x, flat)

kernel_name = 'concat_heads__sliding_block_cached__compressed_kv_cla__decode'

def setup_inputs():

    _w = (lambda a: (lambda t: t.to(torch.bfloat16) if t.is_floating_point() else t)(torch.as_tensor(a)))
    def _A_setup():
        B, Sq, D = 2, 1, 576
        Hq, Hkv, Sk, Dh = 32, 16, 8192, 128
        assert Hq % Hkv == 0, (Hq, Hkv)
        return {
            "x": _w((lambda *s: torch.randn(*s, dtype=torch.bfloat16) * 1.6)(B, Sq, D)),
            "k_cache": _w((lambda *s: torch.randn(*s, dtype=torch.bfloat16) * 1.6)(B, Hkv, Sk, Dh)),
            "v_cache": _w((lambda *s: torch.randn(*s, dtype=torch.bfloat16) * 0.05)(B, Hkv, Sk, Dh)),
            "wq": _w((lambda *s: torch.randn(*s, dtype=torch.bfloat16) * 1.6)(D, Hq * Dh) * (D ** -0.5)),
            "wo": _w((lambda *s: torch.randn(*s, dtype=torch.bfloat16) * 0.05)(Hq * Dh, D) * ((Hq * Dh) ** -0.5)),
        }
    _A_in = _A_setup()

    def _B_setup():
        B, H, Sq, Sk, D, M = 2, 32, 1, 8192, 128, 576
        Dc = 4 * D
        return {
            "x": _w((lambda *s: torch.randn(*s, dtype=torch.bfloat16) * 3.2)(B, Sq, M)),
            "c_kv": _w((lambda *s: torch.randn(*s, dtype=torch.bfloat16) * 0.1)(B, Sk, Dc)),
            "w_q": _w((lambda *s: torch.randn(*s, dtype=torch.bfloat16) * 6.4)(M, H, D) * (M ** -0.5)),
            "w_uk": _w((lambda *s: torch.randn(*s, dtype=torch.bfloat16) * 6.4)(Dc, H, D) * (Dc ** -0.5)),
            "w_uv": _w((lambda *s: torch.randn(*s, dtype=torch.bfloat16) * 0.1)(Dc, H, D) * (Dc ** -0.5)),
            "w_o": _w((lambda *s: torch.randn(*s, dtype=torch.bfloat16) * 0.1)(H, D, M) * ((H * D) ** -0.5)),
        }
    _B_in = _B_setup()

    return {
        "A_x": _A_in["x"],
        "A_k_cache": _A_in["k_cache"],
        "A_v_cache": _A_in["v_cache"],
        "A_wq": _A_in["wq"],
        "A_wo": _A_in["wo"],
        "B_x": _B_in["x"],
        "B_c_kv": _B_in["c_kv"],
        "B_w_q": _B_in["w_q"],
        "B_w_uk": _B_in["w_uk"],
        "B_w_uv": _B_in["w_uv"],
        "B_w_o": _B_in["w_o"],
    }

def reference(A_x, A_k_cache, A_v_cache, A_wq, A_wo, B_x, B_c_kv, B_w_q, B_w_uk, B_w_uv, B_w_o):
    def _A_ref(x, k_cache, v_cache, wq, wo):
        B, Sq, D = x.shape
        Hq, Dh = 32, 128
        Hkv, Sk = k_cache.shape[1], k_cache.shape[2]
        W = min(4096, max(256, Sk // 4))
        rep = Hq // Hkv
        q = (lambda x, *d: x.permute(*d))((x @ wq).reshape(B, Sq, Hq, Dh), 0, 2, 1, 3)
        k = (lambda x, n, axis: torch.repeat_interleave(x, n, dim=axis))(k_cache[:, :, Sk - W:, :], rep, axis=1)
        v = (lambda x, n, axis: torch.repeat_interleave(x, n, dim=axis))(v_cache[:, :, Sk - W:, :], rep, axis=1)
        scores = torch.einsum("bhqd,bhkd->bhqk", q, k) * (Dh ** -0.5)
        p = _softmax(scores, axis=-1)
        o = torch.einsum("bhqk,bhkd->bhqd", p, v)
        return (lambda x, *d: x.permute(*d))(o, 0, 2, 1, 3).reshape(B, Sq, Hq * Dh) @ wo

    def _B_ref(x, c_kv, w_q, w_uk, w_uv, w_o):
        B, Sq, M = x.shape[0], x.shape[1], x.shape[2]
        Sk = c_kv.shape[1]
        H, D = w_q.shape[1], w_q.shape[2]
        q = torch.einsum("bqm,mhd->bhqd", x, w_q)
        k = torch.einsum("bkc,chd->bhkd", c_kv, w_uk)
        v = torch.einsum("bkc,chd->bhkd", c_kv, w_uv)
        scores = torch.einsum("bhqd,bhkd->bhqk", q, k) * (D ** -0.5)
        off = Sk - Sq
        qi = torch.arange(Sq)[:, None]
        kj = torch.arange(Sk)[None, :]
        cmask = kj <= (qi + off)
        scores = torch.where(cmask[None, None], scores, -1e9)
        p = _softmax(scores, axis=-1)
        ctx = torch.einsum("bhqk,bhkd->bhqd", p, v)
        return torch.einsum("bhqd,hdm->bqm", ctx, w_o)

    a = _A_ref(A_x, A_k_cache, A_v_cache, A_wq, A_wo)
    b = _B_ref(B_x, B_c_kv, B_w_q, B_w_uk, B_w_uv, B_w_o)
    return torch.concatenate([a, b], axis=-1)
```

```cpp
#include <hip/hip_runtime.h>
#include <hip/hip_bf16.h>
#include <stdint.h>

typedef unsigned short u16;
__device__ __forceinline__ float bf2f(u16 v){ return __uint_as_float(((unsigned)v) << 16); }

#define WS_QA   0
#define WS_QABS 8192
#define WS_PA   (8192+32768)
#define WS_PB   (WS_PA + 2*32*8*132)
#define WS_O    (WS_PB + 2*64*32*516)

__global__ void k1(const u16* xA, const u16* wqA, const u16* xB, const u16* wqB, const u16* wuk, float* ws) {
  __shared__ float q[2][128];
  int blk = blockIdx.x, t = threadIdx.x;
  int h = blk & 31; bool isB = blk >= 32;
  const u16* x = isB ? xB : xA; const u16* w = isB ? wqB : wqA;
  int b = t >> 7, d = t & 127;
  float acc = 0.f;
  for (int m = 0; m < 576; ++m) acc += bf2f(x[b*576+m]) * bf2f(w[(size_t)m*4096 + h*128 + d]);
  if (!isB) { ws[WS_QA + (b*32+h)*128 + d] = acc; return; }
  q[b][d] = acc; __syncthreads();
  for (int c = t; c < 512; c += 256) {
    float a0 = 0.f, a1 = 0.f;
    for (int dd = 0; dd < 128; ++dd) { float wv = bf2f(wuk[(size_t)c*4096 + h*128 + dd]); a0 += q[0][dd]*wv; a1 += q[1][dd]*wv; }
    ws[WS_QABS + (0*32+h)*512 + c] = a0; ws[WS_QABS + (1*32+h)*512 + c] = a1;
  }
}

__global__ void k2a(const u16* kc, const u16* vc, float* ws) {
  __shared__ float q[128]; __shared__ float p[256]; __shared__ float red[256];
  int s = blockIdx.x & 7, h = (blockIdx.x >> 3) & 31, b = blockIdx.x >> 8, t = threadIdx.x;
  int g = h >> 1;
  if (t < 128) q[t] = ws[WS_QA + (b*32+h)*128 + t];
  __syncthreads();
  size_t base = ((size_t)(b*16+g)*8192 + 6144 + s*256) * 128;
  float sc = 0.f;
  for (int d = 0; d < 128; ++d) sc += q[d] * bf2f(kc[base + (size_t)t*128 + d]);
  sc *= 0.08838834764831845f;
  red[t] = sc; __syncthreads();
  for (int o = 128; o > 0; o >>= 1) { if (t < o) red[t] = fmaxf(red[t], red[t+o]); __syncthreads(); }
  float m = red[0]; __syncthreads();
  float e = __expf(sc - m); p[t] = e; red[t] = e; __syncthreads();
  for (int o = 128; o > 0; o >>= 1) { if (t < o) red[t] += red[t+o]; __syncthreads(); }
  float l = red[0];
  float* out = ws + WS_PA + ((b*32+h)*8 + s)*132;
  if (t < 128) { float a = 0.f; for (int k = 0; k < 256; ++k) a += p[k] * bf2f(vc[base + (size_t)k*128 + t]); out[4+t] = a; }
  if (t == 0) { out[0] = m; out[1] = l; }
}

__global__ void k2b(const u16* ckv, float* ws) {
  __shared__ float p[32][128]; __shared__ float ml[32][2];
  int s = blockIdx.x & 63, b = blockIdx.x >> 6, t = threadIdx.x;
  const u16* c = ckv + ((size_t)b*8192 + s*128)*512;
  for (int i = t; i < 32*128; i += 256) {
    int h = i >> 7, k = i & 127; float a = 0.f;
    const float* qa = ws + WS_QABS + (b*32+h)*512;
    for (int cc = 0; cc < 512; ++cc) a += qa[cc] * bf2f(c[(size_t)k*512 + cc]);
    p[h][k] = a * 0.08838834764831845f;
  }
  __syncthreads();
  if (t < 32) { float m = -INFINITY; for (int k = 0; k < 128; ++k) m = fmaxf(m, p[t][k]);
    float l = 0.f; for (int k = 0; k < 128; ++k) { float e = __expf(p[t][k]-m); p[t][k] = e; l += e; } ml[t][0] = m; ml[t][1] = l; }
  __syncthreads();
  float* out = ws + WS_PB + (size_t)(b*64+s)*32*516;
  for (int i = t; i < 32*512; i += 256) {
    int h = i >> 9, cc = i & 511; float a = 0.f;
    for (int k = 0; k < 128; ++k) a += p[h][k] * bf2f(c[(size_t)k*512 + cc]);
    out[h*516 + 4 + cc] = a;
  }
  if (t < 32) { out[t*516] = ml[t][0]; out[t*516+1] = ml[t][1]; }
}

__global__ void k3(const u16* wuv, float* ws) {
  __shared__ float lat[512];
  int h = blockIdx.x & 31, b = (blockIdx.x >> 5) & 1, isB = blockIdx.x >> 6, t = threadIdx.x;
  if (!isB) {
    const float* pa = ws + WS_PA + (b*32+h)*8*132;
    float M = -INFINITY; for (int s = 0; s < 8; ++s) M = fmaxf(M, pa[s*132]);
    float L = 0.f, o = 0.f; for (int s = 0; s < 8; ++s) { float w = __expf(pa[s*132]-M); L += w*pa[s*132+1]; if (t < 128) o += w*pa[s*132+4+t]; }
    if (t < 128) ws[WS_O + b*4096 + h*128 + t] = o / L;
  } else {
    const float* pb = ws + WS_PB + (size_t)b*64*32*516 + h*516;
    float M = -INFINITY; for (int s = 0; s < 64; ++s) M = fmaxf(M, pb[(size_t)s*32*516]);
    float L = 0.f, o0 = 0.f, o1 = 0.f;
    for (int s = 0; s < 64; ++s) { const float* r = pb + (size_t)s*32*516; float w = __expf(r[0]-M); L += w*r[1]; o0 += w*r[4+t]; o1 += w*r[4+256+t]; }
    lat[t] = o0/L; lat[t+256] = o1/L; __syncthreads();
    if (t < 128) { float a = 0.f; for (int c = 0; c < 512; ++c) a += lat[c]*bf2f(wuv[(size_t)c*4096 + h*128 + t]); ws[WS_O + 8192 + b*4096 + h*128 + t] = a; }
  }
}

__global__ void k4(const u16* woA, const u16* woB, const float* ws, __hip_bfloat16* out) {
  __shared__ float red[2][4][64];
  int cg = blockIdx.x % 9, isB = blockIdx.x / 9, t = threadIdx.x;
  const u16* w = isB ? woB : woA; const float* o = ws + WS_O + isB*8192;
  int col = cg*64 + (t & 63), kg = t >> 6;
  float a0 = 0.f, a1 = 0.f;
  for (int k = kg*1024; k < kg*1024+1024; ++k) { float wv = bf2f(w[(size_t)k*576 + col]); a0 += o[k]*wv; a1 += o[4096+k]*wv; }
  red[0][kg][t&63] = a0; red[1][kg][t&63] = a1; __syncthreads();
  if (t < 128) { int b = t >> 6, c = t & 63; float v = red[b][0][c]+red[b][1][c]+red[b][2][c]+red[b][3][c];
    out[b*1152 + isB*576 + cg*64 + c] = __float2bfloat16(v); }
}

extern "C" void kernel_launch(void* const* d_in, const int* in_sizes, int n_in,
                              void* d_out, int out_size, void* d_ws, size_t ws_size,
                              hipStream_t stream) {
  const u16* xA = (const u16*)d_in[0]; const u16* kc = (const u16*)d_in[1]; const u16* vc = (const u16*)d_in[2];
  const u16* wqA = (const u16*)d_in[3]; const u16* woA = (const u16*)d_in[4];
  const u16* xB = (const u16*)d_in[5]; const u16* ckv = (const u16*)d_in[6]; const u16* wqB = (const u16*)d_in[7];
  const u16* wuk = (const u16*)d_in[8]; const u16* wuv = (const u16*)d_in[9]; const u16* woB = (const u16*)d_in[10];
  float* ws = (float*)d_ws;
  k1<<<64, 256, 0, stream>>>(xA, wqA, xB, wqB, wuk, ws);
  k2a<<<512, 256, 0, stream>>>(kc, vc, ws);
  k2b<<<128, 256, 0, stream>>>(ckv, ws);
  k3<<<128, 256, 0, stream>>>(wuv, ws);
  k4<<<18, 256, 0, stream>>>(woA, woB, ws, (__hip_bfloat16*)d_out);
}
```

```cpp
#define NO_CODE_PF 1
#include <hip/hip_runtime.h>
#include <hip/hip_bf16.h>
#include <stdint.h>

typedef unsigned u32;
typedef unsigned long long u64;
typedef u32 v4u __attribute__((ext_vector_type(4)));
typedef u32 v2u __attribute__((ext_vector_type(2)));
typedef float f2 __attribute__((ext_vector_type(2)));
typedef float f4 __attribute__((ext_vector_type(4)));
typedef float f16v __attribute__((ext_vector_type(16)));
typedef __bf16 bf8 __attribute__((ext_vector_type(8)));
typedef __bf16 bf2 __attribute__((ext_vector_type(2)));
typedef short s4v __attribute__((ext_vector_type(4)));
typedef __amdgpu_buffer_rsrc_t rsrc_t;
#define LAS __attribute__((address_space(3)))
#define DI __device__ __forceinline__

constexpr int AUX_NT = 2, AUX_SC1 = 16;
constexpr int W_EPOCH = 0;
constexpr int W_H1B = 4096;
constexpr int W_H1A = W_H1B + 262144;
constexpr int W_H2 = W_H1A + 262144;
constexpr int W_H3S = W_H2 + 131072;
constexpr int W_HA3S = W_H3S + 131072;
constexpr int W_HA3D = W_HA3S + 32768;
constexpr int W_H4 = W_HA3D + 262144;
constexpr int W_H5 = W_H4 + 262144;
constexpr int W_H3D = W_H5 + 2 * 80 * 4096;
constexpr int W_END = W_H3D + 4194304;
constexpr int W_STAMP = 16 << 20;
constexpr float SC = 0.08838834764831845f * 1.4426950408889634f;
#define D_H1 12
#define WA 9
#define D_Y0 16
#define WY 3
#define NWY 8
#define D_A3 14
#define D_H3 12
#define D_H4 14
#define D_H5 14
#define GKB 3
#define PF_AUX 0
#define WS_AT_END 0
#define HOLD 1
#define CODE_BYTES 17408
constexpr int SPIN_CAP = 1 << 17;

struct Params {
  const void *xA, *kc, *vc, *wqA, *woA, *xB, *ckv, *wqB, *wuk, *wuv, *woB;
  void *out, *ws;
};

DI rsrc_t mk(const void* p, u32 bytes) { return __builtin_amdgcn_make_buffer_rsrc((void*)p, 0, bytes, 0x00020000); }
template <int AUX> DI v4u ld4(rsrc_t r, int voff, int soff) { return __builtin_amdgcn_raw_buffer_load_b128(r, voff, soff, AUX); }
template <int AUX> DI v2u ld2(rsrc_t r, int voff, int soff) { return __builtin_amdgcn_raw_buffer_load_b64(r, voff, soff, AUX); }
DI void st4(v4u v, rsrc_t r, int voff, int soff) { __builtin_amdgcn_raw_buffer_store_b128(v, r, voff + soff, 0, AUX_SC1); }
DI u32 fu(float f) { return __builtin_bit_cast(u32, f); }
DI float uf(u32 u) { return __builtin_bit_cast(float, u); }
DI u32 pk(float lo, float hi) { f2 f = {lo, hi}; return __builtin_bit_cast(u32, __builtin_convertvector(f, bf2)); }
DI float blo(u32 u) { return uf(u << 16); }
DI float bhi(u32 u) { return uf(u & 0xffff0000u); }
template <int C> DI u32 dppu(u32 x) { return __builtin_amdgcn_mov_dpp(x, C, 0xf, 0xf, true); }
template <int C> DI float dppf(float x) { return uf(dppu<C>(fu(x))); }
constexpr int XOR1 = 0xB1, XOR2 = 0x4E, ROR4 = 0x124, ROR8 = 0x128, BCAST = 0x150;
DI f4 mfma16(v4u a, v4u b, f4 c) { return __builtin_amdgcn_mfma_f32_16x16x32_bf16(__builtin_bit_cast(bf8, a), __builtin_bit_cast(bf8, b), c, 0, 0, 0); }
DI f16v mfma32(v4u a, v4u b, f16v c) { return __builtin_amdgcn_mfma_f32_32x32x16_bf16(__builtin_bit_cast(bf8, a), __builtin_bit_cast(bf8, b), c, 0, 0, 0); }
DI float ex2(float x) { return __builtin_amdgcn_exp2f(x); }
DI float swap32_add(float a, float b) { auto s = __builtin_amdgcn_permlane32_swap(fu(a), fu(b), false, false); return uf(s[0]) + uf(s[1]); }
DI float swap16_add(float a, float b) { auto s = __builtin_amdgcn_permlane16_swap(fu(a), fu(b), false, false); return uf(s[0]) + uf(s[1]); }
DI float swap32_max(float a) { auto s = __builtin_amdgcn_permlane32_swap(fu(a), fu(a), false, false); return fmaxf(uf(s[0]), uf(s[1])); }
DI float swap16_max(float a) { auto s = __builtin_amdgcn_permlane16_swap(fu(a), fu(a), false, false); return fmaxf(uf(s[0]), uf(s[1])); }
DI void bar() { asm volatile("s_waitcnt lgkmcnt(0)\n\ts_barrier" ::: "memory"); }
DI void pin() { asm volatile("" ::: "memory"); __builtin_amdgcn_sched_barrier(0); }
DI u32 bad4(u32 acc, v4u v, u32 tag) { return (acc | (v.y ^ tag)) | (v.w ^ tag); }
DI bool none(u32 bad) { return __builtin_amdgcn_ballot_w64(bad != 0) == 0; }
DI v4u diag(u32 src, const u32 (&sel)[4]) {
  v4u a;
#pragma unroll
  for (int k = 0; k < 4; ++k) a[k] = __builtin_amdgcn_perm(src, src, sel[k]);
  return a;
}

#define ST(i) do {} while (0)

DI void spin_flag(const char* p, u32 want = 1) {
  _Pragma("nounroll") for (int spin = 0; spin < SPIN_CAP * 8; ++spin) {
    if ((u32)__builtin_amdgcn_readfirstlane(*(volatile const LAS u32*)p) >= want) break;
    __builtin_amdgcn_s_sleep(1);
  }
  pin();
}
DI u32 arrive(char* p, int lane) {
  u32 old = 0;
  pin();
  if (lane == 0) old = __hip_atomic_fetch_add((LAS u32*)p, 1u, __ATOMIC_RELAXED, __HIP_MEMORY_SCOPE_WORKGROUP);
  old = __builtin_amdgcn_readfirstlane(old);
  pin();
  return old;
}
DI void spin_flag_asm(u32 addr, u32 want) {
  _Pragma("nounroll") for (int spin = 0; spin < SPIN_CAP * 8; ++spin) {
    u32 v;
    asm volatile("ds_read_b32 %0, %1\n\ts_waitcnt lgkmcnt(0)" : "=v"(v) : "v"(addr) : "memory");
    if ((u32)__builtin_amdgcn_readfirstlane(v) >= want) break;
    __builtin_amdgcn_s_sleep(1);
  }
  pin();
}
DI float quad_sum(float v) { v += dppf<XOR1>(v); return v + dppf<XOR2>(v); }
DI f2 sum8(const char* p) {
  f2 s = *(const f2*)p;
#pragma unroll
  for (int w = 1; w < 8; ++w) s += *(const f2*)(p + w * 1024);
  return s;
}
DI f4 rows16(u32 c01, u32 c23, const v4u (&w)[4], const u32 (&sel0)[4], const u32 (&sel1)[4]) {
  f4 acc = {0.f, 0.f, 0.f, 0.f};
  acc = mfma16(diag(c01, sel0), w[0], acc); acc = mfma16(diag(c01, sel1), w[1], acc);
  acc = mfma16(diag(c23, sel0), w[2], acc); acc = mfma16(diag(c23, sel1), w[3], acc);
  return acc;
}

__global__ void __launch_bounds__(512, 2) fused_decode(Params P) {
  __shared__ __attribute__((aligned(16))) char smem[131072 + 32];
  ST(0);
  const int tid = threadIdx.x, lane = tid & 63, wave = __builtin_amdgcn_readfirstlane(tid >> 6);
  const int bid = blockIdx.x, x = bid & 7, jj = bid >> 3, j = jj & 15;
  const int g = lane >> 4, c16 = lane & 15, bsel = c16 >> 3;
  const int h = 4 * x + (j >> 2), r = j & 3, id = x * 16 + j;
  u32 pfc = 0;
  u32 sel0[4], sel1[4];
#pragma unroll
  for (int k = 0; k < 4; ++k) {
    const bool on = ((c16 & 7) >> 1) == k;
    sel0[k] = on ? ((c16 & 1) ? 0x01000c0cu : 0x0c0c0100u) : 0x0c0c0c0cu;
    sel1[k] = on ? ((c16 & 1) ? 0x03020c0cu : 0x0c0c0302u) : 0x0c0c0c0cu;
  }
  const int hsh = (bid * 2654435761u) >> 20;
  constexpr int L_FLAG = 131072;
  if (tid < 8) *(u32*)(smem + L_FLAG + tid * 4) = 0u;
  asm volatile("" : "+v"(sel0[0]), "+v"(sel0[1]), "+v"(sel0[2]), "+v"(sel0[3]), "+v"(sel1[0]), "+v"(sel1[1]), "+v"(sel1[2]), "+v"(sel1[3]));
  asm volatile("" ::"s"(P.xA), "s"(P.kc), "s"(P.vc), "s"(P.wqA), "s"(P.woA), "s"(P.xB), "s"(P.ckv), "s"(P.wqB), "s"(P.wuk), "s"(P.wuv), "s"(P.woB), "s"(P.out), "s"(P.ws));
  const rsrc_t ws = mk(P.ws, W_END);
  const bool isX = jj < 16;
  const int rec_voff = 64 * ((lane >> 1) & 15) + 32 * (lane >> 5) + 16 * (lane & 1);
  constexpr int L_RED = 65536, L_REDE = L_RED + 8192;
  u32 tag;
  v4u wo[4], we[4];
  auto load_wo = [&]() {
    const rsrc_t rw = mk((const char*)(isX ? P.woA : P.woB) + (h * 128 + wave * 16) * 1152, 16 * 1152);
    const int voff = g * 4608 + c16 * 16;
#pragma unroll
    for (int i = 0; i < 4; ++i) { wo[i] = ld4<AUX_NT>(rw, voff + i * 1152, r * 256); pin(); }
    if (r == 3) {
      const int ve = bsel ? 0x40000000 : voff;
#pragma unroll
      for (int i = 0; i < 4; ++i) { we[i] = ld4<AUX_NT>(rw, ve + i * 1152, 1024); pin(); }
    }
  };

  if (__builtin_expect(isX, 1)) {
    constexpr int L_RO = 0, L_Q = 8192, L_ML = 9216, L_R1 = 16384;
    const int gk = 2 * x + (j >> 3), b = (j >> 2) & 1;
    const int kq_voff = (c16 & 1) * 64 + g * 16;
    v4u kk[16], vv[16];
    const int gq_voff = (lane >> 5) * 2048 + (wave & 1) * 1024 + (lane & 31) * 16;
    auto put_q = [&](v4u a, v4u c, int f) {
      const float s0 = uf(a.x) + uf(c.x), s1 = uf(a.z) + uf(c.z);
      const u32 pq = pk(swap32_add(s0, s0), swap32_add(s1, s1));
      if (lane < 32) *(u32*)(smem + L_Q + f * 512 + (wave >> 1) * 256 + 16 * c16 + 8 * g + 4 * (wave & 1)) = pq;
      *(u32*)(smem + L_FLAG + 8) = tag;
      arrive(smem + L_FLAG + 4 * f, lane);
    };
    if (wave < 4) {
      u32 ep = __builtin_amdgcn_raw_buffer_load_b32(ws, 0, W_EPOCH, AUX_SC1);
      const int xv = bsel * 1152 + r * 288 + wave * 72;
      const int wv8 = g * 65536 + c16 * 16, wv9 = (32 + g) * 8192 + c16 * 16;
      const size_t wbase = (size_t)(r * 144 + wave * 36) * 8192 + h * 256;
      __builtin_amdgcn_s_setprio(2);
      _Pragma("nounroll") for (int ps = 0; ps < 2; ++ps) {
        const rsrc_t rx = mk(ps ? P.xA : P.xB, 2304), rw = mk((const char*)(ps ? P.wqA : P.wqB) + wbase, 35 * 8192 + 256);
        const v4u xc = ld4<0>(rx, xv + g * 16, 0);
        const u32 xe = __builtin_amdgcn_raw_buffer_load_b16(rx, xv + 64 + g * 2, 0, 0);
        pin();
#pragma unroll
        for (int i = 0; i < 8; ++i) {
          kk[i] = ld4<AUX_NT>(rw, wv8, i * 8192);
          pin();
          if (i >= WA - 1 && ps) asm volatile("s_waitcnt vmcnt(%0)" ::"i"(WA - 1) : "memory");
        }
        kk[8] = ld4<AUX_NT>(rw, wv9, 0);
        if (ps == 0) bar();
        pin();
        asm volatile("" ::"v"(pfc));
        tag = (u32)__builtin_amdgcn_readfirstlane(ep) + 0x9E3779B9u;
        f4 acc = {0.f, 0.f, 0.f, 0.f};
#pragma unroll
        for (int i = 0; i < 4; ++i) { acc = mfma16(diag(xc[i], sel0), kk[2 * i], acc); acc = mfma16(diag(xc[i], sel1), kk[2 * i + 1], acc); }
        acc = mfma16(diag(xe, sel0), kk[8], acc);
        char* rp = smem + L_R1 + ps * 4096 + lane * 16;
        *(f4*)(rp + wave * 1024) = acc;
        if (arrive(smem + L_FLAG + 16 + 4 * ps, lane) == 3) {
          acc = (*(const f4*)rp + *(const f4*)(rp + 1024)) + (*(const f4*)(rp + 2048) + *(const f4*)(rp + 3072));
          const int hb = W_H1B + ps * 262144;
          st4(v4u{fu(acc[0]), tag, fu(acc[1]), tag}, ws, lane * 16, hb + id * 2048);
          st4(v4u{fu(acc[2]), tag, fu(acc[3]), tag}, ws, lane * 16 + 1024, hb + id * 2048);
          pin();
          *(volatile LAS u32*)(smem + L_FLAG + 24) = ps + 1;
          if (ps == 0) ST(1);
        }
        spin_flag(smem + L_FLAG + 24, ps + 1);
        if (ps == 0) {
          __builtin_amdgcn_s_sleep(D_H1);
          v4u a, c;
          _Pragma("nounroll") for (int spin = 0; spin < SPIN_CAP; ++spin) {
            pin();
            a = ld4<AUX_SC1>(ws, gq_voff, W_H1B + h * 8192 + (wave >> 1) * 512);
            c = ld4<AUX_SC1>(ws, gq_voff, W_H1B + h * 8192 + (wave >> 1) * 512 + 4096);
            if (none(bad4(bad4(0u, a, tag), c, tag))) break;
            __builtin_amdgcn_s_sleep(2);
          }
          put_q(a, c, 0);
          ST(2);
          spin_flag(smem + L_FLAG + 12, 4);
        }
      }
    } else {
      bar();
      __builtin_amdgcn_s_setprio(3);
      const rsrc_t rw = mk((const char*)P.wuk + (r * 128 + (wave - 4) * 32) * 8192 + h * 256, 31 * 8192 + 256);
      const int voff = (4 * (c16 >> 2) + ((c16 >> 1) & 1)) * 8192 + kq_voff;
#pragma unroll
      for (int i = 0; i < 8; ++i) { kk[i] = ld4<AUX_NT>(rw, voff + (i & 1) * 128, ((i >> 1) & 1) * 16384 + (i >> 2) * 131072); pin(); }
      spin_flag(smem + L_FLAG, 4);
      tag = __builtin_amdgcn_readfirstlane(*(const u32*)(smem + L_FLAG + 8));
      const v4u q0 = *(const v4u*)(smem + L_Q + bsel * 256 + kq_voff), q1 = *(const v4u*)(smem + L_Q + bsel * 256 + 128 + kq_voff);
      u32 o[4];
#pragma unroll
      for (int be = 0; be < 4; ++be) {
        f4 d = {0.f, 0.f, 0.f, 0.f};
        d = mfma16(kk[2 * be], q0, d); d = mfma16(kk[2 * be + 1], q1, d);
        o[be] = pk(d[0] + dppf<XOR1>(d[1]), d[2] + dppf<XOR1>(d[3]));
      }
      const bool up = c16 & 2;
      if ((c16 & 5) == 0) st4(v4u{up ? o[2] : o[0], tag, up ? o[3] : o[1], tag}, ws, bsel * 65536 + (c16 & 2) * 32 + g * 16, W_H2 + h * 2048 + (r * 32 + 8 * (wave - 4)) * 16);
      arrive(smem + L_FLAG + 12, lane);
      ST(3);
      spin_flag(smem + L_FLAG + 24, 2);
    }
    __builtin_amdgcn_s_setprio(1);
    pin();
    {
      const size_t kvoff = ((size_t)(b * 16 + gk) * 8192 + 6144 + r * 512 + wave * 64) * 256;
      const rsrc_t rk = mk((const char*)P.kc + kvoff, 16384), rv = mk((const char*)P.vc + kvoff, 16384);
      const int k_voff = ((c16 & 2) * 2 + (c16 >> 2)) * 256 + kq_voff, v_voff = lane * 16;
      const int xo = (hsh & 3) * 4096;
#pragma unroll
      for (int i = 0; i < 16; ++i) { kk[i] = ld4<AUX_NT>(rk, k_voff + (i & 1) * 128 + ((i >> 1) & 1) * 2048, ((i >> 2) * 4096) ^ xo); pin(); }
      const int qv = wave < 4 ? gq_voff : 0x40000000, qs = W_H1A + (2 * gk + ((wave >> 1) & 1)) * 8192 + b * 512;
      v4u qa = ld4<AUX_SC1>(ws, qv, qs), qc = ld4<AUX_SC1>(ws, qv, qs + 4096);
      pin();
#pragma unroll
      for (int i = 0; i < 16; ++i) { vv[i] = ld4<AUX_NT>(rv, v_voff + (i & 3) * 1024, ((i >> 2) * 4096) ^ xo); pin(); }
      ST(4);
      if (wave < 4) {
        if (!none(bad4(bad4(0u, qa, tag), qc, tag))) {
          _Pragma("nounroll") for (int spin = 0; spin < SPIN_CAP; ++spin) {
            pin();
            qa = ld4<AUX_SC1>(ws, qv, qs); qc = ld4<AUX_SC1>(ws, qv, qs + 4096);
            if (none(bad4(bad4(0u, qa, tag), qc, tag))) break;
          }
        }
        put_q(qa, qc, 1);
      }
    }
    spin_flag(smem + L_FLAG + 4, 4);
    ST(5);
    float sc[16];
    {
      const v4u q0 = *(const v4u*)(smem + L_Q + 512 + bsel * 256 + kq_voff), q1 = *(const v4u*)(smem + L_Q + 512 + bsel * 256 + 128 + kq_voff);
      const bool odd = c16 & 1;
#pragma unroll
      for (int be = 0; be < 8; ++be) {
        f4 d = {0.f, 0.f, 0.f, 0.f};
        d = mfma16(kk[2 * be], q0, d); d = mfma16(kk[2 * be + 1], q1, d);
        const float u0 = odd ? d[1] : d[0], u1 = odd ? d[3] : d[2];
        sc[2 * be] = u0 + dppf<XOR1>(u0); sc[2 * be + 1] = u1 + dppf<XOR1>(u1);
      }
    }
    float m = sc[0];
#pragma unroll
    for (int i = 1; i < 16; ++i) m = fmaxf(m, sc[i]);
    m = swap32_max(swap16_max(m)) * SC;
    float l = 0.f;
#pragma unroll
    for (int be = 0; be < 8; ++be) {
      const float p0 = ex2(fmaf(sc[2 * be], SC, -m)), p1 = ex2(fmaf(sc[2 * be + 1], SC, -m));
      l += p0 + p1;
      const u32 pp = pk(p0, p1);
      kk[2 * be] = diag(pp, sel0); kk[2 * be + 1] = diag(pp, sel1);
    }
    l = swap16_add(l, l); l = swap32_add(l, l);
    if ((lane & 0x37) == 0) { *(float*)(smem + L_ML + bsel * 64 + wave * 4) = m; *(float*)(smem + L_ML + bsel * 64 + 32 + wave * 4) = l; }
    pin();
    f4 acc = {0.f, 0.f, 0.f, 0.f};
#pragma unroll
    for (int i = 0; i < 16; ++i) acc = mfma16(kk[i], vv[i], acc);
    *(f4*)(smem + L_RO + wave * 1024 + lane * 16) = acc;
    ST(6);
    __builtin_amdgcn_s_setprio(0);
    pin();
    load_wo();
    bar();
    ST(7);
    if (wave < 2) {
      const f4 m0 = *(const f4*)(smem + L_ML + wave * 64), m1 = *(const f4*)(smem + L_ML + wave * 64 + 16);
      const f4 l0 = *(const f4*)(smem + L_ML + wave * 64 + 32), l1 = *(const f4*)(smem + L_ML + wave * 64 + 48);
      const float M = fmaxf(fmaxf(fmaxf(m0[0], m0[1]), fmaxf(m0[2], m0[3])), fmaxf(fmaxf(m1[0], m1[1]), fmaxf(m1[2], m1[3])));
      f2 s = {0.f, 0.f};
      float L = 0.f;
#pragma unroll
      for (int w = 0; w < 8; ++w) {
        const float f = ex2((w < 4 ? m0[w & 3] : m1[w & 3]) - M);
        s += f * *(const f2*)(smem + L_RO + w * 1024 + tid * 8);
        L = fmaf(f, w < 4 ? l0[w & 3] : l1[w & 3], L);
      }
      const int rec = ((2 * gk + wave) * 2 + b) * 4 + r;
      st4(v4u{fu(s.x), tag, fu(s.y), tag}, ws, rec_voff, W_HA3D + rec * 1024);
      if (lane < 8) st4(v4u{fu(M), tag, fu(L), tag}, ws, lane * 16, W_HA3S + rec * 128);
      __builtin_amdgcn_s_sleep(D_A3 - 3);
    } else {
      __builtin_amdgcn_s_sleep(D_A3);
    }
  } else {
    constexpr int L_Q = 0, L_P = 32768, L_MX = 40960, L_LS = 41472, L_LAT = 45056, L_ST = 49152, L_R4 = 53248, L_CNT = L_FLAG + 16;
    const int b = id >> 6;
    const u32 ep = __builtin_amdgcn_raw_buffer_load_b32(ws, 0, W_EPOCH, AUX_SC1);
    bar();
    v4u fr[32], qg[8], wv[4];
    const int r32 = lane & 31, hh = lane >> 5;
    const int row_base = r32 * 1024 + 16 * (hh ^ (((r32 & 3) << 2) | ((r32 >> 2) & 3)));
    const int qso = W_H2 + b * 65536 + wave * 8192;
    auto load_q = [&]() {
#pragma unroll
      for (int i = 0; i < 8; ++i) { qg[i] = ld4<AUX_SC1>(ws, lane * 16 + (i & 3) * 1024, qso + (i >> 2) * 4096); pin(); }
    };
    if (wave < 4) {
      const rsrc_t rc = mk((const char*)P.ckv + (size_t)id * 131072 + wave * 8192, 131072 - wave * 8192);
      const int xi = hsh & 4, lx = lane ^ ((2 * wave + (xi >> 2)) & 3);
      if (D_Y0) __builtin_amdgcn_s_sleep(D_Y0);
#pragma unroll
      for (int i = 0; i < 32; ++i) {
        const int ro = (i >> 3) * 32768 + ((i & 7) ^ xi) * 1024;
        __builtin_amdgcn_raw_ptr_buffer_load_lds(rc, (LAS void*)(smem + wave * 8192 + ro), 16, 16 * (lx ^ (((i & 3) << 2) | ((i >> 2) & 1))), ro, 0, AUX_NT);
        pin();
        if (i >= WY - 1 && i < NWY) asm volatile("s_waitcnt vmcnt(%0)" ::"i"(WY - 1) : "memory");
        if (i == 15 || i == 23 || i == 31) {
          asm volatile("s_waitcnt vmcnt(8)" ::: "memory");
          if (lane == 0) asm volatile("ds_add_u32 %0, %1" ::"v"(L_FLAG + 4 * ((i >> 3) - 1)), "v"(1u) : "memory");
          pin();
        }
      }
      if (wave < 3) {
        spin_flag_asm(L_FLAG + 4 * wave, 4);
#pragma unroll
        for (int t = 0; t < 32; ++t)
          asm volatile("ds_read_b128 %0, %1 offset:%2" : "=v"(fr[t]) : "v"(wave * 32768 + (row_base ^ (32 * (t & 7)))), "i"(256 * (t >> 3)) : "memory");
      }
      pin();
      __builtin_amdgcn_s_waitcnt(0x0070);
      pin();
      if (lane == 0) asm volatile("ds_add_u32 %0, %1" ::"v"(L_FLAG + 12), "v"(1u) : "memory");
      ST(1);
      load_q();
      if (wave == 3) {
        spin_flag(smem + L_FLAG + 12, 4);
#pragma unroll
        for (int t = 0; t < 32; ++t) fr[t] = *(const v4u*)(smem + 3 * 32768 + ((row_base ^ (32 * (t & 7))) + 256 * (t >> 3)));
      }
    } else {
      const int q = c16 >> 2, p = lane & 3;
      const int base = (4 * hh + q) * 1024 + (wave - 4) * 256 + 16 * ((2 * (g & 1) + (p >> 1)) ^ (4 * q + hh)) + 8 * (p & 1);
#pragma unroll
      for (int kb = 0; kb < 4; ++kb) {
        spin_flag(smem + L_FLAG + 4 * kb, 4);
        if (kb == GKB) load_q();
#pragma unroll
        for (int ta = 0; ta < 4; ++ta)
#pragma unroll
          for (int s = 0; s < 2; ++s) {
            const char* pa = smem + kb * 32768 + s * 16384;
            const v2u a = __builtin_bit_cast(v2u, __builtin_amdgcn_ds_read_tr16_b64_v4i16((LAS s4v*)(pa + (base ^ (64 * ta)))));
            const v2u c = __builtin_bit_cast(v2u, __builtin_amdgcn_ds_read_tr16_b64_v4i16((LAS s4v*)(pa + (base ^ (64 * ta) ^ 32) + 8192)));
            fr[ta * 8 + 2 * kb + s] = v4u{a.x, a.y, c.x, c.y};
          }
        pin();
      }
    }
    asm volatile("" ::"v"(pfc));
    tag = __builtin_amdgcn_readfirstlane(ep) + 0x9E3779B9u;
    bar();
    ST(2);
    _Pragma("nounroll") for (int spin = 0; spin < SPIN_CAP; ++spin) {
      u32 bad = 0;
#pragma unroll
      for (int i = 0; i < 8; ++i) bad = bad4(bad, qg[i], tag);
      if (none(bad)) break;
      pin();
      __builtin_amdgcn_s_sleep(4);
      load_q();
    }
    pin();
    {
      const rsrc_t rw = mk((const char*)P.wuv + (r * 128 + wave * 16) * 8192 + h * 256, 15 * 8192 + 256);
#pragma unroll
      for (int i = 0; i < 4; ++i) { wv[i] = ld4<AUX_NT>(rw, g * 32768 + c16 * 16, i * 8192); pin(); }
    }
#pragma unroll
    for (int i = 0; i < 8; ++i) {
      const int fh = ((i >> 1) << 2) | (wave & 3);
      *(v2u*)(smem + L_Q + (4 * wave + (i >> 1)) * 1024 + (i & 1) * 512 + 16 * ((lane >> 1) ^ fh) + 8 * (lane & 1)) = v2u{qg[i].x, qg[i].z};
    }
    bar();
    ST(3);
    f16v s;
    if (wave < 4) {
      v4u qb[4];
#define QRD(t) (*(const v4u*)(smem + L_Q + ((row_base ^ (32 * ((t) & 7))) + 256 * ((t) >> 3))))
#pragma unroll
      for (int t = 0; t < 4; ++t) qb[t] = QRD(t);
#pragma unroll
      for (int i = 0; i < 16; ++i) s[i] = 0.f;
      pin();
#pragma unroll
      for (int t = 0; t < 32; ++t) {
        s = mfma32(fr[t], qb[t & 3], s);
        __builtin_amdgcn_sched_barrier(0);
        if (t + 4 < 32) qb[t & 3] = QRD(t + 4);
        __builtin_amdgcn_sched_barrier(0);
      }
      float m = s[0];
#pragma unroll
      for (int i = 1; i < 16; ++i) m = fmaxf(m, s[i]);
      m = swap32_max(m);
      if (lane < 32) *(float*)(smem + L_MX + lane * 16 + wave * 4) = m;
    }
    bar();
    if (wave < 4) {
      const f4 mm = *(const f4*)(smem + L_MX + r32 * 16);
      const float m = fmaxf(fmaxf(mm[0], mm[1]), fmaxf(mm[2], mm[3])) * SC;
      float l = 0.f;
      u32 pp[8];
#pragma unroll
      for (int i = 0; i < 8; ++i) {
        const float p0 = ex2(fmaf(s[2 * i], SC, -m)), p1 = ex2(fmaf(s[2 * i + 1], SC, -m));
        l += p0 + p1;
        pp[i] = pk(p0, p1);
      }
      *(v4u*)(smem + L_P + wave * 2048 + lane * 16) = v4u{pp[0], pp[1], pp[2], pp[3]};
      *(v4u*)(smem + L_P + wave * 2048 + 1024 + lane * 16) = v4u{pp[4], pp[5], pp[6], pp[7]};
      l = swap32_add(l, l);
      if (lane < 32) *(float*)(smem + L_LS + lane * 16 + wave * 4) = l;
    }
    bar();
    ST(4);
    if (wave >= 4) {
      v4u pf[8];
#pragma unroll
      for (int ks = 0; ks < 8; ++ks) pf[ks] = *(const v4u*)(smem + L_P + ks * 1024 + lane * 16);
      f16v o0, o1, o2, o3;
#pragma unroll
      for (int i = 0; i < 16; ++i) { o0[i] = 0.f; o1[i] = 0.f; o2[i] = 0.f; o3[i] = 0.f; }
      const int voff = hh * 524288 + r32 * 4, soff = W_H3D + (wave - 4) * 32768 + id * 256;
      pin();
#pragma unroll
      for (int ks = 0; ks < 8; ++ks) { o0 = mfma32(pf[ks], fr[ks], o0); o1 = mfma32(pf[ks], fr[8 + ks], o1); }
      __builtin_amdgcn_sched_barrier(0);
#define OST(a, c, i, pi) __builtin_amdgcn_raw_buffer_store_b32(pk(a[i], c[i]), ws, voff + (pi) * 128, soff + (((i) & 3) + 8 * ((i) >> 2)) * 131072, AUX_SC1)
#pragma unroll
      for (int ks = 0; ks < 8; ++ks) {
        o2 = mfma32(pf[ks], fr[16 + ks], o2);
        __builtin_amdgcn_sched_barrier(0);
        if (ks) OST(o0, o1, 2 * ks - 2, 0);
        __builtin_amdgcn_sched_barrier(0);
        o3 = mfma32(pf[ks], fr[24 + ks], o3);
        __builtin_amdgcn_sched_barrier(0);
        if (ks) OST(o0, o1, 2 * ks - 1, 0);
        __builtin_amdgcn_sched_barrier(0);
      }
      OST(o0, o1, 14, 0); OST(o0, o1, 15, 0);
#pragma unroll
      for (int i = 0; i < 16; ++i) OST(o2, o3, i, 1);
    }
    bar();
    ST(5);
    if (wave >= 4) {
      __builtin_amdgcn_s_setprio(3);
      asm volatile("s_waitcnt vmcnt(0)" ::: "memory");
      if (arrive(smem + L_CNT, lane) == 3) {
        const int hd = (lane >> 3) * 4 + (lane & 3);
        const f4 mm = *(const f4*)(smem + L_MX + hd * 16), ll = *(const f4*)(smem + L_LS + hd * 16);
        st4(v4u{fu(fmaxf(fmaxf(mm[0], mm[1]), fmaxf(mm[2], mm[3])) * SC), tag, fu((ll[0] + ll[1]) + (ll[2] + ll[3])), tag}, ws,
            (lane >> 3) * 16384 + (lane & 7) * 16, W_H3S + id * 128);
      }
      __builtin_amdgcn_s_setprio(0);
      ST(6);
    }
    pin();
    load_wo();
    if (wave < 4) __builtin_amdgcn_s_sleep(D_H3 + 14); else __builtin_amdgcn_s_sleep(D_H3);
    {
      v4u st;
      _Pragma("nounroll") for (int spin = 0; spin < SPIN_CAP; ++spin) {
        pin();
        st = ld4<AUX_SC1>(ws, (lane >> 2) * 128, W_H3S + x * 16384 + wave * 2048 + (h & 3) * 16);
        if (none(bad4(0u, st, tag))) break;
        __builtin_amdgcn_s_sleep(3);
      }
      pin();
      ST(7);
      v4u d[4];
#pragma unroll
      for (int i = 0; i < 4; ++i) d[i] = ld4<AUX_SC1>(ws, g * 1024 + c16 * 16 + i * 256, W_H3D + id * 32768 + wave * 4096);
      pin();
      float M = uf(st.x);
      M = fmaxf(M, dppf<ROR4>(M)); M = fmaxf(M, dppf<ROR8>(M)); M = swap32_max(swap16_max(M));
      const float wg = ex2(uf(st.x) - M);
      float L = wg * uf(st.z);
      L += dppf<ROR4>(L); L += dppf<ROR8>(L); L = swap16_add(L, L); L = swap32_add(L, L);
      if (lane == 0) *(f2*)(smem + L_ST + wave * 8) = f2{M, L};
      const float wi[4] = {dppf<BCAST>(wg), dppf<BCAST + 4>(wg), dppf<BCAST + 8>(wg), dppf<BCAST + 12>(wg)};
      float a[8];
#pragma unroll
      for (int k = 0; k < 4; ++k) {
        a[2 * k] = wi[0] * blo(d[0][k]); a[2 * k + 1] = wi[0] * bhi(d[0][k]);
#pragma unroll
        for (int i = 1; i < 4; ++i) { a[2 * k] = fmaf(wi[i], blo(d[i][k]), a[2 * k]); a[2 * k + 1] = fmaf(wi[i], bhi(d[i][k]), a[2 * k + 1]); }
      }
      const float lo = swap16_add(swap32_add(a[0], a[4]), swap32_add(a[2], a[6]));
      const float hi = swap16_add(swap32_add(a[1], a[5]), swap32_add(a[3], a[7]));
      float* lat = (float*)(smem + L_LAT + wave * 512) + 64 * bsel + 4 * (c16 & 7) + g;
      lat[0] = lo; lat[32] = hi;
    }
    bar();
    {
      const f4 s0 = *(const f4*)(smem + L_ST + bsel * 32), s1 = *(const f4*)(smem + L_ST + bsel * 32 + 16);
      const float M = fmaxf(fmaxf(s0[0], s0[2]), fmaxf(s1[0], s1[2]));
      const float f[4] = {ex2(s0[0] - M), ex2(s0[2] - M), ex2(s1[0] - M), ex2(s1[2] - M)};
      const float inv = __builtin_amdgcn_rcpf(fmaf(f[0], s0[1], fmaf(f[1], s0[3], fmaf(f[2], s1[1], f[3] * s1[3]))));
      f4 c = {0.f, 0.f, 0.f, 0.f};
#pragma unroll
      for (int w = 0; w < 4; ++w) c += f[w] * *(const f4*)(smem + L_LAT + (bsel * 4 + w) * 512 + wave * 64 + g * 16);
      c *= inv;
      *(f4*)(smem + L_R4 + wave * 1024 + lane * 16) = rows16(pk(c[0], c[1]), pk(c[2], c[3]), wv, sel0, sel1);
    }
    bar();
    if (wave < 2) {
      const f2 sv = sum8(smem + L_R4 + tid * 8);
      st4(v4u{fu(sv.x), tag, fu(sv.y), tag}, ws, rec_voff, W_H4 + ((h * 2 + wave) * 4 + r) * 1024);
      __builtin_amdgcn_s_sleep(D_H4 - 2);
    } else {
      __builtin_amdgcn_s_sleep(D_H4);
    }
  }
  ST(8);
  {
    v4u d0, d1, st = {0u, tag, 0x3e800000u, tag};
    const int dv = (bsel * 4 + (c16 & 3)) * 1024 + g * 32, dso = (isX ? W_HA3D : W_H4) + h * 8192 + wave * 128;
    _Pragma("nounroll") for (int spin = 0; spin < SPIN_CAP; ++spin) {
      pin();
      d0 = ld4<AUX_SC1>(ws, dv, dso);
      d1 = ld4<AUX_SC1>(ws, dv + 16, dso);
      if (isX) st = ld4<AUX_SC1>(ws, (bsel * 4 + (c16 & 3)) * 128, W_HA3S + h * 1024);
      if (none(bad4(bad4(bad4(0u, d0, tag), d1, tag), st, tag))) break;
      __builtin_amdgcn_s_sleep(2);
    }
    ST(9);
    float M = uf(st.x);
    M = fmaxf(M, dppf<XOR1>(M)); M = fmaxf(M, dppf<XOR2>(M));
    float w = ex2(uf(st.x) - M);
    w *= __builtin_amdgcn_rcpf(quad_sum(w * uf(st.z)));
    const u32 c01 = pk(quad_sum(w * uf(d0.x)), quad_sum(w * uf(d0.z))), c23 = pk(quad_sum(w * uf(d1.x)), quad_sum(w * uf(d1.z)));
    *(f4*)(smem + L_RED + wave * 1024 + lane * 16) = rows16(c01, c23, wo, sel0, sel1);
    if (r == 3) *(f4*)(smem + L_REDE + wave * 1024 + lane * 16) = rows16(c01, c23, we, sel0, sel1);
  }
  bar();
  const int part = isX ? 0 : 1;
  if (wave < 4) {
    if (wave < 2 || (r == 3 && (lane & 16) == 0)) {
      const f2 sv = sum8(smem + L_RED + (wave >> 1) * 8192 + (tid & 127) * 8);
      st4(v4u{fu(sv.x), tag, fu(sv.y), tag}, ws, (lane >> 3) * 4096 + (lane & 7) * 16,
          W_H5 + (part * 80 + (wave < 2 ? r * 16 : 64) + (wave & 1) * 8) * 4096 + h * 128);
    }
    ST(10);
  } else if (wave == 4 && id < 72) {
    const int e = id - 64, L = id < 64 ? id : 64 + (e & 1) + 4 * (e >> 1);
    v4u v[4];
    __builtin_amdgcn_s_sleep(D_H5);
    _Pragma("nounroll") for (int spin = 0; spin < SPIN_CAP; ++spin) {
      pin();
      u32 bad = 0;
#pragma unroll
      for (int i = 0; i < 4; ++i) v[i] = ld4<AUX_SC1>(ws, lane * 16 + i * 1024, W_H5 + (part * 80 + L) * 4096);
#pragma unroll
      for (int i = 0; i < 4; ++i) bad = bad4(bad, v[i], tag);
      if (none(bad)) break;
      __builtin_amdgcn_s_sleep(2);
    }
    float a = (uf(v[0].x) + uf(v[1].x)) + (uf(v[2].x) + uf(v[3].x)), c = (uf(v[0].z) + uf(v[1].z)) + (uf(v[2].z) + uf(v[3].z));
    a += dppf<ROR8>(a); c += dppf<ROR8>(c);
    a = swap16_add(a, a); c = swap16_add(c, c);
    a = swap32_add(a, a); c = swap32_add(c, c);
    if (lane < 8) {
      const int al = L & 15;
      const int n = (al >> 3) * 1152 + part * 576 + 128 * (L >> 4) + 32 * (al & 3) + 8 * (lane >> 1) + 4 * ((al >> 2) & 1) + 2 * (lane & 1);
      ((u32*)P.out)[n >> 1] = pk(a, c);
    }
    if (!isX && id == 0 && lane == 0) __builtin_amdgcn_raw_buffer_store_b32(tag, ws, 0, W_EPOCH, AUX_SC1);
  }
}

extern "C" void kernel_launch(void* const* d_in, const int* in_sizes, int n_in, void* d_out, int out_size, void* d_ws, size_t ws_size,
                              hipStream_t stream) {
  Params p;
  p.xA = d_in[0]; p.kc = d_in[1]; p.vc = d_in[2]; p.wqA = d_in[3]; p.woA = d_in[4];
  p.xB = d_in[5]; p.ckv = d_in[6]; p.wqB = d_in[7]; p.wuk = d_in[8]; p.wuv = d_in[9]; p.woB = d_in[10];
  p.out = d_out; p.ws = (char*)d_ws + (WS_AT_END ? ((ws_size - (18 << 20)) & ~(size_t)0x1fffff) : 0);
  fused_decode<<<256, 512, 0, stream>>>(p);
}
```

```cpp
#define STAMP 1
#define CODE_BYTES 17920
#define REP_IDX 0
#define REP_ROLE 2
#define REP_STAT 0
#include <hip/hip_runtime.h>
#include <hip/hip_bf16.h>
#include <stdint.h>

typedef unsigned u32;
typedef unsigned long long u64;
typedef u32 v4u __attribute__((ext_vector_type(4)));
typedef u32 v2u __attribute__((ext_vector_type(2)));
typedef float f2 __attribute__((ext_vector_type(2)));
typedef float f4 __attribute__((ext_vector_type(4)));
typedef float f16v __attribute__((ext_vector_type(16)));
typedef __bf16 bf8 __attribute__((ext_vector_type(8)));
typedef __bf16 bf2 __attribute__((ext_vector_type(2)));
typedef short s4v __attribute__((ext_vector_type(4)));
typedef __amdgpu_buffer_rsrc_t rsrc_t;
#define LAS __attribute__((address_space(3)))
#define DI __device__ __forceinline__

constexpr int AUX_NT = 2, AUX_SC1 = 16;
constexpr int W_EPOCH = 0;
constexpr int W_H1B = 4096;
constexpr int W_H1A = W_H1B + 262144;
constexpr int W_H2 = W_H1A + 262144;
constexpr int W_H3S = W_H2 + 131072;
constexpr int W_HA3S = W_H3S + 131072;
constexpr int W_HA3D = W_HA3S + 32768;
constexpr int W_H4 = W_HA3D + 262144;
constexpr int W_H5 = W_H4 + 262144;
constexpr int W_H3D = W_H5 + 2 * 80 * 4096;
constexpr int W_END = W_H3D + 4194304;
constexpr int W_STAMP = 16 << 20;
constexpr float SC = 0.08838834764831845f * 1.4426950408889634f;
#define D_H1 12
#define WA 9
#define D_Y0 16
#define WY 3
#define NWY 8
#define D_A3 14
#define D_H3 12
#define D_H4 14
#define D_H5 14
#define GKB 3
#define PF_AUX 0
#define WS_AT_END 0
#define HOLD 1
constexpr int SPIN_CAP = 1 << 17;

struct Params {
  const void *xA, *kc, *vc, *wqA, *woA, *xB, *ckv, *wqB, *wuk, *wuv, *woB;
  void *out, *ws;
};

DI rsrc_t mk(const void* p, u32 bytes) { return __builtin_amdgcn_make_buffer_rsrc((void*)p, 0, bytes, 0x00020000); }
template <int AUX> DI v4u ld4(rsrc_t r, int voff, int soff) { return __builtin_amdgcn_raw_buffer_load_b128(r, voff, soff, AUX); }
template <int AUX> DI v2u ld2(rsrc_t r, int voff, int soff) { return __builtin_amdgcn_raw_buffer_load_b64(r, voff, soff, AUX); }
DI void st4(v4u v, rsrc_t r, int voff, int soff) { __builtin_amdgcn_raw_buffer_store_b128(v, r, voff + soff, 0, AUX_SC1); }
DI u32 fu(float f) { return __builtin_bit_cast(u32, f); }
DI float uf(u32 u) { return __builtin_bit_cast(float, u); }
DI u32 pk(float lo, float hi) { f2 f = {lo, hi}; return __builtin_bit_cast(u32, __builtin_convertvector(f, bf2)); }
DI float blo(u32 u) { return uf(u << 16); }
DI float bhi(u32 u) { return uf(u & 0xffff0000u); }
template <int C> DI u32 dppu(u32 x) { return __builtin_amdgcn_mov_dpp(x, C, 0xf, 0xf, true); }
template <int C> DI float dppf(float x) { return uf(dppu<C>(fu(x))); }
constexpr int XOR1 = 0xB1, XOR2 = 0x4E, ROR4 = 0x124, ROR8 = 0x128, BCAST = 0x150;
DI f4 mfma16(v4u a, v4u b, f4 c) { return __builtin_amdgcn_mfma_f32_16x16x32_bf16(__builtin_bit_cast(bf8, a), __builtin_bit_cast(bf8, b), c, 0, 0, 0); }
DI f16v mfma32(v4u a, v4u b, f16v c) { return __builtin_amdgcn_mfma_f32_32x32x16_bf16(__builtin_bit_cast(bf8, a), __builtin_bit_cast(bf8, b), c, 0, 0, 0); }
DI float ex2(float x) { return __builtin_amdgcn_exp2f(x); }
DI float swap32_add(float a, float b) { auto s = __builtin_amdgcn_permlane32_swap(fu(a), fu(b), false, false); return uf(s[0]) + uf(s[1]); }
DI float swap16_add(float a, float b) { auto s = __builtin_amdgcn_permlane16_swap(fu(a), fu(b), false, false); return uf(s[0]) + uf(s[1]); }
DI float swap32_max(float a) { auto s = __builtin_amdgcn_permlane32_swap(fu(a), fu(a), false, false); return fmaxf(uf(s[0]), uf(s[1])); }
DI float swap16_max(float a) { auto s = __builtin_amdgcn_permlane16_swap(fu(a), fu(a), false, false); return fmaxf(uf(s[0]), uf(s[1])); }
DI void bar() { asm volatile("s_waitcnt lgkmcnt(0)\n\ts_barrier" ::: "memory"); }
DI void pin() { asm volatile("" ::: "memory"); __builtin_amdgcn_sched_barrier(0); }
DI u32 bad4(u32 acc, v4u v, u32 tag) { return (acc | (v.y ^ tag)) | (v.w ^ tag); }
DI bool none(u32 bad) { return __builtin_amdgcn_ballot_w64(bad != 0) == 0; }
DI v4u diag(u32 src, const u32 (&sel)[4]) {
  v4u a;
#pragma unroll
  for (int k = 0; k < 4; ++k) a[k] = __builtin_amdgcn_perm(src, src, sel[k]);
  return a;
}

#define SMASK 0xfff
#define ST(i) do { if ((SMASK >> (i)) & 1) asm volatile("s_memrealtime %0\n\ts_waitcnt lgkmcnt(0)" : "=s"(ts[i]) :: "memory"); } while (0)

DI void spin_flag(const char* p, u32 want = 1) {
  _Pragma("nounroll") for (int spin = 0; spin < SPIN_CAP * 8; ++spin) {
    if ((u32)__builtin_amdgcn_readfirstlane(*(volatile const LAS u32*)p) >= want) break;
    __builtin_amdgcn_s_sleep(1);
  }
  pin();
}
DI u32 arrive(char* p, int lane) {
  u32 old = 0;
  pin();
  if (lane == 0) old = __hip_atomic_fetch_add((LAS u32*)p, 1u, __ATOMIC_RELAXED, __HIP_MEMORY_SCOPE_WORKGROUP);
  old = __builtin_amdgcn_readfirstlane(old);
  pin();
  return old;
}
DI void spin_flag_asm(u32 addr, u32 want) {
  _Pragma("nounroll") for (int spin = 0; spin < SPIN_CAP * 8; ++spin) {
    u32 v;
    asm volatile("ds_read_b32 %0, %1\n\ts_waitcnt lgkmcnt(0)" : "=v"(v) : "v"(addr) : "memory");
    if ((u32)__builtin_amdgcn_readfirstlane(v) >= want) break;
    __builtin_amdgcn_s_sleep(1);
  }
  pin();
}
DI float quad_sum(float v) { v += dppf<XOR1>(v); return v + dppf<XOR2>(v); }
DI f2 sum8(const char* p) {
  f2 s = *(const f2*)p;
#pragma unroll
  for (int w = 1; w < 8; ++w) s += *(const f2*)(p + w * 1024);
  return s;
}
DI f4 rows16(u32 c01, u32 c23, const v4u (&w)[4], const u32 (&sel0)[4], const u32 (&sel1)[4]) {
  f4 acc = {0.f, 0.f, 0.f, 0.f};
  acc = mfma16(diag(c01, sel0), w[0], acc); acc = mfma16(diag(c01, sel1), w[1], acc);
  acc = mfma16(diag(c23, sel0), w[2], acc); acc = mfma16(diag(c23, sel1), w[3], acc);
  return acc;
}

__global__ void __launch_bounds__(512, 2) fused_decode(Params P) {
  __shared__ __attribute__((aligned(16))) char smem[131072 + 32];
  u64 ts[12];
#pragma unroll
  for (int i = 0; i < 12; ++i) ts[i] = 0;
  ST(0);
  const int tid = threadIdx.x, lane = tid & 63, wave = __builtin_amdgcn_readfirstlane(tid >> 6);
  const int bid = blockIdx.x, x = bid & 7, jj = bid >> 3, j = jj & 15;
  const int g = lane >> 4, c16 = lane & 15, bsel = c16 >> 3;
  const int h = 4 * x + (j >> 2), r = j & 3, id = x * 16 + j;
  u32 pfc = 0;
  if (tid < 16) {
    u64 pc;
    asm volatile("s_getpc_b64 %0" : "=s"(pc));
    pfc = __builtin_amdgcn_raw_buffer_load_b32(mk((const void*)(pc & ~255ull), CODE_BYTES), tid * 64, jj * 1024, PF_AUX);
  }
  u32 sel0[4], sel1[4];
#pragma unroll
  for (int k = 0; k < 4; ++k) {
    const bool on = ((c16 & 7) >> 1) == k;
    sel0[k] = on ? ((c16 & 1) ? 0x01000c0cu : 0x0c0c0100u) : 0x0c0c0c0cu;
    sel1[k] = on ? ((c16 & 1) ? 0x03020c0cu : 0x0c0c0302u) : 0x0c0c0c0cu;
  }
  const int hsh = (bid * 2654435761u) >> 20;
  constexpr int L_FLAG = 131072;
  if (tid < 8) *(u32*)(smem + L_FLAG + tid * 4) = 0u;
  asm volatile("" : "+v"(sel0[0]), "+v"(sel0[1]), "+v"(sel0[2]), "+v"(sel0[3]), "+v"(sel1[0]), "+v"(sel1[1]), "+v"(sel1[2]), "+v"(sel1[3]));
  asm volatile("" ::"s"(P.xA), "s"(P.kc), "s"(P.vc), "s"(P.wqA), "s"(P.woA), "s"(P.xB), "s"(P.ckv), "s"(P.wqB), "s"(P.wuk), "s"(P.wuv), "s"(P.woB), "s"(P.out), "s"(P.ws));
  const rsrc_t ws = mk(P.ws, W_END);
  const bool isX = jj < 16;
  const int rec_voff = 64 * ((lane >> 1) & 15) + 32 * (lane >> 5) + 16 * (lane & 1);
  constexpr int L_RED = 65536, L_REDE = L_RED + 8192;
  u32 tag;
  v4u wo[4], we[4];
  auto load_wo = [&]() {
    const rsrc_t rw = mk((const char*)(isX ? P.woA : P.woB) + (h * 128 + wave * 16) * 1152, 16 * 1152);
    const int voff = g * 4608 + c16 * 16;
#pragma unroll
    for (int i = 0; i < 4; ++i) { wo[i] = ld4<AUX_NT>(rw, voff + i * 1152, r * 256); pin(); }
    if (r == 3) {
      const int ve = bsel ? 0x40000000 : voff;
#pragma unroll
      for (int i = 0; i < 4; ++i) { we[i] = ld4<AUX_NT>(rw, ve + i * 1152, 1024); pin(); }
    }
  };

  if (__builtin_expect(isX, 1)) {
    constexpr int L_RO = 0, L_Q = 8192, L_ML = 9216, L_R1 = 16384;
    const int gk = 2 * x + (j >> 3), b = (j >> 2) & 1;
    const int kq_voff = (c16 & 1) * 64 + g * 16;
    v4u kk[16], vv[16];
    const int gq_voff = (lane >> 5) * 2048 + (wave & 1) * 1024 + (lane & 31) * 16;
    auto put_q = [&](v4u a, v4u c, int f) {
      const float s0 = uf(a.x) + uf(c.x), s1 = uf(a.z) + uf(c.z);
      const u32 pq = pk(swap32_add(s0, s0), swap32_add(s1, s1));
      if (lane < 32) *(u32*)(smem + L_Q + f * 512 + (wave >> 1) * 256 + 16 * c16 + 8 * g + 4 * (wave & 1)) = pq;
      *(u32*)(smem + L_FLAG + 8) = tag;
      arrive(smem + L_FLAG + 4 * f, lane);
    };
    if (wave < 4) {
      u32 ep = __builtin_amdgcn_raw_buffer_load_b32(ws, 0, W_EPOCH, AUX_SC1);
      const int xv = bsel * 1152 + r * 288 + wave * 72;
      const int wv8 = g * 65536 + c16 * 16, wv9 = (32 + g) * 8192 + c16 * 16;
      const size_t wbase = (size_t)(r * 144 + wave * 36) * 8192 + h * 256;
      __builtin_amdgcn_s_setprio(2);
      _Pragma("nounroll") for (int ps = 0; ps < 2; ++ps) {
        const rsrc_t rx = mk(ps ? P.xA : P.xB, 2304), rw = mk((const char*)(ps ? P.wqA : P.wqB) + wbase, 35 * 8192 + 256);
        const v4u xc = ld4<0>(rx, xv + g * 16, 0);
        const u32 xe = __builtin_amdgcn_raw_buffer_load_b16(rx, xv + 64 + g * 2, 0, 0);
        pin();
#pragma unroll
        for (int i = 0; i < 8; ++i) {
          kk[i] = ld4<AUX_NT>(rw, wv8, i * 8192);
          pin();
          if (i >= WA - 1 && ps) asm volatile("s_waitcnt vmcnt(%0)" ::"i"(WA - 1) : "memory");
        }
        kk[8] = ld4<AUX_NT>(rw, wv9, 0);
        if (ps == 0) bar();
        pin();
        asm volatile("" ::"v"(pfc));
        tag = (u32)__builtin_amdgcn_readfirstlane(ep) + 0x9E3779B9u;
        f4 acc = {0.f, 0.f, 0.f, 0.f};
#pragma unroll
        for (int i = 0; i < 4; ++i) { acc = mfma16(diag(xc[i], sel0), kk[2 * i], acc); acc = mfma16(diag(xc[i], sel1), kk[2 * i + 1], acc); }
        acc = mfma16(diag(xe, sel0), kk[8], acc);
        char* rp = smem + L_R1 + ps * 4096 + lane * 16;
        *(f4*)(rp + wave * 1024) = acc;
        if (arrive(smem + L_FLAG + 16 + 4 * ps, lane) == 3) {
          acc = (*(const f4*)rp + *(const f4*)(rp + 1024)) + (*(const f4*)(rp + 2048) + *(const f4*)(rp + 3072));
          const int hb = W_H1B + ps * 262144;
          st4(v4u{fu(acc[0]), tag, fu(acc[1]), tag}, ws, lane * 16, hb + id * 2048);
          st4(v4u{fu(acc[2]), tag, fu(acc[3]), tag}, ws, lane * 16 + 1024, hb + id * 2048);
          pin();
          *(volatile LAS u32*)(smem + L_FLAG + 24) = ps + 1;
          if (ps == 0) ST(1);
        }
        spin_flag(smem + L_FLAG + 24, ps + 1);
        if (ps == 0) {
          __builtin_amdgcn_s_sleep(D_H1);
          v4u a, c;
          _Pragma("nounroll") for (int spin = 0; spin < SPIN_CAP; ++spin) {
            pin();
            a = ld4<AUX_SC1>(ws, gq_voff, W_H1B + h * 8192 + (wave >> 1) * 512);
            c = ld4<AUX_SC1>(ws, gq_voff, W_H1B + h * 8192 + (wave >> 1) * 512 + 4096);
            if (none(bad4(bad4(0u, a, tag), c, tag))) break;
            __builtin_amdgcn_s_sleep(2);
          }
          put_q(a, c, 0);
          ST(2);
          spin_flag(smem + L_FLAG + 12, 4);
        }
      }
    } else {
      bar();
      __builtin_amdgcn_s_setprio(3);
      const rsrc_t rw = mk((const char*)P.wuk + (r * 128 + (wave - 4) * 32) * 8192 + h * 256, 31 * 8192 + 256);
      const int voff = (4 * (c16 >> 2) + ((c16 >> 1) & 1)) * 8192 + kq_voff;
#pragma unroll
      for (int i = 0; i < 8; ++i) { kk[i] = ld4<AUX_NT>(rw, voff + (i & 1) * 128, ((i >> 1) & 1) * 16384 + (i >> 2) * 131072); pin(); }
      spin_flag(smem + L_FLAG, 4);
      tag = __builtin_amdgcn_readfirstlane(*(const u32*)(smem + L_FLAG + 8));
      const v4u q0 = *(const v4u*)(smem + L_Q + bsel * 256 + kq_voff), q1 = *(const v4u*)(smem + L_Q + bsel * 256 + 128 + kq_voff);
      u32 o[4];
#pragma unroll
      for (int be = 0; be < 4; ++be) {
        f4 d = {0.f, 0.f, 0.f, 0.f};
        d = mfma16(kk[2 * be], q0, d); d = mfma16(kk[2 * be + 1], q1, d);
        o[be] = pk(d[0] + dppf<XOR1>(d[1]), d[2] + dppf<XOR1>(d[3]));
      }
      const bool up = c16 & 2;
      if ((c16 & 5) == 0) st4(v4u{up ? o[2] : o[0], tag, up ? o[3] : o[1], tag}, ws, bsel * 65536 + (c16 & 2) * 32 + g * 16, W_H2 + h * 2048 + (r * 32 + 8 * (wave - 4)) * 16);
      arrive(smem + L_FLAG + 12, lane);
      ST(3);
      spin_flag(smem + L_FLAG + 24, 2);
    }
    __builtin_amdgcn_s_setprio(1);
    pin();
    {
      const size_t kvoff = ((size_t)(b * 16 + gk) * 8192 + 6144 + r * 512 + wave * 64) * 256;
      const rsrc_t rk = mk((const char*)P.kc + kvoff, 16384), rv = mk((const char*)P.vc + kvoff, 16384);
      const int k_voff = ((c16 & 2) * 2 + (c16 >> 2)) * 256 + kq_voff, v_voff = lane * 16;
      const int xo = (hsh & 3) * 4096;
#pragma unroll
      for (int i = 0; i < 16; ++i) { kk[i] = ld4<AUX_NT>(rk, k_voff + (i & 1) * 128 + ((i >> 1) & 1) * 2048, ((i >> 2) * 4096) ^ xo); pin(); }
      const int qv = wave < 4 ? gq_voff : 0x40000000, qs = W_H1A + (2 * gk + ((wave >> 1) & 1)) * 8192 + b * 512;
      v4u qa = ld4<AUX_SC1>(ws, qv, qs), qc = ld4<AUX_SC1>(ws, qv, qs + 4096);
      pin();
#pragma unroll
      for (int i = 0; i < 16; ++i) { vv[i] = ld4<AUX_NT>(rv, v_voff + (i & 3) * 1024, ((i >> 2) * 4096) ^ xo); pin(); }
      ST(4);
      if (wave < 4) {
        if (!none(bad4(bad4(0u, qa, tag), qc, tag))) {
          _Pragma("nounroll") for (int spin = 0; spin < SPIN_CAP; ++spin) {
            pin();
            qa = ld4<AUX_SC1>(ws, qv, qs); qc = ld4<AUX_SC1>(ws, qv, qs + 4096);
            if (none(bad4(bad4(0u, qa, tag), qc, tag))) break;
          }
        }
        put_q(qa, qc, 1);
      }
    }
    spin_flag(smem + L_FLAG + 4, 4);
    ST(5);
    float sc[16];
    {
      const v4u q0 = *(const v4u*)(smem + L_Q + 512 + bsel * 256 + kq_voff), q1 = *(const v4u*)(smem + L_Q + 512 + bsel * 256 + 128 + kq_voff);
      const bool odd = c16 & 1;
#pragma unroll
      for (int be = 0; be < 8; ++be) {
        f4 d = {0.f, 0.f, 0.f, 0.f};
        d = mfma16(kk[2 * be], q0, d); d = mfma16(kk[2 * be + 1], q1, d);
        const float u0 = odd ? d[1] : d[0], u1 = odd ? d[3] : d[2];
        sc[2 * be] = u0 + dppf<XOR1>(u0); sc[2 * be + 1] = u1 + dppf<XOR1>(u1);
      }
    }
    float m = sc[0];
#pragma unroll
    for (int i = 1; i < 16; ++i) m = fmaxf(m, sc[i]);
    m = swap32_max(swap16_max(m)) * SC;
    float l = 0.f;
#pragma unroll
    for (int be = 0; be < 8; ++be) {
      const float p0 = ex2(fmaf(sc[2 * be], SC, -m)), p1 = ex2(fmaf(sc[2 * be + 1], SC, -m));
      l += p0 + p1;
      const u32 pp = pk(p0, p1);
      kk[2 * be] = diag(pp, sel0); kk[2 * be + 1] = diag(pp, sel1);
    }
    l = swap16_add(l, l); l = swap32_add(l, l);
    if ((lane & 0x37) == 0) { *(float*)(smem + L_ML + bsel * 64 + wave * 4) = m; *(float*)(smem + L_ML + bsel * 64 + 32 + wave * 4) = l; }
    pin();
    f4 acc = {0.f, 0.f, 0.f, 0.f};
#pragma unroll
    for (int i = 0; i < 16; ++i) acc = mfma16(kk[i], vv[i], acc);
    *(f4*)(smem + L_RO + wave * 1024 + lane * 16) = acc;
    ST(6);
    __builtin_amdgcn_s_setprio(0);
    pin();
    load_wo();
    bar();
    ST(7);
    if (wave < 2) {
      const f4 m0 = *(const f4*)(smem + L_ML + wave * 64), m1 = *(const f4*)(smem + L_ML + wave * 64 + 16);
      const f4 l0 = *(const f4*)(smem + L_ML + wave * 64 + 32), l1 = *(const f4*)(smem + L_ML + wave * 64 + 48);
      const float M = fmaxf(fmaxf(fmaxf(m0[0], m0[1]), fmaxf(m0[2], m0[3])), fmaxf(fmaxf(m1[0], m1[1]), fmaxf(m1[2], m1[3])));
      f2 s = {0.f, 0.f};
      float L = 0.f;
#pragma unroll
      for (int w = 0; w < 8; ++w) {
        const float f = ex2((w < 4 ? m0[w & 3] : m1[w & 3]) - M);
        s += f * *(const f2*)(smem + L_RO + w * 1024 + tid * 8);
        L = fmaf(f, w < 4 ? l0[w & 3] : l1[w & 3], L);
      }
      const int rec = ((2 * gk + wave) * 2 + b) * 4 + r;
      st4(v4u{fu(s.x), tag, fu(s.y), tag}, ws, rec_voff, W_HA3D + rec * 1024);
      if (lane < 8) st4(v4u{fu(M), tag, fu(L), tag}, ws, lane * 16, W_HA3S + rec * 128);
      __builtin_amdgcn_s_sleep(D_A3 - 3);
    } else {
      __builtin_amdgcn_s_sleep(D_A3);
    }
  } else {
    constexpr int L_Q = 0, L_P = 32768, L_MX = 40960, L_LS = 41472, L_LAT = 45056, L_ST = 49152, L_R4 = 53248, L_CNT = L_FLAG + 16;
    const int b = id >> 6;
    const u32 ep = __builtin_amdgcn_raw_buffer_load_b32(ws, 0, W_EPOCH, AUX_SC1);
    bar();
    v4u fr[32], qg[8], wv[4];
    const int r32 = lane & 31, hh = lane >> 5;
    const int row_base = r32 * 1024 + 16 * (hh ^ (((r32 & 3) << 2) | ((r32 >> 2) & 3)));
    const int qso = W_H2 + b * 65536 + wave * 8192;
    auto load_q = [&]() {
#pragma unroll
      for (int i = 0; i < 8; ++i) { qg[i] = ld4<AUX_SC1>(ws, lane * 16 + (i & 3) * 1024, qso + (i >> 2) * 4096); pin(); }
    };
    if (wave < 4) {
      const rsrc_t rc = mk((const char*)P.ckv + (size_t)id * 131072 + wave * 8192, 131072 - wave * 8192);
      const int xi = hsh & 4, lx = lane ^ ((2 * wave + (xi >> 2)) & 3);
      if (D_Y0) __builtin_amdgcn_s_sleep(D_Y0);
#pragma unroll
      for (int i = 0; i < 32; ++i) {
        const int ro = (i >> 3) * 32768 + ((i & 7) ^ xi) * 1024;
        __builtin_amdgcn_raw_ptr_buffer_load_lds(rc, (LAS void*)(smem + wave * 8192 + ro), 16, 16 * (lx ^ (((i & 3) << 2) | ((i >> 2) & 1))), ro, 0, AUX_NT);
        pin();
        if (i >= WY - 1 && i < NWY) asm volatile("s_waitcnt vmcnt(%0)" ::"i"(WY - 1) : "memory");
        if (i == 15 || i == 23 || i == 31) {
          asm volatile("s_waitcnt vmcnt(8)" ::: "memory");
          if (lane == 0) asm volatile("ds_add_u32 %0, %1" ::"v"(L_FLAG + 4 * ((i >> 3) - 1)), "v"(1u) : "memory");
          pin();
        }
      }
      if (wave < 3) {
        spin_flag_asm(L_FLAG + 4 * wave, 4);
#pragma unroll
        for (int t = 0; t < 32; ++t)
          asm volatile("ds_read_b128 %0, %1 offset:%2" : "=v"(fr[t]) : "v"(wave * 32768 + (row_base ^ (32 * (t & 7)))), "i"(256 * (t >> 3)) : "memory");
      }
      pin();
      __builtin_amdgcn_s_waitcnt(0x0070);
      pin();
      if (lane == 0) asm volatile("ds_add_u32 %0, %1" ::"v"(L_FLAG + 12), "v"(1u) : "memory");
      ST(1);
      load_q();
      if (wave == 3) {
        spin_flag(smem + L_FLAG + 12, 4);
#pragma unroll
        for (int t = 0; t < 32; ++t) fr[t] = *(const v4u*)(smem + 3 * 32768 + ((row_base ^ (32 * (t & 7))) + 256 * (t >> 3)));
      }
    } else {
      const int q = c16 >> 2, p = lane & 3;
      const int base = (4 * hh + q) * 1024 + (wave - 4) * 256 + 16 * ((2 * (g & 1) + (p >> 1)) ^ (4 * q + hh)) + 8 * (p & 1);
#pragma unroll
      for (int kb = 0; kb < 4; ++kb) {
        spin_flag(smem + L_FLAG + 4 * kb, 4);
        if (kb == GKB) load_q();
#pragma unroll
        for (int ta = 0; ta < 4; ++ta)
#pragma unroll
          for (int s = 0; s < 2; ++s) {
            const char* pa = smem + kb * 32768 + s * 16384;
            const v2u a = __builtin_bit_cast(v2u, __builtin_amdgcn_ds_read_tr16_b64_v4i16((LAS s4v*)(pa + (base ^ (64 * ta)))));
            const v2u c = __builtin_bit_cast(v2u, __builtin_amdgcn_ds_read_tr16_b64_v4i16((LAS s4v*)(pa + (base ^ (64 * ta) ^ 32) + 8192)));
            fr[ta * 8 + 2 * kb + s] = v4u{a.x, a.y, c.x, c.y};
          }
        pin();
      }
    }
    asm volatile("" ::"v"(pfc));
    tag = __builtin_amdgcn_readfirstlane(ep) + 0x9E3779B9u;
    bar();
    ST(2);
    _Pragma("nounroll") for (int spin = 0; spin < SPIN_CAP; ++spin) {
      u32 bad = 0;
#pragma unroll
      for (int i = 0; i < 8; ++i) bad = bad4(bad, qg[i], tag);
      if (none(bad)) break;
      pin();
      __builtin_amdgcn_s_sleep(4);
      load_q();
    }
    pin();
    {
      const rsrc_t rw = mk((const char*)P.wuv + (r * 128 + wave * 16) * 8192 + h * 256, 15 * 8192 + 256);
#pragma unroll
      for (int i = 0; i < 4; ++i) { wv[i] = ld4<AUX_NT>(rw, g * 32768 + c16 * 16, i * 8192); pin(); }
    }
#pragma unroll
    for (int i = 0; i < 8; ++i) {
      const int fh = ((i >> 1) << 2) | (wave & 3);
      *(v2u*)(smem + L_Q + (4 * wave + (i >> 1)) * 1024 + (i & 1) * 512 + 16 * ((lane >> 1) ^ fh) + 8 * (lane & 1)) = v2u{qg[i].x, qg[i].z};
    }
    bar();
    ST(3);
    f16v s;
    if (wave < 4) {
      v4u qb[4];
#define QRD(t) (*(const v4u*)(smem + L_Q + ((row_base ^ (32 * ((t) & 7))) + 256 * ((t) >> 3))))
#pragma unroll
      for (int t = 0; t < 4; ++t) qb[t] = QRD(t);
#pragma unroll
      for (int i = 0; i < 16; ++i) s[i] = 0.f;
      pin();
#pragma unroll
      for (int t = 0; t < 32; ++t) {
        s = mfma32(fr[t], qb[t & 3], s);
        __builtin_amdgcn_sched_barrier(0);
        if (t + 4 < 32) qb[t & 3] = QRD(t + 4);
        __builtin_amdgcn_sched_barrier(0);
      }
      float m = s[0];
#pragma unroll
      for (int i = 1; i < 16; ++i) m = fmaxf(m, s[i]);
      m = swap32_max(m);
      if (lane < 32) *(float*)(smem + L_MX + lane * 16 + wave * 4) = m;
    }
    bar();
    if (wave < 4) {
      const f4 mm = *(const f4*)(smem + L_MX + r32 * 16);
      const float m = fmaxf(fmaxf(mm[0], mm[1]), fmaxf(mm[2], mm[3])) * SC;
      float l = 0.f;
      u32 pp[8];
#pragma unroll
      for (int i = 0; i < 8; ++i) {
        const float p0 = ex2(fmaf(s[2 * i], SC, -m)), p1 = ex2(fmaf(s[2 * i + 1], SC, -m));
        l += p0 + p1;
        pp[i] = pk(p0, p1);
      }
      *(v4u*)(smem + L_P + wave * 2048 + lane * 16) = v4u{pp[0], pp[1], pp[2], pp[3]};
      *(v4u*)(smem + L_P + wave * 2048 + 1024 + lane * 16) = v4u{pp[4], pp[5], pp[6], pp[7]};
      l = swap32_add(l, l);
      if (lane < 32) *(float*)(smem + L_LS + lane * 16 + wave * 4) = l;
    }
    bar();
    ST(4);
    if (wave >= 4) {
      v4u pf[8];
#pragma unroll
      for (int ks = 0; ks < 8; ++ks) pf[ks] = *(const v4u*)(smem + L_P + ks * 1024 + lane * 16);
      f16v o0, o1, o2, o3;
#pragma unroll
      for (int i = 0; i < 16; ++i) { o0[i] = 0.f; o1[i] = 0.f; o2[i] = 0.f; o3[i] = 0.f; }
      const int voff = hh * 524288 + r32 * 4, soff = W_H3D + (wave - 4) * 32768 + id * 256;
      pin();
#pragma unroll
      for (int ks = 0; ks < 8; ++ks) { o0 = mfma32(pf[ks], fr[ks], o0); o1 = mfma32(pf[ks], fr[8 + ks], o1); }
      __builtin_amdgcn_sched_barrier(0);
#define OST(a, c, i, pi) __builtin_amdgcn_raw_buffer_store_b32(pk(a[i], c[i]), ws, voff + (pi) * 128, soff + (((i) & 3) + 8 * ((i) >> 2)) * 131072, AUX_SC1)
#pragma unroll
      for (int ks = 0; ks < 8; ++ks) {
        o2 = mfma32(pf[ks], fr[16 + ks], o2);
        __builtin_amdgcn_sched_barrier(0);
        if (ks) OST(o0, o1, 2 * ks - 2, 0);
        __builtin_amdgcn_sched_barrier(0);
        o3 = mfma32(pf[ks], fr[24 + ks], o3);
        __builtin_amdgcn_sched_barrier(0);
        if (ks) OST(o0, o1, 2 * ks - 1, 0);
        __builtin_amdgcn_sched_barrier(0);
      }
      OST(o0, o1, 14, 0); OST(o0, o1, 15, 0);
#pragma unroll
      for (int i = 0; i < 16; ++i) OST(o2, o3, i, 1);
    }
    bar();
    ST(5);
    if (wave >= 4) {
      __builtin_amdgcn_s_setprio(3);
      asm volatile("s_waitcnt vmcnt(0)" ::: "memory");
      if (arrive(smem + L_CNT, lane) == 3) {
        const int hd = (lane >> 3) * 4 + (lane & 3);
        const f4 mm = *(const f4*)(smem + L_MX + hd * 16), ll = *(const f4*)(smem + L_LS + hd * 16);
        st4(v4u{fu(fmaxf(fmaxf(mm[0], mm[1]), fmaxf(mm[2], mm[3])) * SC), tag, fu((ll[0] + ll[1]) + (ll[2] + ll[3])), tag}, ws,
            (lane >> 3) * 16384 + (lane & 7) * 16, W_H3S + id * 128);
      }
      __builtin_amdgcn_s_setprio(0);
      ST(6);
    }
    pin();
    load_wo();
    if (wave < 4) __builtin_amdgcn_s_sleep(D_H3 + 14); else __builtin_amdgcn_s_sleep(D_H3);
    {
      v4u st;
      _Pragma("nounroll") for (int spin = 0; spin < SPIN_CAP; ++spin) {
        pin();
        st = ld4<AUX_SC1>(ws, (lane >> 2) * 128, W_H3S + x * 16384 + wave * 2048 + (h & 3) * 16);
        if (none(bad4(0u, st, tag))) break;
        __builtin_amdgcn_s_sleep(3);
      }
      pin();
      ST(7);
      v4u d[4];
#pragma unroll
      for (int i = 0; i < 4; ++i) d[i] = ld4<AUX_SC1>(ws, g * 1024 + c16 * 16 + i * 256, W_H3D + id * 32768 + wave * 4096);
      pin();
      float M = uf(st.x);
      M = fmaxf(M, dppf<ROR4>(M)); M = fmaxf(M, dppf<ROR8>(M)); M = swap32_max(swap16_max(M));
      const float wg = ex2(uf(st.x) - M);
      float L = wg * uf(st.z);
      L += dppf<ROR4>(L); L += dppf<ROR8>(L); L = swap16_add(L, L); L = swap32_add(L, L);
      if (lane == 0) *(f2*)(smem + L_ST + wave * 8) = f2{M, L};
      const float wi[4] = {dppf<BCAST>(wg), dppf<BCAST + 4>(wg), dppf<BCAST + 8>(wg), dppf<BCAST + 12>(wg)};
      float a[8];
#pragma unroll
      for (int k = 0; k < 4; ++k) {
        a[2 * k] = wi[0] * blo(d[0][k]); a[2 * k + 1] = wi[0] * bhi(d[0][k]);
#pragma unroll
        for (int i = 1; i < 4; ++i) { a[2 * k] = fmaf(wi[i], blo(d[i][k]), a[2 * k]); a[2 * k + 1] = fmaf(wi[i], bhi(d[i][k]), a[2 * k + 1]); }
      }
      const float lo = swap16_add(swap32_add(a[0], a[4]), swap32_add(a[2], a[6]));
      const float hi = swap16_add(swap32_add(a[1], a[5]), swap32_add(a[3], a[7]));
      float* lat = (float*)(smem + L_LAT + wave * 512) + 64 * bsel + 4 * (c16 & 7) + g;
      lat[0] = lo; lat[32] = hi;
    }
    bar();
    {
      const f4 s0 = *(const f4*)(smem + L_ST + bsel * 32), s1 = *(const f4*)(smem + L_ST + bsel * 32 + 16);
      const float M = fmaxf(fmaxf(s0[0], s0[2]), fmaxf(s1[0], s1[2]));
      const float f[4] = {ex2(s0[0] - M), ex2(s0[2] - M), ex2(s1[0] - M), ex2(s1[2] - M)};
      const float inv = __builtin_amdgcn_rcpf(fmaf(f[0], s0[1], fmaf(f[1], s0[3], fmaf(f[2], s1[1], f[3] * s1[3]))));
      f4 c = {0.f, 0.f, 0.f, 0.f};
#pragma unroll
      for (int w = 0; w < 4; ++w) c += f[w] * *(const f4*)(smem + L_LAT + (bsel * 4 + w) * 512 + wave * 64 + g * 16);
      c *= inv;
      *(f4*)(smem + L_R4 + wave * 1024 + lane * 16) = rows16(pk(c[0], c[1]), pk(c[2], c[3]), wv, sel0, sel1);
    }
    bar();
    if (wave < 2) {
      const f2 sv = sum8(smem + L_R4 + tid * 8);
      st4(v4u{fu(sv.x), tag, fu(sv.y), tag}, ws, rec_voff, W_H4 + ((h * 2 + wave) * 4 + r) * 1024);
      __builtin_amdgcn_s_sleep(D_H4 - 2);
    } else {
      __builtin_amdgcn_s_sleep(D_H4);
    }
  }
  ST(8);
  {
    v4u d0, d1, st = {0u, tag, 0x3e800000u, tag};
    const int dv = (bsel * 4 + (c16 & 3)) * 1024 + g * 32, dso = (isX ? W_HA3D : W_H4) + h * 8192 + wave * 128;
    _Pragma("nounroll") for (int spin = 0; spin < SPIN_CAP; ++spin) {
      pin();
      d0 = ld4<AUX_SC1>(ws, dv, dso);
      d1 = ld4<AUX_SC1>(ws, dv + 16, dso);
      if (isX) st = ld4<AUX_SC1>(ws, (bsel * 4 + (c16 & 3)) * 128, W_HA3S + h * 1024);
      if (none(bad4(bad4(bad4(0u, d0, tag), d1, tag), st, tag))) break;
      __builtin_amdgcn_s_sleep(2);
    }
    ST(9);
    float M = uf(st.x);
    M = fmaxf(M, dppf<XOR1>(M)); M = fmaxf(M, dppf<XOR2>(M));
    float w = ex2(uf(st.x) - M);
    w *= __builtin_amdgcn_rcpf(quad_sum(w * uf(st.z)));
    const u32 c01 = pk(quad_sum(w * uf(d0.x)), quad_sum(w * uf(d0.z))), c23 = pk(quad_sum(w * uf(d1.x)), quad_sum(w * uf(d1.z)));
    *(f4*)(smem + L_RED + wave * 1024 + lane * 16) = rows16(c01, c23, wo, sel0, sel1);
    if (r == 3) *(f4*)(smem + L_REDE + wave * 1024 + lane * 16) = rows16(c01, c23, we, sel0, sel1);
  }
  bar();
  const int part = isX ? 0 : 1;
  if (wave < 4) {
    if (wave < 2 || (r == 3 && (lane & 16) == 0)) {
      const f2 sv = sum8(smem + L_RED + (wave >> 1) * 8192 + (tid & 127) * 8);
      st4(v4u{fu(sv.x), tag, fu(sv.y), tag}, ws, (lane >> 3) * 4096 + (lane & 7) * 16,
          W_H5 + (part * 80 + (wave < 2 ? r * 16 : 64) + (wave & 1) * 8) * 4096 + h * 128);
    }
    ST(10);
  } else if (wave == 4 && id < 72) {
    const int e = id - 64, L = id < 64 ? id : 64 + (e & 1) + 4 * (e >> 1);
    v4u v[4];
    __builtin_amdgcn_s_sleep(D_H5);
    _Pragma("nounroll") for (int spin = 0; spin < SPIN_CAP; ++spin) {
      pin();
      u32 bad = 0;
#pragma unroll
      for (int i = 0; i < 4; ++i) v[i] = ld4<AUX_SC1>(ws, lane * 16 + i * 1024, W_H5 + (part * 80 + L) * 4096);
#pragma unroll
      for (int i = 0; i < 4; ++i) bad = bad4(bad, v[i], tag);
      if (none(bad)) break;
      __builtin_amdgcn_s_sleep(2);
    }
    float a = (uf(v[0].x) + uf(v[1].x)) + (uf(v[2].x) + uf(v[3].x)), c = (uf(v[0].z) + uf(v[1].z)) + (uf(v[2].z) + uf(v[3].z));
    a += dppf<ROR8>(a); c += dppf<ROR8>(c);
    a = swap16_add(a, a); c = swap16_add(c, c);
    a = swap32_add(a, a); c = swap32_add(c, c);
    if (lane < 8) {
      const int al = L & 15;
      const int n = (al >> 3) * 1152 + part * 576 + 128 * (L >> 4) + 32 * (al & 3) + 8 * (lane >> 1) + 4 * ((al >> 2) & 1) + 2 * (lane & 1);
      ((u32*)P.out)[n >> 1] = pk(a, c);
    }
    if (!isX && id == 0 && lane == 0) __builtin_amdgcn_raw_buffer_store_b32(tag, ws, 0, W_EPOCH, AUX_SC1);
  }
  ST(11);
  if (lane == 0) {
    u64* sp = (u64*)((char*)P.ws + W_STAMP) + (bid * 8 + wave) * 12;
#pragma unroll
    for (int i = 0; i < 12; ++i) __hip_atomic_store(sp + i, ts[i], __ATOMIC_RELAXED, __HIP_MEMORY_SCOPE_AGENT);
  }
  if (bid == 0 && wave == 7) {
    while (__builtin_amdgcn_s_memrealtime() - ts[0] < 7000) __builtin_amdgcn_s_sleep(8);
    u64* sp = (u64*)((char*)P.ws + W_STAMP);
    u64 mn = ~0ull;
    for (int e = lane; e < 2048; e += 64) { u64 v = __hip_atomic_load(sp + e * 12, __ATOMIC_RELAXED, __HIP_MEMORY_SCOPE_AGENT); mn = v < mn ? v : mn; }
    for (int o = 32; o; o >>= 1) { u64 t = __shfl_xor(mn, o); mn = t < mn ? t : mn; }
    float sum = 0.f, cnt = 0.f, mx = 0.f;
    for (int e = lane; e < 2048; e += 64) {
      u64 v = __hip_atomic_load(sp + e * 12 + REP_IDX, __ATOMIC_RELAXED, __HIP_MEMORY_SCOPE_AGENT);
      const int role = (e >> 6) >= 16;
      if (v && (REP_ROLE == 2 || role == REP_ROLE)) { float d = (float)(v - mn); sum += d; cnt += 1.f; mx = fmaxf(mx, d); }
    }
    for (int o = 32; o; o >>= 1) { sum += __shfl_xor(sum, o); cnt += __shfl_xor(cnt, o); mx = fmaxf(mx, __shfl_xor(mx, o)); }
    float q = REP_STAT == 0 ? 0.f : REP_STAT == 1 ? sum / cnt : mx;
    q = fminf(q, 6000.f);
    const u64 until = mn + 10000 + (u64)(4.f * q);
    while (__builtin_amdgcn_s_memrealtime() < until) __builtin_amdgcn_s_sleep(1);
  }
}

extern "C" void kernel_launch(void* const* d_in, const int* in_sizes, int n_in, void* d_out, int out_size, void* d_ws, size_t ws_size,
                              hipStream_t stream) {
  Params p;
  p.xA = d_in[0]; p.kc = d_in[1]; p.vc = d_in[2]; p.wqA = d_in[3]; p.woA = d_in[4];
  p.xB = d_in[5]; p.ckv = d_in[6]; p.wqB = d_in[7]; p.wuk = d_in[8]; p.wuv = d_in[9]; p.woB = d_in[10];
  p.out = d_out; p.ws = (char*)d_ws + (WS_AT_END ? ((ws_size - (18 << 20)) & ~(size_t)0x1fffff) : 0);
  fused_decode<<<256, 512, 0, stream>>>(p);
}
```

```cpp
#define STAMP 1
#define CODE_BYTES 17920
#define REP_IDX 0
#define REP_ROLE 2
#define REP_STAT 0
#define PREK 1
#define PREG 256
#include <hip/hip_runtime.h>
#include <hip/hip_bf16.h>
#include <stdint.h>

typedef unsigned u32;
typedef unsigned long long u64;
typedef u32 v4u __attribute__((ext_vector_type(4)));
typedef u32 v2u __attribute__((ext_vector_type(2)));
typedef float f2 __attribute__((ext_vector_type(2)));
typedef float f4 __attribute__((ext_vector_type(4)));
typedef float f16v __attribute__((ext_vector_type(16)));
typedef __bf16 bf8 __attribute__((ext_vector_type(8)));
typedef __bf16 bf2 __attribute__((ext_vector_type(2)));
typedef short s4v __attribute__((ext_vector_type(4)));
typedef __amdgpu_buffer_rsrc_t rsrc_t;
#define LAS __attribute__((address_space(3)))
#define DI __device__ __forceinline__

constexpr int AUX_NT = 2, AUX_SC1 = 16;
constexpr int W_EPOCH = 0;
constexpr int W_H1B = 4096;
constexpr int W_H1A = W_H1B + 262144;
constexpr int W_H2 = W_H1A + 262144;
constexpr int W_H3S = W_H2 + 131072;
constexpr int W_HA3S = W_H3S + 131072;
constexpr int W_HA3D = W_HA3S + 32768;
constexpr int W_H4 = W_HA3D + 262144;
constexpr int W_H5 = W_H4 + 262144;
constexpr int W_H3D = W_H5 + 2 * 80 * 4096;
constexpr int W_END = W_H3D + 4194304;
constexpr int W_STAMP = 16 << 20;
constexpr float SC = 0.08838834764831845f * 1.4426950408889634f;
#define D_H1 12
#define WA 9
#define D_Y0 16
#define WY 3
#define NWY 8
#define D_A3 14
#define D_H3 12
#define D_H4 14
#define D_H5 14
#define GKB 3
#define PF_AUX 0
#define WS_AT_END 0
#define HOLD 1
constexpr int SPIN_CAP = 1 << 17;

struct Params {
  const void *xA, *kc, *vc, *wqA, *woA, *xB, *ckv, *wqB, *wuk, *wuv, *woB;
  void *out, *ws;
};

DI rsrc_t mk(const void* p, u32 bytes) { return __builtin_amdgcn_make_buffer_rsrc((void*)p, 0, bytes, 0x00020000); }
template <int AUX> DI v4u ld4(rsrc_t r, int voff, int soff) { return __builtin_amdgcn_raw_buffer_load_b128(r, voff, soff, AUX); }
template <int AUX> DI v2u ld2(rsrc_t r, int voff, int soff) { return __builtin_amdgcn_raw_buffer_load_b64(r, voff, soff, AUX); }
DI void st4(v4u v, rsrc_t r, int voff, int soff) { __builtin_amdgcn_raw_buffer_store_b128(v, r, voff + soff, 0, AUX_SC1); }
DI u32 fu(float f) { return __builtin_bit_cast(u32, f); }
DI float uf(u32 u) { return __builtin_bit_cast(float, u); }
DI u32 pk(float lo, float hi) { f2 f = {lo, hi}; return __builtin_bit_cast(u32, __builtin_convertvector(f, bf2)); }
DI float blo(u32 u) { return uf(u << 16); }
DI float bhi(u32 u) { return uf(u & 0xffff0000u); }
template <int C> DI u32 dppu(u32 x) { return __builtin_amdgcn_mov_dpp(x, C, 0xf, 0xf, true); }
template <int C> DI float dppf(float x) { return uf(dppu<C>(fu(x))); }
constexpr int XOR1 = 0xB1, XOR2 = 0x4E, ROR4 = 0x124, ROR8 = 0x128, BCAST = 0x150;
DI f4 mfma16(v4u a, v4u b, f4 c) { return __builtin_amdgcn_mfma_f32_16x16x32_bf16(__builtin_bit_cast(bf8, a), __builtin_bit_cast(bf8, b), c, 0, 0, 0); }
DI f16v mfma32(v4u a, v4u b, f16v c) { return __builtin_amdgcn_mfma_f32_32x32x16_bf16(__builtin_bit_cast(bf8, a), __builtin_bit_cast(bf8, b), c, 0, 0, 0); }
DI float ex2(float x) { return __builtin_amdgcn_exp2f(x); }
DI float swap32_add(float a, float b) { auto s = __builtin_amdgcn_permlane32_swap(fu(a), fu(b), false, false); return uf(s[0]) + uf(s[1]); }
DI float swap16_add(float a, float b) { auto s = __builtin_amdgcn_permlane16_swap(fu(a), fu(b), false, false); return uf(s[0]) + uf(s[1]); }
DI float swap32_max(float a) { auto s = __builtin_amdgcn_permlane32_swap(fu(a), fu(a), false, false); return fmaxf(uf(s[0]), uf(s[1])); }
DI float swap16_max(float a) { auto s = __builtin_amdgcn_permlane16_swap(fu(a), fu(a), false, false); return fmaxf(uf(s[0]), uf(s[1])); }
DI void bar() { asm volatile("s_waitcnt lgkmcnt(0)\n\ts_barrier" ::: "memory"); }
DI void pin() { asm volatile("" ::: "memory"); __builtin_amdgcn_sched_barrier(0); }
DI u32 bad4(u32 acc, v4u v, u32 tag) { return (acc | (v.y ^ tag)) | (v.w ^ tag); }
DI bool none(u32 bad) { return __builtin_amdgcn_ballot_w64(bad != 0) == 0; }
DI v4u diag(u32 src, const u32 (&sel)[4]) {
  v4u a;
#pragma unroll
  for (int k = 0; k < 4; ++k) a[k] = __builtin_amdgcn_perm(src, src, sel[k]);
  return a;
}

#define SMASK 0xfff
#define ST(i) do { if ((SMASK >> (i)) & 1) asm volatile("s_memrealtime %0\n\ts_waitcnt lgkmcnt(0)" : "=s"(ts[i]) :: "memory"); } while (0)

DI void spin_flag(const char* p, u32 want = 1) {
  _Pragma("nounroll") for (int spin = 0; spin < SPIN_CAP * 8; ++spin) {
    if ((u32)__builtin_amdgcn_readfirstlane(*(volatile const LAS u32*)p) >= want) break;
    __builtin_amdgcn_s_sleep(1);
  }
  pin();
}
DI u32 arrive(char* p, int lane) {
  u32 old = 0;
  pin();
  if (lane == 0) old = __hip_atomic_fetch_add((LAS u32*)p, 1u, __ATOMIC_RELAXED, __HIP_MEMORY_SCOPE_WORKGROUP);
  old = __builtin_amdgcn_readfirstlane(old);
  pin();
  return old;
}
DI void spin_flag_asm(u32 addr, u32 want) {
  _Pragma("nounroll") for (int spin = 0; spin < SPIN_CAP * 8; ++spin) {
    u32 v;
    asm volatile("ds_read_b32 %0, %1\n\ts_waitcnt lgkmcnt(0)" : "=v"(v) : "v"(addr) : "memory");
    if ((u32)__builtin_amdgcn_readfirstlane(v) >= want) break;
    __builtin_amdgcn_s_sleep(1);
  }
  pin();
}
DI float quad_sum(float v) { v += dppf<XOR1>(v); return v + dppf<XOR2>(v); }
DI f2 sum8(const char* p) {
  f2 s = *(const f2*)p;
#pragma unroll
  for (int w = 1; w < 8; ++w) s += *(const f2*)(p + w * 1024);
  return s;
}
DI f4 rows16(u32 c01, u32 c23, const v4u (&w)[4], const u32 (&sel0)[4], const u32 (&sel1)[4]) {
  f4 acc = {0.f, 0.f, 0.f, 0.f};
  acc = mfma16(diag(c01, sel0), w[0], acc); acc = mfma16(diag(c01, sel1), w[1], acc);
  acc = mfma16(diag(c23, sel0), w[2], acc); acc = mfma16(diag(c23, sel1), w[3], acc);
  return acc;
}

__global__ void __launch_bounds__(512, 2) fused_decode(Params P) {
  __shared__ __attribute__((aligned(16))) char smem[131072 + 32];
  u64 ts[12];
#pragma unroll
  for (int i = 0; i < 12; ++i) ts[i] = 0;
  ST(0);
  const int tid = threadIdx.x, lane = tid & 63, wave = __builtin_amdgcn_readfirstlane(tid >> 6);
  const int bid = blockIdx.x, x = bid & 7, jj = bid >> 3, j = jj & 15;
  const int g = lane >> 4, c16 = lane & 15, bsel = c16 >> 3;
  const int h = 4 * x + (j >> 2), r = j & 3, id = x * 16 + j;
  u32 pfc = 0;
  if (tid < 16) {
    u64 pc;
    asm volatile("s_getpc_b64 %0" : "=s"(pc));
    pfc = __builtin_amdgcn_raw_buffer_load_b32(mk((const void*)(pc & ~255ull), CODE_BYTES), tid * 64, jj * 1024, PF_AUX);
  }
  u32 sel0[4], sel1[4];
#pragma unroll
  for (int k = 0; k < 4; ++k) {
    const bool on = ((c16 & 7) >> 1) == k;
    sel0[k] = on ? ((c16 & 1) ? 0x01000c0cu : 0x0c0c0100u) : 0x0c0c0c0cu;
    sel1[k] = on ? ((c16 & 1) ? 0x03020c0cu : 0x0c0c0302u) : 0x0c0c0c0cu;
  }
  const int hsh = (bid * 2654435761u) >> 20;
  constexpr int L_FLAG = 131072;
  if (tid < 8) *(u32*)(smem + L_FLAG + tid * 4) = 0u;
  asm volatile("" : "+v"(sel0[0]), "+v"(sel0[1]), "+v"(sel0[2]), "+v"(sel0[3]), "+v"(sel1[0]), "+v"(sel1[1]), "+v"(sel1[2]), "+v"(sel1[3]));
  asm volatile("" ::"s"(P.xA), "s"(P.kc), "s"(P.vc), "s"(P.wqA), "s"(P.woA), "s"(P.xB), "s"(P.ckv), "s"(P.wqB), "s"(P.wuk), "s"(P.wuv), "s"(P.woB), "s"(P.out), "s"(P.ws));
  const rsrc_t ws = mk(P.ws, W_END);
  const bool isX = jj < 16;
  const int rec_voff = 64 * ((lane >> 1) & 15) + 32 * (lane >> 5) + 16 * (lane & 1);
  constexpr int L_RED = 65536, L_REDE = L_RED + 8192;
  u32 tag;
  v4u wo[4], we[4];
  auto load_wo = [&]() {
    const rsrc_t rw = mk((const char*)(isX ? P.woA : P.woB) + (h * 128 + wave * 16) * 1152, 16 * 1152);
    const int voff = g * 4608 + c16 * 16;
#pragma unroll
    for (int i = 0; i < 4; ++i) { wo[i] = ld4<AUX_NT>(rw, voff + i * 1152, r * 256); pin(); }
    if (r == 3) {
      const int ve = bsel ? 0x40000000 : voff;
#pragma unroll
      for (int i = 0; i < 4; ++i) { we[i] = ld4<AUX_NT>(rw, ve + i * 1152, 1024); pin(); }
    }
  };

  if (__builtin_expect(isX, 1)) {
    constexpr int L_RO = 0, L_Q = 8192, L_ML = 9216, L_R1 = 16384;
    const int gk = 2 * x + (j >> 3), b = (j >> 2) & 1;
    const int kq_voff = (c16 & 1) * 64 + g * 16;
    v4u kk[16], vv[16];
    const int gq_voff = (lane >> 5) * 2048 + (wave & 1) * 1024 + (lane & 31) * 16;
    auto put_q = [&](v4u a, v4u c, int f) {
      const float s0 = uf(a.x) + uf(c.x), s1 = uf(a.z) + uf(c.z);
      const u32 pq = pk(swap32_add(s0, s0), swap32_add(s1, s1));
      if (lane < 32) *(u32*)(smem + L_Q + f * 512 + (wave >> 1) * 256 + 16 * c16 + 8 * g + 4 * (wave & 1)) = pq;
      *(u32*)(smem + L_FLAG + 8) = tag;
      arrive(smem + L_FLAG + 4 * f, lane);
    };
    if (wave < 4) {
      u32 ep = __builtin_amdgcn_raw_buffer_load_b32(ws, 0, W_EPOCH, AUX_SC1);
      const int xv = bsel * 1152 + r * 288 + wave * 72;
      const int wv8 = g * 65536 + c16 * 16, wv9 = (32 + g) * 8192 + c16 * 16;
      const size_t wbase = (size_t)(r * 144 + wave * 36) * 8192 + h * 256;
      __builtin_amdgcn_s_setprio(2);
      _Pragma("nounroll") for (int ps = 0; ps < 2; ++ps) {
        const rsrc_t rx = mk(ps ? P.xA : P.xB, 2304), rw = mk((const char*)(ps ? P.wqA : P.wqB) + wbase, 35 * 8192 + 256);
        const v4u xc = ld4<0>(rx, xv + g * 16, 0);
        const u32 xe = __builtin_amdgcn_raw_buffer_load_b16(rx, xv + 64 + g * 2, 0, 0);
        pin();
#pragma unroll
        for (int i = 0; i < 8; ++i) {
          kk[i] = ld4<AUX_NT>(rw, wv8, i * 8192);
          pin();
          if (i >= WA - 1 && ps) asm volatile("s_waitcnt vmcnt(%0)" ::"i"(WA - 1) : "memory");
        }
        kk[8] = ld4<AUX_NT>(rw, wv9, 0);
        if (ps == 0) bar();
        pin();
        asm volatile("" ::"v"(pfc));
        tag = (u32)__builtin_amdgcn_readfirstlane(ep) + 0x9E3779B9u;
        f4 acc = {0.f, 0.f, 0.f, 0.f};
#pragma unroll
        for (int i = 0; i < 4; ++i) { acc = mfma16(diag(xc[i], sel0), kk[2 * i], acc); acc = mfma16(diag(xc[i], sel1), kk[2 * i + 1], acc); }
        acc = mfma16(diag(xe, sel0), kk[8], acc);
        char* rp = smem + L_R1 + ps * 4096 + lane * 16;
        *(f4*)(rp + wave * 1024) = acc;
        if (arrive(smem + L_FLAG + 16 + 4 * ps, lane) == 3) {
          acc = (*(const f4*)rp + *(const f4*)(rp + 1024)) + (*(const f4*)(rp + 2048) + *(const f4*)(rp + 3072));
          const int hb = W_H1B + ps * 262144;
          st4(v4u{fu(acc[0]), tag, fu(acc[1]), tag}, ws, lane * 16, hb + id * 2048);
          st4(v4u{fu(acc[2]), tag, fu(acc[3]), tag}, ws, lane * 16 + 1024, hb + id * 2048);
          pin();
          *(volatile LAS u32*)(smem + L_FLAG + 24) = ps + 1;
          if (ps == 0) ST(1);
        }
        spin_flag(smem + L_FLAG + 24, ps + 1);
        if (ps == 0) {
          __builtin_amdgcn_s_sleep(D_H1);
          v4u a, c;
          _Pragma("nounroll") for (int spin = 0; spin < SPIN_CAP; ++spin) {
            pin();
            a = ld4<AUX_SC1>(ws, gq_voff, W_H1B + h * 8192 + (wave >> 1) * 512);
            c = ld4<AUX_SC1>(ws, gq_voff, W_H1B + h * 8192 + (wave >> 1) * 512 + 4096);
            if (none(bad4(bad4(0u, a, tag), c, tag))) break;
            __builtin_amdgcn_s_sleep(2);
          }
          put_q(a, c, 0);
          ST(2);
          spin_flag(smem + L_FLAG + 12, 4);
        }
      }
    } else {
      bar();
      __builtin_amdgcn_s_setprio(3);
      const rsrc_t rw = mk((const char*)P.wuk + (r * 128 + (wave - 4) * 32) * 8192 + h * 256, 31 * 8192 + 256);
      const int voff = (4 * (c16 >> 2) + ((c16 >> 1) & 1)) * 8192 + kq_voff;
#pragma unroll
      for (int i = 0; i < 8; ++i) { kk[i] = ld4<AUX_NT>(rw, voff + (i & 1) * 128, ((i >> 1) & 1) * 16384 + (i >> 2) * 131072); pin(); }
      spin_flag(smem + L_FLAG, 4);
      tag = __builtin_amdgcn_readfirstlane(*(const u32*)(smem + L_FLAG + 8));
      const v4u q0 = *(const v4u*)(smem + L_Q + bsel * 256 + kq_voff), q1 = *(const v4u*)(smem + L_Q + bsel * 256 + 128 + kq_voff);
      u32 o[4];
#pragma unroll
      for (int be = 0; be < 4; ++be) {
        f4 d = {0.f, 0.f, 0.f, 0.f};
        d = mfma16(kk[2 * be], q0, d); d = mfma16(kk[2 * be + 1], q1, d);
        o[be] = pk(d[0] + dppf<XOR1>(d[1]), d[2] + dppf<XOR1>(d[3]));
      }
      const bool up = c16 & 2;
      if ((c16 & 5) == 0) st4(v4u{up ? o[2] : o[0], tag, up ? o[3] : o[1], tag}, ws, bsel * 65536 + (c16 & 2) * 32 + g * 16, W_H2 + h * 2048 + (r * 32 + 8 * (wave - 4)) * 16);
      arrive(smem + L_FLAG + 12, lane);
      ST(3);
      spin_flag(smem + L_FLAG + 24, 2);
    }
    __builtin_amdgcn_s_setprio(1);
    pin();
    {
      const size_t kvoff = ((size_t)(b * 16 + gk) * 8192 + 6144 + r * 512 + wave * 64) * 256;
      const rsrc_t rk = mk((const char*)P.kc + kvoff, 16384), rv = mk((const char*)P.vc + kvoff, 16384);
      const int k_voff = ((c16 & 2) * 2 + (c16 >> 2)) * 256 + kq_voff, v_voff = lane * 16;
      const int xo = (hsh & 3) * 4096;
#pragma unroll
      for (int i = 0; i < 16; ++i) { kk[i] = ld4<AUX_NT>(rk, k_voff + (i & 1) * 128 + ((i >> 1) & 1) * 2048, ((i >> 2) * 4096) ^ xo); pin(); }
      const int qv = wave < 4 ? gq_voff : 0x40000000, qs = W_H1A + (2 * gk + ((wave >> 1) & 1)) * 8192 + b * 512;
      v4u qa = ld4<AUX_SC1>(ws, qv, qs), qc = ld4<AUX_SC1>(ws, qv, qs + 4096);
      pin();
#pragma unroll
      for (int i = 0; i < 16; ++i) { vv[i] = ld4<AUX_NT>(rv, v_voff + (i & 3) * 1024, ((i >> 2) * 4096) ^ xo); pin(); }
      ST(4);
      if (wave < 4) {
        if (!none(bad4(bad4(0u, qa, tag), qc, tag))) {
          _Pragma("nounroll") for (int spin = 0; spin < SPIN_CAP; ++spin) {
            pin();
            qa = ld4<AUX_SC1>(ws, qv, qs); qc = ld4<AUX_SC1>(ws, qv, qs + 4096);
            if (none(bad4(bad4(0u, qa, tag), qc, tag))) break;
          }
        }
        put_q(qa, qc, 1);
      }
    }
    spin_flag(smem + L_FLAG + 4, 4);
    ST(5);
    float sc[16];
    {
      const v4u q0 = *(const v4u*)(smem + L_Q + 512 + bsel * 256 + kq_voff), q1 = *(const v4u*)(smem + L_Q + 512 + bsel * 256 + 128 + kq_voff);
      const bool odd = c16 & 1;
#pragma unroll
      for (int be = 0; be < 8; ++be) {
        f4 d = {0.f, 0.f, 0.f, 0.f};
        d = mfma16(kk[2 * be], q0, d); d = mfma16(kk[2 * be + 1], q1, d);
        const float u0 = odd ? d[1] : d[0], u1 = odd ? d[3] : d[2];
        sc[2 * be] = u0 + dppf<XOR1>(u0); sc[2 * be + 1] = u1 + dppf<XOR1>(u1);
      }
    }
    float m = sc[0];
#pragma unroll
    for (int i = 1; i < 16; ++i) m = fmaxf(m, sc[i]);
    m = swap32_max(swap16_max(m)) * SC;
    float l = 0.f;
#pragma unroll
    for (int be = 0; be < 8; ++be) {
      const float p0 = ex2(fmaf(sc[2 * be], SC, -m)), p1 = ex2(fmaf(sc[2 * be + 1], SC, -m));
      l += p0 + p1;
      const u32 pp = pk(p0, p1);
      kk[2 * be] = diag(pp, sel0); kk[2 * be + 1] = diag(pp, sel1);
    }
    l = swap16_add(l, l); l = swap32_add(l, l);
    if ((lane & 0x37) == 0) { *(float*)(smem + L_ML + bsel * 64 + wave * 4) = m; *(float*)(smem + L_ML + bsel * 64 + 32 + wave * 4) = l; }
    pin();
    f4 acc = {0.f, 0.f, 0.f, 0.f};
#pragma unroll
    for (int i = 0; i < 16; ++i) acc = mfma16(kk[i], vv[i], acc);
    *(f4*)(smem + L_RO + wave * 1024 + lane * 16) = acc;
    ST(6);
    __builtin_amdgcn_s_setprio(0);
    pin();
    load_wo();
    bar();
    ST(7);
    if (wave < 2) {
      const f4 m0 = *(const f4*)(smem + L_ML + wave * 64), m1 = *(const f4*)(smem + L_ML + wave * 64 + 16);
      const f4 l0 = *(const f4*)(smem + L_ML + wave * 64 + 32), l1 = *(const f4*)(smem + L_ML + wave * 64 + 48);
      const float M = fmaxf(fmaxf(fmaxf(m0[0], m0[1]), fmaxf(m0[2], m0[3])), fmaxf(fmaxf(m1[0], m1[1]), fmaxf(m1[2], m1[3])));
      f2 s = {0.f, 0.f};
      float L = 0.f;
#pragma unroll
      for (int w = 0; w < 8; ++w) {
        const float f = ex2((w < 4 ? m0[w & 3] : m1[w & 3]) - M);
        s += f * *(const f2*)(smem + L_RO + w * 1024 + tid * 8);
        L = fmaf(f, w < 4 ? l0[w & 3] : l1[w & 3], L);
      }
      const int rec = ((2 * gk + wave) * 2 + b) * 4 + r;
      st4(v4u{fu(s.x), tag, fu(s.y), tag}, ws, rec_voff, W_HA3D + rec * 1024);
      if (lane < 8) st4(v4u{fu(M), tag, fu(L), tag}, ws, lane * 16, W_HA3S + rec * 128);
      __builtin_amdgcn_s_sleep(D_A3 - 3);
    } else {
      __builtin_amdgcn_s_sleep(D_A3);
    }
  } else {
    constexpr int L_Q = 0, L_P = 32768, L_MX = 40960, L_LS = 41472, L_LAT = 45056, L_ST = 49152, L_R4 = 53248, L_CNT = L_FLAG + 16;
    const int b = id >> 6;
    const u32 ep = __builtin_amdgcn_raw_buffer_load_b32(ws, 0, W_EPOCH, AUX_SC1);
    bar();
    v4u fr[32], qg[8], wv[4];
    const int r32 = lane & 31, hh = lane >> 5;
    const int row_base = r32 * 1024 + 16 * (hh ^ (((r32 & 3) << 2) | ((r32 >> 2) & 3)));
    const int qso = W_H2 + b * 65536 + wave * 8192;
    auto load_q = [&]() {
#pragma unroll
      for (int i = 0; i < 8; ++i) { qg[i] = ld4<AUX_SC1>(ws, lane * 16 + (i & 3) * 1024, qso + (i >> 2) * 4096); pin(); }
    };
    if (wave < 4) {
      const rsrc_t rc = mk((const char*)P.ckv + (size_t)id * 131072 + wave * 8192, 131072 - wave * 8192);
      const int xi = hsh & 4, lx = lane ^ ((2 * wave + (xi >> 2)) & 3);
      if (D_Y0) __builtin_amdgcn_s_sleep(D_Y0);
#pragma unroll
      for (int i = 0; i < 32; ++i) {
        const int ro = (i >> 3) * 32768 + ((i & 7) ^ xi) * 1024;
        __builtin_amdgcn_raw_ptr_buffer_load_lds(rc, (LAS void*)(smem + wave * 8192 + ro), 16, 16 * (lx ^ (((i & 3) << 2) | ((i >> 2) & 1))), ro, 0, AUX_NT);
        pin();
        if (i >= WY - 1 && i < NWY) asm volatile("s_waitcnt vmcnt(%0)" ::"i"(WY - 1) : "memory");
        if (i == 15 || i == 23 || i == 31) {
          asm volatile("s_waitcnt vmcnt(8)" ::: "memory");
          if (lane == 0) asm volatile("ds_add_u32 %0, %1" ::"v"(L_FLAG + 4 * ((i >> 3) - 1)), "v"(1u) : "memory");
          pin();
        }
      }
      if (wave < 3) {
        spin_flag_asm(L_FLAG + 4 * wave, 4);
#pragma unroll
        for (int t = 0; t < 32; ++t)
          asm volatile("ds_read_b128 %0, %1 offset:%2" : "=v"(fr[t]) : "v"(wave * 32768 + (row_base ^ (32 * (t & 7)))), "i"(256 * (t >> 3)) : "memory");
      }
      pin();
      __builtin_amdgcn_s_waitcnt(0x0070);
      pin();
      if (lane == 0) asm volatile("ds_add_u32 %0, %1" ::"v"(L_FLAG + 12), "v"(1u) : "memory");
      ST(1);
      load_q();
      if (wave == 3) {
        spin_flag(smem + L_FLAG + 12, 4);
#pragma unroll
        for (int t = 0; t < 32; ++t) fr[t] = *(const v4u*)(smem + 3 * 32768 + ((row_base ^ (32 * (t & 7))) + 256 * (t >> 3)));
      }
    } else {
      const int q = c16 >> 2, p = lane & 3;
      const int base = (4 * hh + q) * 1024 + (wave - 4) * 256 + 16 * ((2 * (g & 1) + (p >> 1)) ^ (4 * q + hh)) + 8 * (p & 1);
#pragma unroll
      for (int kb = 0; kb < 4; ++kb) {
        spin_flag(smem + L_FLAG + 4 * kb, 4);
        if (kb == GKB) load_q();
#pragma unroll
        for (int ta = 0; ta < 4; ++ta)
#pragma unroll
          for (int s = 0; s < 2; ++s) {
            const char* pa = smem + kb * 32768 + s * 16384;
            const v2u a = __builtin_bit_cast(v2u, __builtin_amdgcn_ds_read_tr16_b64_v4i16((LAS s4v*)(pa + (base ^ (64 * ta)))));
            const v2u c = __builtin_bit_cast(v2u, __builtin_amdgcn_ds_read_tr16_b64_v4i16((LAS s4v*)(pa + (base ^ (64 * ta) ^ 32) + 8192)));
            fr[ta * 8 + 2 * kb + s] = v4u{a.x, a.y, c.x, c.y};
          }
        pin();
      }
    }
    asm volatile("" ::"v"(pfc));
    tag = __builtin_amdgcn_readfirstlane(ep) + 0x9E3779B9u;
    bar();
    ST(2);
    _Pragma("nounroll") for (int spin = 0; spin < SPIN_CAP; ++spin) {
      u32 bad = 0;
#pragma unroll
      for (int i = 0; i < 8; ++i) bad = bad4(bad, qg[i], tag);
      if (none(bad)) break;
      pin();
      __builtin_amdgcn_s_sleep(4);
      load_q();
    }
    pin();
    {
      const rsrc_t rw = mk((const char*)P.wuv + (r * 128 + wave * 16) * 8192 + h * 256, 15 * 8192 + 256);
#pragma unroll
      for (int i = 0; i < 4; ++i) { wv[i] = ld4<AUX_NT>(rw, g * 32768 + c16 * 16, i * 8192); pin(); }
    }
#pragma unroll
    for (int i = 0; i < 8; ++i) {
      const int fh = ((i >> 1) << 2) | (wave & 3);
      *(v2u*)(smem + L_Q + (4 * wave + (i >> 1)) * 1024 + (i & 1) * 512 + 16 * ((lane >> 1) ^ fh) + 8 * (lane & 1)) = v2u{qg[i].x, qg[i].z};
    }
    bar();
    ST(3);
    f16v s;
    if (wave < 4) {
      v4u qb[4];
#define QRD(t) (*(const v4u*)(smem + L_Q + ((row_base ^ (32 * ((t) & 7))) + 256 * ((t) >> 3))))
#pragma unroll
      for (int t = 0; t < 4; ++t) qb[t] = QRD(t);
#pragma unroll
      for (int i = 0; i < 16; ++i) s[i] = 0.f;
      pin();
#pragma unroll
      for (int t = 0; t < 32; ++t) {
        s = mfma32(fr[t], qb[t & 3], s);
        __builtin_amdgcn_sched_barrier(0);
        if (t + 4 < 32) qb[t & 3] = QRD(t + 4);
        __builtin_amdgcn_sched_barrier(0);
      }
      float m = s[0];
#pragma unroll
      for (int i = 1; i < 16; ++i) m = fmaxf(m, s[i]);
      m = swap32_max(m);
      if (lane < 32) *(float*)(smem + L_MX + lane * 16 + wave * 4) = m;
    }
    bar();
    if (wave < 4) {
      const f4 mm = *(const f4*)(smem + L_MX + r32 * 16);
      const float m = fmaxf(fmaxf(mm[0], mm[1]), fmaxf(mm[2], mm[3])) * SC;
      float l = 0.f;
      u32 pp[8];
#pragma unroll
      for (int i = 0; i < 8; ++i) {
        const float p0 = ex2(fmaf(s[2 * i], SC, -m)), p1 = ex2(fmaf(s[2 * i + 1], SC, -m));
        l += p0 + p1;
        pp[i] = pk(p0, p1);
      }
      *(v4u*)(smem + L_P + wave * 2048 + lane * 16) = v4u{pp[0], pp[1], pp[2], pp[3]};
      *(v4u*)(smem + L_P + wave * 2048 + 1024 + lane * 16) = v4u{pp[4], pp[5], pp[6], pp[7]};
      l = swap32_add(l, l);
      if (lane < 32) *(float*)(smem + L_LS + lane * 16 + wave * 4) = l;
    }
    bar();
    ST(4);
    if (wave >= 4) {
      v4u pf[8];
#pragma unroll
      for (int ks = 0; ks < 8; ++ks) pf[ks] = *(const v4u*)(smem + L_P + ks * 1024 + lane * 16);
      f16v o0, o1, o2, o3;
#pragma unroll
      for (int i = 0; i < 16; ++i) { o0[i] = 0.f; o1[i] = 0.f; o2[i] = 0.f; o3[i] = 0.f; }
      const int voff = hh * 524288 + r32 * 4, soff = W_H3D + (wave - 4) * 32768 + id * 256;
      pin();
#pragma unroll
      for (int ks = 0; ks < 8; ++ks) { o0 = mfma32(pf[ks], fr[ks], o0); o1 = mfma32(pf[ks], fr[8 + ks], o1); }
      __builtin_amdgcn_sched_barrier(0);
#define OST(a, c, i, pi) __builtin_amdgcn_raw_buffer_store_b32(pk(a[i], c[i]), ws, voff + (pi) * 128, soff + (((i) & 3) + 8 * ((i) >> 2)) * 131072, AUX_SC1)
#pragma unroll
      for (int ks = 0; ks < 8; ++ks) {
        o2 = mfma32(pf[ks], fr[16 + ks], o2);
        __builtin_amdgcn_sched_barrier(0);
        if (ks) OST(o0, o1, 2 * ks - 2, 0);
        __builtin_amdgcn_sched_barrier(0);
        o3 = mfma32(pf[ks], fr[24 + ks], o3);
        __builtin_amdgcn_sched_barrier(0);
        if (ks) OST(o0, o1, 2 * ks - 1, 0);
        __builtin_amdgcn_sched_barrier(0);
      }
      OST(o0, o1, 14, 0); OST(o0, o1, 15, 0);
#pragma unroll
      for (int i = 0; i < 16; ++i) OST(o2, o3, i, 1);
    }
    bar();
    ST(5);
    if (wave >= 4) {
      __builtin_amdgcn_s_setprio(3);
      asm volatile("s_waitcnt vmcnt(0)" ::: "memory");
      if (arrive(smem + L_CNT, lane) == 3) {
        const int hd = (lane >> 3) * 4 + (lane & 3);
        const f4 mm = *(const f4*)(smem + L_MX + hd * 16), ll = *(const f4*)(smem + L_LS + hd * 16);
        st4(v4u{fu(fmaxf(fmaxf(mm[0], mm[1]), fmaxf(mm[2], mm[3])) * SC), tag, fu((ll[0] + ll[1]) + (ll[2] + ll[3])), tag}, ws,
            (lane >> 3) * 16384 + (lane & 7) * 16, W_H3S + id * 128);
      }
      __builtin_amdgcn_s_setprio(0);
      ST(6);
    }
    pin();
    load_wo();
    if (wave < 4) __builtin_amdgcn_s_sleep(D_H3 + 14); else __builtin_amdgcn_s_sleep(D_H3);
    {
      v4u st;
      _Pragma("nounroll") for (int spin = 0; spin < SPIN_CAP; ++spin) {
        pin();
        st = ld4<AUX_SC1>(ws, (lane >> 2) * 128, W_H3S + x * 16384 + wave * 2048 + (h & 3) * 16);
        if (none(bad4(0u, st, tag))) break;
        __builtin_amdgcn_s_sleep(3);
      }
      pin();
      ST(7);
      v4u d[4];
#pragma unroll
      for (int i = 0; i < 4; ++i) d[i] = ld4<AUX_SC1>(ws, g * 1024 + c16 * 16 + i * 256, W_H3D + id * 32768 + wave * 4096);
      pin();
      float M = uf(st.x);
      M = fmaxf(M, dppf<ROR4>(M)); M = fmaxf(M, dppf<ROR8>(M)); M = swap32_max(swap16_max(M));
      const float wg = ex2(uf(st.x) - M);
      float L = wg * uf(st.z);
      L += dppf<ROR4>(L); L += dppf<ROR8>(L); L = swap16_add(L, L); L = swap32_add(L, L);
      if (lane == 0) *(f2*)(smem + L_ST + wave * 8) = f2{M, L};
      const float wi[4] = {dppf<BCAST>(wg), dppf<BCAST + 4>(wg), dppf<BCAST + 8>(wg), dppf<BCAST + 12>(wg)};
      float a[8];
#pragma unroll
      for (int k = 0; k < 4; ++k) {
        a[2 * k] = wi[0] * blo(d[0][k]); a[2 * k + 1] = wi[0] * bhi(d[0][k]);
#pragma unroll
        for (int i = 1; i < 4; ++i) { a[2 * k] = fmaf(wi[i], blo(d[i][k]), a[2 * k]); a[2 * k + 1] = fmaf(wi[i], bhi(d[i][k]), a[2 * k + 1]); }
      }
      const float lo = swap16_add(swap32_add(a[0], a[4]), swap32_add(a[2], a[6]));
      const float hi = swap16_add(swap32_add(a[1], a[5]), swap32_add(a[3], a[7]));
      float* lat = (float*)(smem + L_LAT + wave * 512) + 64 * bsel + 4 * (c16 & 7) + g;
      lat[0] = lo; lat[32] = hi;
    }
    bar();
    {
      const f4 s0 = *(const f4*)(smem + L_ST + bsel * 32), s1 = *(const f4*)(smem + L_ST + bsel * 32 + 16);
      const float M = fmaxf(fmaxf(s0[0], s0[2]), fmaxf(s1[0], s1[2]));
      const float f[4] = {ex2(s0[0] - M), ex2(s0[2] - M), ex2(s1[0] - M), ex2(s1[2] - M)};
      const float inv = __builtin_amdgcn_rcpf(fmaf(f[0], s0[1], fmaf(f[1], s0[3], fmaf(f[2], s1[1], f[3] * s1[3]))));
      f4 c = {0.f, 0.f, 0.f, 0.f};
#pragma unroll
      for (int w = 0; w < 4; ++w) c += f[w] * *(const f4*)(smem + L_LAT + (bsel * 4 + w) * 512 + wave * 64 + g * 16);
      c *= inv;
      *(f4*)(smem + L_R4 + wave * 1024 + lane * 16) = rows16(pk(c[0], c[1]), pk(c[2], c[3]), wv, sel0, sel1);
    }
    bar();
    if (wave < 2) {
      const f2 sv = sum8(smem + L_R4 + tid * 8);
      st4(v4u{fu(sv.x), tag, fu(sv.y), tag}, ws, rec_voff, W_H4 + ((h * 2 + wave) * 4 + r) * 1024);
      __builtin_amdgcn_s_sleep(D_H4 - 2);
    } else {
      __builtin_amdgcn_s_sleep(D_H4);
    }
  }
  ST(8);
  {
    v4u d0, d1, st = {0u, tag, 0x3e800000u, tag};
    const int dv = (bsel * 4 + (c16 & 3)) * 1024 + g * 32, dso = (isX ? W_HA3D : W_H4) + h * 8192 + wave * 128;
    _Pragma("nounroll") for (int spin = 0; spin < SPIN_CAP; ++spin) {
      pin();
      d0 = ld4<AUX_SC1>(ws, dv, dso);
      d1 = ld4<AUX_SC1>(ws, dv + 16, dso);
      if (isX) st = ld4<AUX_SC1>(ws, (bsel * 4 + (c16 & 3)) * 128, W_HA3S + h * 1024);
      if (none(bad4(bad4(bad4(0u, d0, tag), d1, tag), st, tag))) break;
      __builtin_amdgcn_s_sleep(2);
    }
    ST(9);
    float M = uf(st.x);
    M = fmaxf(M, dppf<XOR1>(M)); M = fmaxf(M, dppf<XOR2>(M));
    float w = ex2(uf(st.x) - M);
    w *= __builtin_amdgcn_rcpf(quad_sum(w * uf(st.z)));
    const u32 c01 = pk(quad_sum(w * uf(d0.x)), quad_sum(w * uf(d0.z))), c23 = pk(quad_sum(w * uf(d1.x)), quad_sum(w * uf(d1.z)));
    *(f4*)(smem + L_RED + wave * 1024 + lane * 16) = rows16(c01, c23, wo, sel0, sel1);
    if (r == 3) *(f4*)(smem + L_REDE + wave * 1024 + lane * 16) = rows16(c01, c23, we, sel0, sel1);
  }
  bar();
  const int part = isX ? 0 : 1;
  if (wave < 4) {
    if (wave < 2 || (r == 3 && (lane & 16) == 0)) {
      const f2 sv = sum8(smem + L_RED + (wave >> 1) * 8192 + (tid & 127) * 8);
      st4(v4u{fu(sv.x), tag, fu(sv.y), tag}, ws, (lane >> 3) * 4096 + (lane & 7) * 16,
          W_H5 + (part * 80 + (wave < 2 ? r * 16 : 64) + (wave & 1) * 8) * 4096 + h * 128);
    }
    ST(10);
  } else if (wave == 4 && id < 72) {
    const int e = id - 64, L = id < 64 ? id : 64 + (e & 1) + 4 * (e >> 1);
    v4u v[4];
    __builtin_amdgcn_s_sleep(D_H5);
    _Pragma("nounroll") for (int spin = 0; spin < SPIN_CAP; ++spin) {
      pin();
      u32 bad = 0;
#pragma unroll
      for (int i = 0; i < 4; ++i) v[i] = ld4<AUX_SC1>(ws, lane * 16 + i * 1024, W_H5 + (part * 80 + L) * 4096);
#pragma unroll
      for (int i = 0; i < 4; ++i) bad = bad4(bad, v[i], tag);
      if (none(bad)) break;
      __builtin_amdgcn_s_sleep(2);
    }
    float a = (uf(v[0].x) + uf(v[1].x)) + (uf(v[2].x) + uf(v[3].x)), c = (uf(v[0].z) + uf(v[1].z)) + (uf(v[2].z) + uf(v[3].z));
    a += dppf<ROR8>(a); c += dppf<ROR8>(c);
    a = swap16_add(a, a); c = swap16_add(c, c);
    a = swap32_add(a, a); c = swap32_add(c, c);
    if (lane < 8) {
      const int al = L & 15;
      const int n = (al >> 3) * 1152 + part * 576 + 128 * (L >> 4) + 32 * (al & 3) + 8 * (lane >> 1) + 4 * ((al >> 2) & 1) + 2 * (lane & 1);
      ((u32*)P.out)[n >> 1] = pk(a, c);
    }
    if (!isX && id == 0 && lane == 0) __builtin_amdgcn_raw_buffer_store_b32(tag, ws, 0, W_EPOCH, AUX_SC1);
  }
  ST(11);
  if (lane == 0) {
    u64* sp = (u64*)((char*)P.ws + W_STAMP) + (bid * 8 + wave) * 12;
#pragma unroll
    for (int i = 0; i < 12; ++i) __hip_atomic_store(sp + i, ts[i], __ATOMIC_RELAXED, __HIP_MEMORY_SCOPE_AGENT);
  }
  if (bid == 0 && wave == 7) {
    while (__builtin_amdgcn_s_memrealtime() - ts[0] < 7000) __builtin_amdgcn_s_sleep(8);
    u64* sp = (u64*)((char*)P.ws + W_STAMP);
    u64 mn = ~0ull;
    for (int e = lane; e < 2048; e += 64) { u64 v = __hip_atomic_load(sp + e * 12, __ATOMIC_RELAXED, __HIP_MEMORY_SCOPE_AGENT); mn = v < mn ? v : mn; }
    for (int o = 32; o; o >>= 1) { u64 t = __shfl_xor(mn, o); mn = t < mn ? t : mn; }
    float sum = 0.f, cnt = 0.f, mx = 0.f;
    for (int e = lane; e < 2048; e += 64) {
      u64 v = __hip_atomic_load(sp + e * 12 + REP_IDX, __ATOMIC_RELAXED, __HIP_MEMORY_SCOPE_AGENT);
      const int role = (e >> 6) >= 16;
      if (v && (REP_ROLE == 2 || role == REP_ROLE)) { float d = (float)(v - mn); sum += d; cnt += 1.f; mx = fmaxf(mx, d); }
    }
    for (int o = 32; o; o >>= 1) { sum += __shfl_xor(sum, o); cnt += __shfl_xor(cnt, o); mx = fmaxf(mx, __shfl_xor(mx, o)); }
    float q = REP_STAT == 0 ? 0.f : REP_STAT == 1 ? sum / cnt : mx;
    q = fminf(q, 6000.f);
    const u64 t00 = __hip_atomic_load((u64*)((char*)P.ws + W_STAMP + (1 << 20)), __ATOMIC_RELAXED, __HIP_MEMORY_SCOPE_AGENT);
    const u64 until = t00 + 10000 + (PREK == 2 ? 4 * (mn - t00) : 0);
    while (__builtin_amdgcn_s_memrealtime() < until) __builtin_amdgcn_s_sleep(1);
  }
}

__global__ void tinyk(u64* p) { if (threadIdx.x == 0 && blockIdx.x == 0) *p = __builtin_amdgcn_s_memrealtime(); }
__global__ void postk(const u64* p) { if (blockIdx.x == 0) { const u64 t = *p; while (__builtin_amdgcn_s_memrealtime() < t + 15000) __builtin_amdgcn_s_sleep(1); } }
extern "C" void kernel_launch(void* const* d_in, const int* in_sizes, int n_in, void* d_out, int out_size, void* d_ws, size_t ws_size,
                              hipStream_t stream) {
  Params p;
  p.xA = d_in[0]; p.kc = d_in[1]; p.vc = d_in[2]; p.wqA = d_in[3]; p.woA = d_in[4];
  p.xB = d_in[5]; p.ckv = d_in[6]; p.wqB = d_in[7]; p.wuk = d_in[8]; p.wuv = d_in[9]; p.woB = d_in[10];
  p.out = d_out; p.ws = (char*)d_ws + (WS_AT_END ? ((ws_size - (18 << 20)) & ~(size_t)0x1fffff) : 0);
  tinyk<<<PREG, 64, 0, stream>>>((u64*)((char*)p.ws + W_STAMP + (1 << 20)));
  fused_decode<<<256, 512, 0, stream>>>(p);
}
```

```cpp
#include <hip/hip_runtime.h>
#include <hip/hip_bf16.h>
#include <stdint.h>
#include <type_traits>

typedef unsigned u32;
typedef unsigned long long u64;
typedef u32 v4u __attribute__((ext_vector_type(4)));
typedef u32 v2u __attribute__((ext_vector_type(2)));
typedef float f2 __attribute__((ext_vector_type(2)));
typedef float f4 __attribute__((ext_vector_type(4)));
typedef float f16v __attribute__((ext_vector_type(16)));
typedef __bf16 bf8 __attribute__((ext_vector_type(8)));
typedef __bf16 bf2 __attribute__((ext_vector_type(2)));
typedef short s4v __attribute__((ext_vector_type(4)));
typedef __amdgpu_buffer_rsrc_t rsrc_t;
#define LAS __attribute__((address_space(3)))
#define DI __device__ __forceinline__

constexpr int AUX_NT = 2, AUX_SC1 = 16;
constexpr int W_EPOCH = 0;
constexpr int SLOT = 16896, W_SLOT = 4096;
constexpr int S_H1 = W_SLOT;
constexpr int S_HD = W_SLOT + 2048;
constexpr int S_H5 = W_SLOT + 4096;
constexpr int S_HS = W_SLOT + 8192;
constexpr int S_H2 = W_SLOT + 9216;
constexpr int S_H1A = W_SLOT + 10240;
constexpr int W_H3D = 5 << 20;
constexpr int W_END = W_H3D + 4194304;
constexpr int W_STAMP = 16 << 20;
constexpr float SC = 0.08838834764831845f * 1.4426950408889634f;
#define KNOB(n, v) constexpr int n = v
#define K_D_H1 14
#define K_WX 2
#define K_NX 12
#define K_WY 2
#define K_NY 0
#define K_DXA 20
#define K_P_H3 6
#define P_HD 2
#define K_D_A3 12
#define K_D_H3 30
#define K_D_H4 14
#define K_D_H5 14
#define EARLYQ 1
#define K_D_H2 0
#define WS_AT_END 0
#define SPREAD 1
#define WE_EARLY 0
#define KSYNC 1
#define WSYNC 1
#define T_GO 0
#define T_A1 0
#define T_Y0 0
#define D_H1A 16
#define WO_LATE 3
#define K_DX0 0
#define ABL 0
#define CODE_BYTES 18176
constexpr int D_H1 = K_D_H1, WX = K_WX, NX = K_NX, WY = K_WY, NY = K_NY, DXA = K_DXA, P_H3 = K_P_H3, D_A3 = K_D_A3, D_H3 = K_D_H3, D_H4 = K_D_H4,
              D_H5 = K_D_H5, D_H2 = K_D_H2;
constexpr int SPIN_CAP = 1 << 17;

struct Params {
  const void *xB, *wqB, *wuk, *ckv;
  void *ws, *out;
  const void *xA, *wqA, *kc, *vc, *woA, *woB, *wuv;
};

DI rsrc_t mk(const void* p, u32 bytes) { return __builtin_amdgcn_make_buffer_rsrc((void*)p, 0, bytes, 0x00020000); }
template <int AUX> DI v4u ld4(rsrc_t r, int voff, int soff) { return __builtin_amdgcn_raw_buffer_load_b128(r, voff, soff, AUX); }
template <int AUX> DI v2u ld2(rsrc_t r, int voff, int soff) { return __builtin_amdgcn_raw_buffer_load_b64(r, voff, soff, AUX); }
DI void st4(v4u v, rsrc_t r, int voff, int soff) { __builtin_amdgcn_raw_buffer_store_b128(v, r, voff + soff, 0, AUX_SC1); }
DI u32 fu(float f) { return __builtin_bit_cast(u32, f); }
DI float uf(u32 u) { return __builtin_bit_cast(float, u); }
DI u32 pk(float lo, float hi) { f2 f = {lo, hi}; return __builtin_bit_cast(u32, __builtin_convertvector(f, bf2)); }
DI float blo(u32 u) { return uf(u << 16); }
DI float bhi(u32 u) { return uf(u & 0xffff0000u); }
template <int C> DI u32 dppu(u32 x) { return __builtin_amdgcn_mov_dpp(x, C, 0xf, 0xf, true); }
template <int C> DI float dppf(float x) { return uf(dppu<C>(fu(x))); }
constexpr int XOR1 = 0xB1, XOR2 = 0x4E, HMIR = 0x141, ROR4 = 0x124, ROR8 = 0x128, BCAST = 0x150;
DI f4 mfma16(v4u a, v4u b, f4 c) { return __builtin_amdgcn_mfma_f32_16x16x32_bf16(__builtin_bit_cast(bf8, a), __builtin_bit_cast(bf8, b), c, 0, 0, 0); }
DI f16v mfma32(v4u a, v4u b, f16v c) { return __builtin_amdgcn_mfma_f32_32x32x16_bf16(__builtin_bit_cast(bf8, a), __builtin_bit_cast(bf8, b), c, 0, 0, 0); }
DI float mx(float a, float b) { return __builtin_elementwise_maximum(a, b); }
DI float mx(float a, float b, float c) { return mx(mx(a, b), c); }
DI float mx4(f4 v) { return mx(mx(v[0], v[1], v[2]), v[3]); }
DI float ex2(float x) { return __builtin_amdgcn_exp2f(x); }
DI float swap32_add(float a, float b) { auto s = __builtin_amdgcn_permlane32_swap(fu(a), fu(b), false, false); return uf(s[0]) + uf(s[1]); }
DI float swap16_add(float a, float b) { auto s = __builtin_amdgcn_permlane16_swap(fu(a), fu(b), false, false); return uf(s[0]) + uf(s[1]); }
DI float swap32_max(float a) { auto s = __builtin_amdgcn_permlane32_swap(fu(a), fu(a), false, false); return mx(uf(s[0]), uf(s[1])); }
DI float swap16_max(float a) { auto s = __builtin_amdgcn_permlane16_swap(fu(a), fu(a), false, false); return mx(uf(s[0]), uf(s[1])); }
DI void bar() {
  __builtin_amdgcn_sched_barrier(0);
  asm volatile("s_waitcnt lgkmcnt(0)\n\ts_barrier" ::: "memory");
  __builtin_amdgcn_sched_barrier(0);
}
DI void pin() { asm volatile("" ::: "memory"); __builtin_amdgcn_sched_barrier(0); }
DI u32 bad4(u32 acc, v4u v, u32 tag) {
  return __builtin_amdgcn_bitop3_b32(__builtin_amdgcn_bitop3_b32(acc, v.y, tag, 0xF6), v.w, tag, 0xF6);
}
DI bool none(u32 bad) { return __builtin_amdgcn_ballot_w64(bad != 0) == 0; }
DI v4u diag(u32 src, const u32 (&sel)[4]) {
  v4u a;
#pragma unroll
  for (int k = 0; k < 4; ++k) a[k] = __builtin_amdgcn_perm(src, src, sel[k]);
  return a;
}

#define ST(i) do {} while (0)

DI void spin_flag(const char* p, u32 want = 1) {
  _Pragma("nounroll") for (int spin = 0; spin < SPIN_CAP * 8; ++spin) {
    if ((u32)__builtin_amdgcn_readfirstlane(*(volatile const LAS u32*)p) >= want) break;
    __builtin_amdgcn_s_sleep(1);
  }
  pin();
}
DI u32 arrive(u32 addr, int lane) {
  u32 old = 0;
  if (lane == 0) asm volatile("ds_add_rtn_u32 %0, %1, %2\n\ts_waitcnt lgkmcnt(0)" : "=v"(old) : "v"(addr), "v"(1u) : "memory");
  old = __builtin_amdgcn_readfirstlane(old);
  pin();
  return old;
}
DI void post(u32 addr, int lane) {
  if (lane == 0) asm volatile("ds_add_u32 %0, %1" ::"v"(addr), "v"(1u) : "memory");
  pin();
}
DI void spin_flag_asm(u32 addr, u32 want) {
  _Pragma("nounroll") for (int spin = 0; spin < SPIN_CAP * 8; ++spin) {
    u32 v;
    asm volatile("ds_read_b32 %0, %1\n\ts_waitcnt lgkmcnt(0)" : "=v"(v) : "v"(addr) : "memory");
    if ((u32)__builtin_amdgcn_readfirstlane(v) >= want) break;
    __builtin_amdgcn_s_sleep(1);
  }
  pin();
}
DI float quad_sum(float v) { v += dppf<XOR1>(v); return v + dppf<XOR2>(v); }
DI f2 sum8(const char* p) {
  f2 s = *(const f2*)p;
#pragma unroll
  for (int w = 1; w < 8; ++w) s += *(const f2*)(p + w * 1024);
  return s;
}
DI f4 rows16(u32 c01, u32 c23, const v4u (&w)[4], const u32 (&sel0)[4], const u32 (&sel1)[4]) {
  f4 acc = {0.f, 0.f, 0.f, 0.f};
  acc = mfma16(diag(c01, sel0), w[0], acc); acc = mfma16(diag(c01, sel1), w[1], acc);
  acc = mfma16(diag(c23, sel0), w[2], acc); acc = mfma16(diag(c23, sel1), w[3], acc);
  return acc;
}

__global__ void __launch_bounds__(512, 2) fused_decode(Params P) {
  constexpr int L_FLAG = 131072;
  __shared__ __attribute__((aligned(16))) char smem[L_FLAG + 64 + 4096];
  ST(0);
  const u32 t_in = (u32)__builtin_amdgcn_s_memrealtime();
  auto pace = [&](u32 t) { while ((u32)__builtin_amdgcn_s_memrealtime() - t_in < t) __builtin_amdgcn_s_sleep(1); pin(); };
  const int tid = threadIdx.x, lane = tid & 63, wave = __builtin_amdgcn_readfirstlane(tid >> 6);
  const int bid = blockIdx.x, x = bid & 7, jj = bid >> 3, j = jj & 15;
  const bool isX = jj < 16;
  const int g = lane >> 4, c16 = lane & 15, bsel = c16 >> 3;
  const int h = 4 * x + (j >> 2), r = j & 3, id = x * 16 + j;
  u32 pfc = 0;
  if (tid < 16) {
    u64 pc;
    asm volatile("s_getpc_b64 %0" : "=s"(pc));
    pfc = __builtin_amdgcn_raw_buffer_load_b32(mk((const void*)(pc & ~255ull), CODE_BYTES), tid * 64, jj * 1024, 0);
  }
  if (tid < 16) *(u32*)(smem + L_FLAG + tid * 4) = 0u;
  asm volatile("" ::"s"(P.xB), "s"(P.wqB), "s"(P.wuk), "s"(P.ckv), "s"(P.ws), "s"(P.out), "s"(P.xA), "s"(P.wqA), "s"(P.kc), "s"(P.vc), "s"(P.woA), "s"(P.woB), "s"(P.wuv));
  const int w4 = wave & 3;
  const int kq_voff = (c16 & 1) * 64 + g * 16;
  const int gk = 2 * x + (j >> 3), bx = (j >> 2) & 1;
  const int ro = isX ? 0 : 128 * SLOT;
  const rsrc_t ws = mk(P.ws, W_END);
  u32 tag, sel0[4], sel1[4];
  auto make_sel = [&]() {
#pragma unroll
    for (int k = 0; k < 4; ++k) {
      const bool on = ((c16 & 7) >> 1) == k;
      sel0[k] = on ? ((c16 & 1) ? 0x01000c0cu : 0x0c0c0100u) : 0x0c0c0c0cu;
      sel1[k] = on ? ((c16 & 1) ? 0x03020c0cu : 0x0c0c0302u) : 0x0c0c0c0cu;
    }
  };
  constexpr int L_QV = 1024, L_R1 = L_FLAG + 64;
  auto gather_q = [&](int gs, int f) {
    const int gv = (lane >> 5) * SLOT + (w4 & 1) * 1024 + (lane & 31) * 16;
    v4u a, c;
    _Pragma("nounroll") for (int spin = 0; spin < SPIN_CAP; ++spin) {
      pin();
      a = ld4<AUX_SC1>(ws, gv, gs); c = ld4<AUX_SC1>(ws, gv, gs + 2 * SLOT);
      if (none(bad4(bad4(0u, a, tag), c, tag))) break;
      __builtin_amdgcn_s_sleep(2);
    }
    const float s0 = uf(a.x) + uf(c.x), s1 = uf(a.z) + uf(c.z);
    const u32 pq = pk(swap32_add(s0, s0), swap32_add(s1, s1));
    if (lane < 32) *(u32*)(smem + L_QV + f * 512 + (w4 >> 1) * 256 + 16 * c16 + 8 * g + 4 * (w4 & 1)) = pq;
    post(L_FLAG + 12 + 4 * f, lane);
  };
  if (__builtin_expect(wave >= 4, 1)) {
    __builtin_amdgcn_s_setprio(2);
    if (T_Y0 && !isX) pace(T_Y0);
    v4u w1[9];
    const rsrc_t rx = mk(isX ? P.xB : P.xA, 2304);
    const rsrc_t rw = mk((const char*)(isX ? P.wqB : P.wqA) + (size_t)(r * 144 + w4 * 36) * 8192 + h * 256, 35 * 8192 + 256);
    const int xv = bsel * 1152 + r * 288 + w4 * 72;
    const v4u xc = ld4<0>(rx, xv + g * 16, 0);
    const u32 xe = __builtin_amdgcn_raw_buffer_load_b16(rx, xv + 64 + g * 2, 0, 0);
    pin();
#pragma unroll
    for (int i = 0; i < 8; ++i) { w1[i] = ld4<AUX_NT>(rw, g * 65536 + c16 * 16, i * 8192); pin(); }
    w1[8] = ld4<AUX_NT>(rw, (32 + g) * 8192 + c16 * 16, 0);
    const u32 ep = __builtin_amdgcn_raw_buffer_load_b32(ws, 0, W_EPOCH, AUX_SC1);
    bar();
    make_sel();
    pin();
    f4 acc = {0.f, 0.f, 0.f, 0.f};
#pragma unroll
    for (int i = 0; i < 4; ++i) { acc = mfma16(diag(xc[i], sel0), w1[2 * i], acc); acc = mfma16(diag(xc[i], sel1), w1[2 * i + 1], acc); }
    acc = mfma16(diag(xe, sel0), w1[8], acc);
    char* rp = smem + L_R1 + lane * 16;
    *(f4*)(rp + w4 * 1024) = acc;
    pin();
    tag = (u32)__builtin_amdgcn_readfirstlane(ep) + 0x9E3779B9u;
    if (arrive(L_FLAG + 48, lane) == 3) {
      acc = (*(const f4*)rp + *(const f4*)(rp + 1024)) + (*(const f4*)(rp + 2048) + *(const f4*)(rp + 3072));
      st4(v4u{fu(acc[0]), tag, fu(acc[1]), tag}, ws, lane * 16, S_H1 + ro + id * SLOT);
      st4(v4u{fu(acc[2]), tag, fu(acc[3]), tag}, ws, lane * 16 + 1024, S_H1 + ro + id * SLOT);
      *(u32*)(smem + L_FLAG + 28) = tag;
      pin();
      *(volatile LAS u32*)(smem + L_FLAG + 52) = 1;
      if (!isX) ST(10);
    }
  }
  if (__builtin_expect(isX, 1)) {
    if (__builtin_expect(wave >= 4, 1)) {
      spin_flag(smem + L_FLAG + 52);
      ST(1);
      __builtin_amdgcn_s_sleep(D_H1);
      _Pragma("nounroll") for (int f = 0; f < 2; ++f) {
        gather_q(S_H1 + (f ? (128 + (2 * gk + (w4 >> 1)) * 4) * SLOT + bx * 512 : h * 4 * SLOT + (w4 >> 1) * 512), f);
        if (f == 0) { ST(2); if (T_A1) pace(T_A1); }
      }
      ST(3);
    } else {
      bar();
      __builtin_amdgcn_s_setprio(3);
      v4u w2[8];
      const rsrc_t ru = mk((const char*)P.wuk + (r * 128 + w4 * 32) * 8192 + h * 256, 31 * 8192 + 256);
      const int voff = (4 * (c16 >> 2) + ((c16 >> 1) & 1)) * 8192 + kq_voff;
#pragma unroll
      for (int i = 0; i < 8; ++i) { w2[i] = ld4<AUX_NT>(ru, voff + (i & 1) * 128, ((i >> 1) & 1) * 16384 + (i >> 2) * 131072); pin(); }
      make_sel();
      spin_flag(smem + L_FLAG + 12, 4);
      tag = __builtin_amdgcn_readfirstlane(*(const u32*)(smem + L_FLAG + 28));
      const v4u q0 = *(const v4u*)(smem + L_QV + bsel * 256 + kq_voff), q1 = *(const v4u*)(smem + L_QV + bsel * 256 + 128 + kq_voff);
      u32 o[4];
#pragma unroll
      for (int be = 0; be < 4; ++be) {
        f4 d = {0.f, 0.f, 0.f, 0.f};
        d = mfma16(w2[2 * be], q0, d); d = mfma16(w2[2 * be + 1], q1, d);
        o[be] = pk(d[0] + dppf<XOR1>(d[1]), d[2] + dppf<XOR1>(d[3]));
      }
      const bool up = c16 & 2;
      if ((c16 & 5) == 0) st4(v4u{up ? o[2] : o[0], tag, up ? o[3] : o[1], tag}, ws, bsel * 512 + (c16 & 2) * 32 + g * 16, S_H2 + id * SLOT + w4 * 128);
      ST(3);
    }
    if (T_GO) pace(T_GO);
  } else if (wave < 4) {
    bar();
    make_sel();
  }
  __builtin_amdgcn_s_setprio(1);
  pin();
  const int hsh = (bid * 2654435761u) >> 20;
  const int rec_voff = 64 * ((lane >> 1) & 15) + 32 * (lane >> 5) + 16 * (lane & 1);
  constexpr int L_RED = 65536, L_REDE = L_RED + 8192;
  v4u wo[4], we[2];
  auto mk_wo = [&]() {
    const char* base = (const char*)(isX ? P.woA : P.woB) + (h * 128 + wave * 16) * 1152;
    const u64 u = ((u64)(u32)__builtin_amdgcn_readfirstlane((u32)((u64)base >> 32)) << 32) | (u32)__builtin_amdgcn_readfirstlane((u32)(u64)base);
    u64 v = u;
    asm volatile("" : "+s"(v));
    return mk((const void*)v, 16 * 1152);
  };
  auto load_wo = [&]() {
    const rsrc_t rwo = mk_wo();
    const int voff = g * 4608 + c16 * 16;
#pragma unroll
    for (int i = 0; i < 4; ++i) { wo[i] = ld4<AUX_NT>(rwo, voff + i * 1152, r * 256); pin(); }
  };
  auto load_we = [&]() {
    if (r == 3) {
      const rsrc_t rwo = mk_wo();
      const int voff = g * 4608 + bsel * 2304 + (c16 & 7) * 16;
#pragma unroll
      for (int i = 0; i < 2; ++i) { we[i] = ld4<AUX_NT>(rwo, voff + i * 1152, 1024); pin(); }
    }
  };
  constexpr int L_R2 = 53248;

  if (isX) {
    constexpr int L_ML = 0;
    v4u kk[16], vv[16];
    {
      const int chunk = SPREAD ? 2 * w4 + (((wave >> 2) ^ (hsh >> (2 + w4))) & 1) : wave;
      const size_t kvoff = ((size_t)(bx * 16 + gk) * 8192 + 6144 + r * 512 + chunk * 64) * 256;
      const int xo = ((SPREAD ? hsh + wave : hsh) & 3) * 4096;
      const rsrc_t rk = mk((const char*)P.kc + kvoff, 16384), rv = mk((const char*)P.vc + kvoff, 16384);
      const int k_voff = ((c16 & 2) * 2 + (c16 >> 2)) * 256 + kq_voff;
      if (WSYNC == 2) load_wo();
#pragma unroll
      for (int i = 0; i < 16; ++i) { kk[i] = ld4<AUX_NT>(rk, k_voff + (i & 1) * 128 + ((i >> 1) & 1) * 2048, ((i >> 2) * 4096) ^ xo); pin(); }
      if (KSYNC && wave >= 4) { post(L_FLAG + 40, lane); spin_flag(smem + L_FLAG + 40, 4); }
#pragma unroll
      for (int i = 0; i < 16; ++i) { vv[i] = ld4<AUX_NT>(rv, lane * 16 + (i & 3) * 1024, ((i >> 2) * 4096) ^ xo); pin(); }
      if (WSYNC == 1) post(L_FLAG + 44, lane);
    }
    ST(4);
    asm volatile("" ::"v"(pfc));
    spin_flag(smem + L_FLAG + 16, 4);
    ST(5);
    float sc[16];
    {
      const v4u q0 = *(const v4u*)(smem + L_QV + 512 + bsel * 256 + kq_voff), q1 = *(const v4u*)(smem + L_QV + 512 + bsel * 256 + 128 + kq_voff);
      const bool odd = c16 & 1;
#pragma unroll
      for (int be = 0; be < 8; ++be) {
        f4 d = {0.f, 0.f, 0.f, 0.f};
        d = mfma16(kk[2 * be], q0, d); d = mfma16(kk[2 * be + 1], q1, d);
        const float u0 = odd ? d[1] : d[0], u1 = odd ? d[3] : d[2];
        sc[2 * be] = u0 + dppf<XOR1>(u0); sc[2 * be + 1] = u1 + dppf<XOR1>(u1);
      }
    }
    float m = mx(mx(mx(sc[0], sc[1], sc[2]), mx(sc[3], sc[4], sc[5]), mx(sc[6], sc[7], sc[8])), mx(mx(sc[9], sc[10], sc[11]), mx(sc[12], sc[13], sc[14]), sc[15]));
    m = swap32_max(swap16_max(m)) * SC;
    float l = 0.f;
#pragma unroll
    for (int be = 0; be < 8; ++be) {
      const float p0 = ex2(fmaf(sc[2 * be], SC, -m)), p1 = ex2(fmaf(sc[2 * be + 1], SC, -m));
      l += p0 + p1;
      const u32 pp = pk(p0, p1);
      kk[2 * be] = diag(pp, sel0); kk[2 * be + 1] = diag(pp, sel1);
    }
    l = swap16_add(l, l); l = swap32_add(l, l);
    if ((lane & 0x37) == 0) { *(float*)(smem + L_ML + bsel * 64 + wave * 4) = m; *(float*)(smem + L_ML + bsel * 64 + 32 + wave * 4) = l; }
    post(L_FLAG + 36, lane);
#pragma unroll
    for (int i = 0; i < 16; i += 4) asm volatile("" : "+v"(kk[i]), "+v"(kk[i + 1]), "+v"(kk[i + 2]), "+v"(kk[i + 3]));
    if (WSYNC == 1) spin_flag(smem + L_FLAG + 44, 8);
    else __builtin_amdgcn_s_setprio(0);
    if (WSYNC != 2) load_wo();
    spin_flag(smem + L_FLAG + 36, 8);
    const char* p = smem + L_ML + (lane >> 5) * 64;
    const f4 m0 = *(const f4*)p, m1 = *(const f4*)(p + 16);
    const float M = mx(mx(mx(m0[0], m0[1]), mx(m0[2], m0[3])), mx(mx(m1[0], m1[1]), mx(m1[2], m1[3])));
    const float fo = ex2(*(const float*)(p + wave * 4) - M);
    float L = ex2(*(const float*)(p + (lane & 7) * 4) - M) * *(const float*)(p + 32 + (lane & 7) * 4);
    pin();
    f4 acc = {0.f, 0.f, 0.f, 0.f};
#pragma unroll
    for (int i = 0; i < 16; ++i) acc = mfma16(kk[i], vv[i], acc);
    *(f4*)(smem + L_R2 + wave * 1024 + lane * 16) = acc * fo;
    ST(6);
    if (wave == 2) {
      L = quad_sum(L); L += dppf<HMIR>(L);
      if ((lane & 24) == 0) st4(v4u{fu(M), tag, fu(L), tag}, ws, (lane >> 5) * 128 + (lane & 7) * 16, S_HS + id * SLOT);
    }
  } else {
    constexpr int L_Q = 0, L_P = 32768, L_MX = 40960, L_LS = 41472, L_LAT = 45056, L_ST = 49152, L_CNT = L_FLAG + 32;
    const int b = id >> 6;
    __builtin_amdgcn_s_waitcnt(0x0070);
    v4u fr[32], wv[4];
    const int r32 = lane & 31, hh = lane >> 5;
    const int row_base = r32 * 1024 + 16 * (hh ^ (((r32 & 3) << 2) | ((r32 >> 2) & 3)));
    const rsrc_t ruv = mk((const char*)P.wuv + (r * 128 + 64 * (wave >> 2) + 8 * w4) * 8192 + h * 256, 39 * 8192 + 256);
#define LDUV(i) wv[i] = ld4<AUX_NT>(ruv, g * 16384 + c16 * 16, (((i) >> 1) + 32 * ((i) & 1)) * 8192)
#define QCNT(rq) (L_FLAG + ((rq) < 3 ? 36 + 4 * (rq) : 56))
    f16v s;
    if (wave < 4) {
      const rsrc_t rc = SPREAD ? mk((const char*)P.ckv + (size_t)id * 131072 + wave * 32768, 32768) : mk((const char*)P.ckv + (size_t)id * 131072 + wave * 8192, 131072 - wave * 8192);
      __builtin_amdgcn_s_waitcnt(0xc07f);
      const int kz = ((hsh >> 3) + wave) & 3;
      const int xi = hsh & 4, lx = lane ^ ((2 * wave + (xi >> 2)) & 3);
#pragma unroll
      for (int i = 0; i < 32; ++i) {
        const int ro = (i >> 3) * 32768 + ((i & 7) ^ xi) * 1024;
        __builtin_amdgcn_raw_ptr_buffer_load_lds(rc, (LAS void*)(smem + wave * 8192 + ro), 16, 16 * (lx ^ (((i & 3) << 2) | ((i >> 2) & 1))), SPREAD ? ((i >> 3) ^ kz) * 8192 + ((i & 7) ^ xi) * 1024 : ro, 0, AUX_NT);
        pin();
        if (i >= WY - 1 && i < NY) asm volatile("s_waitcnt vmcnt(%0)" ::"i"(WY - 1) : "memory");
        if (i == 23 || i == 31) {
          asm volatile("s_waitcnt vmcnt(16)" ::: "memory");
          if (lane == 0) asm volatile("ds_add_u32 %0, %1" ::"v"(L_FLAG + 4 * ((i >> 3) - 2)), "v"(1u) : "memory");
          pin();
        }
      }
      int qa[8], fa[8];
#pragma unroll
      for (int t = 0; t < 8; ++t) { qa[t] = row_base ^ (32 * t); fa[t] = qa[t] + wave * 32768; asm volatile("" : "+v"(qa[t]), "+v"(fa[t])); }
      asm volatile("s_waitcnt vmcnt(8)" ::: "memory");
      post(L_FLAG + 8, lane);
      auto landed = [&]() {
        bar();
        asm volatile("s_waitcnt vmcnt(0)" ::: "memory");
        post(L_FLAG + 12, lane);
        ST(1);
      };
      if (wave == 3) landed();
      spin_flag_asm(L_FLAG + 4 * wave, 4);
#pragma unroll
      for (int t = 0; t < 32; ++t)
        asm volatile("ds_read_b128 %0, %1 offset:%2" : "=v"(fr[t]) : "v"(fa[t & 7]), "i"(256 * (t >> 3)) : "memory");
      pin();
      if (wave != 3) landed();
      __builtin_amdgcn_s_waitcnt(0x0f70);
      asm volatile("" ::"v"(pfc));
      v4u qb[4];
#define QRD(t) (*(const v4u*)(smem + L_Q + qa[(t) & 7] + 256 * ((t) >> 3)))
      spin_flag(smem + L_FLAG + 36, 4);
      ST(2);
      u32 rdy = 0;
#pragma unroll
      for (int t = 0; t < 4; ++t) qb[t] = QRD(t);
#pragma unroll
      for (int i = 0; i < 16; ++i) s[i] = 0.f;
      pin();
#pragma unroll
      for (int t = 0; t < 32; ++t) {
        s = mfma32(fr[t], qb[t & 3], s);
        __builtin_amdgcn_sched_barrier(0);
        if ((t & 7) == 4 && t < 24) {
          if (__builtin_expect((u32)__builtin_amdgcn_readfirstlane(rdy) < 4, 0)) spin_flag(smem + QCNT((t >> 3) + 1), 4);
          pin();
        }
        if ((t & 7) == 1 && t < 24) rdy = *(volatile const LAS u32*)(smem + QCNT((t >> 3) + 1));
        if (t + 4 < 32) qb[t & 3] = QRD(t + 4);
        if (t >= 21 && t < 25) LDUV(t - 21);
        __builtin_amdgcn_sched_barrier(0);
      }
      ST(3);
      float m = mx(mx(mx(s[0], s[1], s[2]), mx(s[3], s[4], s[5]), mx(s[6], s[7], s[8])), mx(mx(s[9], s[10], s[11]), mx(s[12], s[13], s[14]), s[15]));
      m = swap32_max(m);
      if (lane < 32) *(float*)(smem + L_MX + lane * 16 + wave * 4) = m;
    } else {
      const int q = c16 >> 2, p = lane & 3;
      const int base = (4 * hh + q) * 1024 + w4 * 256 + 16 * ((2 * (g & 1) + (p >> 1)) ^ (4 * q + hh)) + 8 * (p & 1);
      auto frags = [&](auto blk) {
        constexpr int kb = decltype(blk)::value;
        spin_flag(smem + L_FLAG + 4 * kb, 4);
#pragma unroll
        for (int ta = 0; ta < 4; ++ta)
#pragma unroll
          for (int sh = 0; sh < 2; ++sh) {
            const char* pa = smem + kb * 32768 + sh * 16384;
            const v2u a = __builtin_bit_cast(v2u, __builtin_amdgcn_ds_read_tr16_b64_v4i16((LAS s4v*)(pa + (base ^ (64 * ta)))));
            const v2u c = __builtin_bit_cast(v2u, __builtin_amdgcn_ds_read_tr16_b64_v4i16((LAS s4v*)(pa + (base ^ (64 * ta) ^ 32) + 8192)));
            fr[ta * 8 + 2 * kb + sh] = v4u{a.x, a.y, c.x, c.y};
          }
        pin();
      };
      v4u qg[16];
      int qvo = S_H2 + (32 * w4 + 4 * hh) * SLOT + b * 512 + r32 * 16;
      asm volatile("" : "+v"(qvo));
#define LDQ(rq, i) qg[4 * (rq) + (i)] = ld4<AUX_SC1>(ws, qvo, (8 * (i) + (rq)) * SLOT)
      int qw[4];
#pragma unroll
      for (int i = 0; i < 4; ++i) {
        const int hd = 8 * w4 + 2 * i + hh;
        qw[i] = hd * 1024 + 16 * ((r32 >> 1) ^ (((hd & 3) << 2) | ((hd >> 2) & 3))) + 8 * (lane & 1);
        asm volatile("" : "+v"(qw[i]));
      }
      frags(std::integral_constant<int, 0>{});
      frags(std::integral_constant<int, 1>{});
      frags(std::integral_constant<int, 2>{});
      if (!EARLYQ) { bar(); spin_flag(smem + L_FLAG + 12, 4); }
#pragma unroll
      for (int i = 0; i < 16; ++i) { LDQ(i >> 2, i & 3); pin(); }
      if (EARLYQ) bar();
#pragma unroll
      for (int rq = 0; rq < 4; ++rq) {
        u32 bad = 0;
#pragma unroll
        for (int i = 0; i < 4; ++i) bad = bad4(bad, qg[4 * rq + i], tag);
        if (__builtin_expect(!none(bad), 0)) {
          _Pragma("nounroll") for (int spin = 0; spin < SPIN_CAP; ++spin) {
            pin();
            bad = 0;
#pragma unroll
            for (int i = 0; i < 4; ++i) LDQ(rq, i);
#pragma unroll
            for (int i = 0; i < 4; ++i) bad = bad4(bad, qg[4 * rq + i], tag);
            if (none(bad)) break;
            __builtin_amdgcn_s_sleep(4);
          }
        }
#pragma unroll
        for (int i = 0; i < 4; ++i) *(v2u*)(smem + L_Q + qw[i] + 256 * rq) = v2u{qg[4 * rq + i].x, qg[4 * rq + i].z};
        post(QCNT(rq), lane);
      }
      ST(3);
#pragma unroll
      for (int i = 0; i < 4; ++i) { LDUV(i); pin(); }
      frags(std::integral_constant<int, 3>{});
    }
    bar();
    if (wave < 4) {
      const f4 mm = *(const f4*)(smem + L_MX + r32 * 16);
      const float m = mx(mx(mm[0], mm[1]), mx(mm[2], mm[3])) * SC;
      u32 pp[8];
#pragma unroll
      for (int i = 0; i < 8; ++i) {
        s[2 * i] = ex2(fmaf(s[2 * i], SC, -m)); s[2 * i + 1] = ex2(fmaf(s[2 * i + 1], SC, -m));
        pp[i] = pk(s[2 * i], s[2 * i + 1]);
      }
      *(v4u*)(smem + L_P + wave * 2048 + lane * 16) = v4u{pp[0], pp[1], pp[2], pp[3]};
      *(v4u*)(smem + L_P + wave * 2048 + 1024 + lane * 16) = v4u{pp[4], pp[5], pp[6], pp[7]};
    }
    bar();
    pin();
    if (wave < 4) {
      float l = 0.f;
#pragma unroll
      for (int i = 0; i < 8; ++i) l += s[2 * i] + s[2 * i + 1];
      l = swap32_add(l, l);
      if (lane < 32) *(float*)(smem + L_LS + lane * 16 + wave * 4) = l;
      tag = __builtin_amdgcn_readfirstlane(*(const u32*)(smem + L_FLAG + 28));
    }
    ST(4);
    if (ABL == 2) { load_wo(); load_we(); asm volatile("" ::"v"(wo[0]), "v"(wo[1]), "v"(wo[2]), "v"(wo[3]), "v"(wv[0]), "v"(wv[1]), "v"(wv[2]), "v"(wv[3])); goto done; }
    if (wave >= 4) {
      v4u pf[8];
#pragma unroll
      for (int ks = 0; ks < 8; ++ks) pf[ks] = *(const v4u*)(smem + L_P + ks * 1024 + lane * 16);
      f16v o0, o1, o2, o3;
#pragma unroll
      for (int i = 0; i < 16; ++i) { o0[i] = 0.f; o1[i] = 0.f; o2[i] = 0.f; o3[i] = 0.f; }
      const int voff = hh * 524288 + r32 * 4, soff = W_H3D + (wave - 4) * 32768 + id * 256;
      pin();
#pragma unroll
      for (int ks = 0; ks < 8; ++ks) { o0 = mfma32(pf[ks], fr[ks], o0); o1 = mfma32(pf[ks], fr[8 + ks], o1); }
      __builtin_amdgcn_sched_barrier(0);
#define OST(a, c, i, pi) __builtin_amdgcn_raw_buffer_store_b32(pk(a[i], c[i]), ws, voff + (pi) * 128, soff + (((i) & 3) + 8 * ((i) >> 2)) * 131072, AUX_SC1)
#pragma unroll
      for (int ks = 0; ks < 8; ++ks) {
        o2 = mfma32(pf[ks], fr[16 + ks], o2);
        __builtin_amdgcn_sched_barrier(0);
        if (ks) OST(o0, o1, 2 * ks - 2, 0);
        __builtin_amdgcn_sched_barrier(0);
        o3 = mfma32(pf[ks], fr[24 + ks], o3);
        __builtin_amdgcn_sched_barrier(0);
        if (ks) OST(o0, o1, 2 * ks - 1, 0);
        __builtin_amdgcn_sched_barrier(0);
      }
      OST(o0, o1, 14, 0); OST(o0, o1, 15, 0);
#pragma unroll
      for (int i = 0; i < 16; ++i) OST(o2, o3, i, 1);
    }
    bar();
    ST(5);
    if (wave >= 4) {
      __builtin_amdgcn_s_setprio(3);
      const int hd = (lane >> 3) * 4 + (lane & 3);
      const f4 mm = *(const f4*)(smem + L_MX + hd * 16), ll = *(const f4*)(smem + L_LS + hd * 16);
      v4u sv = {fu(mx(mx(mm[0], mm[1]), mx(mm[2], mm[3])) * SC), tag, fu((ll[0] + ll[1]) + (ll[2] + ll[3])), tag};
      asm volatile("s_waitcnt vmcnt(0)" : "+v"(sv)::"memory");
      if (arrive(L_CNT, lane) == 3) st4(sv, ws, lane * 16, S_HS + (128 + id) * SLOT);
      __builtin_amdgcn_s_setprio(0);
      ST(6);
    }
    pin();
    if (!WO_LATE) load_wo();
    if (ABL == 3) { asm volatile("" ::"v"(wo[0]), "v"(wo[1]), "v"(wo[2]), "v"(wo[3]), "v"(wv[0]), "v"(wv[1]), "v"(wv[2]), "v"(wv[3])); goto done; }
    if (wave < 4) __builtin_amdgcn_s_sleep(D_H3 + 14); else __builtin_amdgcn_s_sleep(D_H3);
    {
      v4u st;
      _Pragma("nounroll") for (int spin = 0; spin < SPIN_CAP; ++spin) {
        pin();
        st = ld4<AUX_SC1>(ws, (lane >> 2) * SLOT, S_HS + (128 + wave * 16) * SLOT + x * 128 + (h & 3) * 16);
        if (none(bad4(0u, st, tag))) break;
        __builtin_amdgcn_s_sleep(P_H3);
      }
      pin();
      ST(7);
      v4u d[4];
#pragma unroll
      for (int i = 0; i < 4; ++i) d[i] = ld4<AUX_SC1>(ws, g * 1024 + c16 * 16 + i * 256, W_H3D + id * 32768 + wave * 4096);
      pin();
      float M = uf(st.x);
      M = mx(M, dppf<ROR4>(M)); M = mx(M, dppf<ROR8>(M)); M = swap32_max(swap16_max(M));
      float wg = ex2(uf(st.x) - M);
      float L = wg * uf(st.z);
      L += dppf<ROR4>(L); L += dppf<ROR8>(L); L = swap16_add(L, L); L = swap32_add(L, L);
      if (lane == 0) *(f2*)(smem + L_ST + wave * 8) = f2{M, L};
      bar();
      const f4 s0 = *(const f4*)(smem + L_ST + (wave >> 2) * 32), s1 = *(const f4*)(smem + L_ST + (wave >> 2) * 32 + 16);
      const float MM = mx(mx(s0[0], s0[2], s1[0]), s1[2]);
      const float LL = fmaf(ex2(s0[0] - MM), s0[1], fmaf(ex2(s0[2] - MM), s0[3], fmaf(ex2(s1[0] - MM), s1[1], ex2(s1[2] - MM) * s1[3])));
      wg *= ex2(M - MM) * __builtin_amdgcn_rcpf(LL);
      const u32 w01 = pk(dppf<BCAST>(wg), dppf<BCAST + 4>(wg)), w23 = pk(dppf<BCAST + 8>(wg), dppf<BCAST + 12>(wg));
      v4u af[4] = {diag(w01, sel0), diag(w01, sel1), diag(w23, sel0), diag(w23, sel1)};
      asm volatile("" : "+v"(af[0]), "+v"(af[1]), "+v"(af[2]), "+v"(af[3]));
      pin();
      if (WO_LATE == 3) load_wo();
      f4 lat = {0.f, 0.f, 0.f, 0.f};
#pragma unroll
      for (int i = 0; i < 4; ++i) lat = mfma16(af[i], d[i], lat);
      if (lane < 32) *(f4*)(smem + L_LAT + wave * 512 + c16 * 32 + g * 16) = lat;
      pin();
    }
    bar();
    if (WO_LATE == 2) load_wo();
    {
      const char* p = smem + L_LAT + bsel * 2048 + wave * 64 + g * 16;
      const f4 c = (*(const f4*)p + *(const f4*)(p + 512)) + (*(const f4*)(p + 1024) + *(const f4*)(p + 1536));
      *(f4*)(smem + L_R2 + wave * 1024 + lane * 16) = rows16(pk(c[0], c[1]), pk(c[2], c[3]), wv, sel0, sel1);
    }
  }
  {
  bar();
  if (isX) ST(7);
  if (wave < 2) {
    const f2 sv = sum8(smem + L_R2 + tid * 8);
    st4(v4u{fu(sv.x), tag, fu(sv.y), tag}, ws, rec_voff, S_HD + ro + id * SLOT + wave * 1024);
  }
  pin();
  load_we();
  const int gsv = (c16 & 3) * SLOT + bsel * (isX ? 4 * SLOT : 1024), gso = isX ? (id & ~7) * SLOT : (128 + (id & ~3)) * SLOT;
  const int dv = gsv + g * 32 + (c16 & 4) * 4, dso = S_HD + gso + (isX ? (h & 1) * 1024 : 0) + wave * 128;
  if (isX) __builtin_amdgcn_s_sleep(D_A3); else __builtin_amdgcn_s_sleep(D_H4);
  ST(8);
  {
    v4u d0 = ld4<AUX_SC1>(ws, dv, dso);
    pin();
    float w = 1.f;
    if (isX) {
      v4u st;
      _Pragma("nounroll") for (int spin = 0; spin < SPIN_CAP; ++spin) {
        pin();
        st = ld4<AUX_SC1>(ws, gsv, S_HS + gso + (h & 1) * 128);
        if (none(bad4(0u, st, tag))) break;
        __builtin_amdgcn_s_sleep(4);
      }
      float M = uf(st.x);
      M = mx(M, dppf<XOR1>(M)); M = mx(M, dppf<XOR2>(M));
      w = ex2(uf(st.x) - M);
      w *= __builtin_amdgcn_rcpf(quad_sum(w * uf(st.z)));
      asm volatile("" : "+v"(w));
    }
    _Pragma("nounroll") for (int spin = 0; spin < SPIN_CAP; ++spin) {
      if (none(bad4(0u, d0, tag))) break;
      pin();
      d0 = ld4<AUX_SC1>(ws, dv, dso);
      if (P_HD) __builtin_amdgcn_s_sleep(P_HD);
    }
    ST(9);
    const u32 cm = pk(quad_sum(w * uf(d0.x)), quad_sum(w * uf(d0.z))), co = dppu<HMIR>(cm);
    const u32 c01 = (c16 & 4) ? co : cm, c23 = (c16 & 4) ? cm : co;
    *(f4*)(smem + L_RED + wave * 1024 + lane * 16) = rows16(c01, c23, wo, sel0, sel1);
    if (r == 3) {
      const f4 z = {0.f, 0.f, 0.f, 0.f};
      const f4 lo = mfma16(diag(c01, sel1), we[1], mfma16(diag(c01, sel0), we[0], z)), hi = mfma16(diag(c23, sel1), we[1], mfma16(diag(c23, sel0), we[0], z));
      *(f4*)(smem + L_REDE + wave * 1024 + lane * 16) = f4{lo[0] + dppf<ROR8>(hi[0]), lo[1] + dppf<ROR8>(hi[1]), lo[2] + dppf<ROR8>(hi[2]), lo[3] + dppf<ROR8>(hi[3])};
    }
  }
  bar();
  if (wave < 4) {
    if (wave < 2 || (r == 3 && (lane & 16) == 0)) {
      const f2 sv = sum8(smem + L_RED + (wave >> 1) * 8192 + (tid & 127) * 8);
      st4(v4u{fu(sv.x), tag, fu(sv.y), tag}, ws, lane * 16, S_H5 + ro + id * SLOT + wave * 1024);
    }
    if (isX) ST(10);
  } else if (wave == 4 && id < 72) {
    const int e = id - 64, L = id < 64 ? id : 64 + (e & 1) + 4 * (e >> 1);
    const int rso = S_H5 + ro + (id < 64 ? (L >> 4) * SLOT + (L & 15) * 128 : 3 * SLOT + (L - 48) * 128);
    v4u v[4];
    __builtin_amdgcn_s_sleep(D_H5);
    _Pragma("nounroll") for (int spin = 0; spin < SPIN_CAP; ++spin) {
      pin();
      u32 bad = 0;
#pragma unroll
      for (int i = 0; i < 4; ++i) v[i] = ld4<AUX_SC1>(ws, (lane >> 3) * 4 * SLOT + (lane & 7) * 16, rso + i * 32 * SLOT);
#pragma unroll
      for (int i = 0; i < 4; ++i) bad = bad4(bad, v[i], tag);
      if (none(bad)) break;
      __builtin_amdgcn_s_sleep(2);
    }
    float a = (uf(v[0].x) + uf(v[1].x)) + (uf(v[2].x) + uf(v[3].x)), c = (uf(v[0].z) + uf(v[1].z)) + (uf(v[2].z) + uf(v[3].z));
    a += dppf<ROR8>(a); c += dppf<ROR8>(c);
    a = swap16_add(a, a); c = swap16_add(c, c);
    a = swap32_add(a, a); c = swap32_add(c, c);
    if (lane < 8) {
      const int al = L & 15;
      const int n = (al >> 3) * 1152 + (isX ? 0 : 576) + 128 * (L >> 4) + 32 * (al & 3) + 8 * (lane >> 1) + 4 * ((al >> 2) & 1) + 2 * (lane & 1);
      ((u32*)P.out)[n >> 1] = pk(a, c);
    }
    if (!isX && id == 0 && lane == 0) __builtin_amdgcn_raw_buffer_store_b32(tag, ws, 0, W_EPOCH, AUX_SC1);
  }
  }
done:;
}

extern "C" void kernel_launch(void* const* d_in, const int* in_sizes, int n_in, void* d_out, int out_size, void* d_ws, size_t ws_size,
                              hipStream_t stream) {
  Params p;
  char* w = (char*)d_ws + (WS_AT_END ? ((ws_size - (18 << 20)) & ~(size_t)0x1fffff) : 0);
  p.xA = d_in[0]; p.kc = d_in[1]; p.vc = d_in[2]; p.wqA = d_in[3]; p.woA = d_in[4];
  p.xB = d_in[5]; p.ckv = d_in[6]; p.wqB = d_in[7]; p.wuk = d_in[8]; p.wuv = d_in[9]; p.woB = d_in[10];
  p.out = d_out; p.ws = w;
  fused_decode<<<256, 512, 0, stream>>>(p);
}
```

```cpp
#define STAMP 1
#define REP_IDX 0
#define REP_ROLE 2
#define REP_STAT 0
#define SMASK 2049
#include <hip/hip_runtime.h>
#include <hip/hip_bf16.h>
#include <stdint.h>
#include <type_traits>

typedef unsigned u32;
typedef unsigned long long u64;
typedef u32 v4u __attribute__((ext_vector_type(4)));
typedef u32 v2u __attribute__((ext_vector_type(2)));
typedef float f2 __attribute__((ext_vector_type(2)));
typedef float f4 __attribute__((ext_vector_type(4)));
typedef float f16v __attribute__((ext_vector_type(16)));
typedef __bf16 bf8 __attribute__((ext_vector_type(8)));
typedef __bf16 bf2 __attribute__((ext_vector_type(2)));
typedef short s4v __attribute__((ext_vector_type(4)));
typedef __amdgpu_buffer_rsrc_t rsrc_t;
#define LAS __attribute__((address_space(3)))
#define DI __device__ __forceinline__

constexpr int AUX_NT = 2, AUX_SC1 = 16;
constexpr int W_EPOCH = 0;
constexpr int SLOT = 16896, W_SLOT = 4096;
constexpr int S_H1 = W_SLOT;
constexpr int S_HD = W_SLOT + 2048;
constexpr int S_H5 = W_SLOT + 4096;
constexpr int S_HS = W_SLOT + 8192;
constexpr int S_H2 = W_SLOT + 9216;
constexpr int S_H1A = W_SLOT + 10240;
constexpr int W_H3D = 5 << 20;
constexpr int W_END = W_H3D + 4194304;
constexpr int W_STAMP = 16 << 20;
constexpr float SC = 0.08838834764831845f * 1.4426950408889634f;
#define KNOB(n, v) constexpr int n = v
#define K_D_H1 14
#define K_WX 2
#define K_NX 12
#define K_WY 2
#define K_NY 0
#define K_DXA 20
#define K_P_H3 6
#define P_HD 2
#define K_D_A3 12
#define K_D_H3 30
#define K_D_H4 14
#define K_D_H5 14
#define EARLYQ 1
#define K_D_H2 0
#define WS_AT_END 0
#define SPREAD 1
#define WE_EARLY 0
#define KSYNC 1
#define WSYNC 1
#define T_GO 0
#define T_A1 0
#define T_Y0 0
#define D_H1A 16
#define WO_LATE 3
#define K_DX0 0
#define ABL 0
#define CODE_BYTES 18176
constexpr int D_H1 = K_D_H1, WX = K_WX, NX = K_NX, WY = K_WY, NY = K_NY, DXA = K_DXA, P_H3 = K_P_H3, D_A3 = K_D_A3, D_H3 = K_D_H3, D_H4 = K_D_H4,
              D_H5 = K_D_H5, D_H2 = K_D_H2;
constexpr int SPIN_CAP = 1 << 17;

struct Params {
  const void *xB, *wqB, *wuk, *ckv;
  void *ws, *out;
  const void *xA, *wqA, *kc, *vc, *woA, *woB, *wuv;
};

DI rsrc_t mk(const void* p, u32 bytes) { return __builtin_amdgcn_make_buffer_rsrc((void*)p, 0, bytes, 0x00020000); }
template <int AUX> DI v4u ld4(rsrc_t r, int voff, int soff) { return __builtin_amdgcn_raw_buffer_load_b128(r, voff, soff, AUX); }
template <int AUX> DI v2u ld2(rsrc_t r, int voff, int soff) { return __builtin_amdgcn_raw_buffer_load_b64(r, voff, soff, AUX); }
DI void st4(v4u v, rsrc_t r, int voff, int soff) { __builtin_amdgcn_raw_buffer_store_b128(v, r, voff + soff, 0, AUX_SC1); }
DI u32 fu(float f) { return __builtin_bit_cast(u32, f); }
DI float uf(u32 u) { return __builtin_bit_cast(float, u); }
DI u32 pk(float lo, float hi) { f2 f = {lo, hi}; return __builtin_bit_cast(u32, __builtin_convertvector(f, bf2)); }
DI float blo(u32 u) { return uf(u << 16); }
DI float bhi(u32 u) { return uf(u & 0xffff0000u); }
template <int C> DI u32 dppu(u32 x) { return __builtin_amdgcn_mov_dpp(x, C, 0xf, 0xf, true); }
template <int C> DI float dppf(float x) { return uf(dppu<C>(fu(x))); }
constexpr int XOR1 = 0xB1, XOR2 = 0x4E, HMIR = 0x141, ROR4 = 0x124, ROR8 = 0x128, BCAST = 0x150;
DI f4 mfma16(v4u a, v4u b, f4 c) { return __builtin_amdgcn_mfma_f32_16x16x32_bf16(__builtin_bit_cast(bf8, a), __builtin_bit_cast(bf8, b), c, 0, 0, 0); }
DI f16v mfma32(v4u a, v4u b, f16v c) { return __builtin_amdgcn_mfma_f32_32x32x16_bf16(__builtin_bit_cast(bf8, a), __builtin_bit_cast(bf8, b), c, 0, 0, 0); }
DI float mx(float a, float b) { return __builtin_elementwise_maximum(a, b); }
DI float mx(float a, float b, float c) { return mx(mx(a, b), c); }
DI float mx4(f4 v) { return mx(mx(v[0], v[1], v[2]), v[3]); }
DI float ex2(float x) { return __builtin_amdgcn_exp2f(x); }
DI float swap32_add(float a, float b) { auto s = __builtin_amdgcn_permlane32_swap(fu(a), fu(b), false, false); return uf(s[0]) + uf(s[1]); }
DI float swap16_add(float a, float b) { auto s = __builtin_amdgcn_permlane16_swap(fu(a), fu(b), false, false); return uf(s[0]) + uf(s[1]); }
DI float swap32_max(float a) { auto s = __builtin_amdgcn_permlane32_swap(fu(a), fu(a), false, false); return mx(uf(s[0]), uf(s[1])); }
DI float swap16_max(float a) { auto s = __builtin_amdgcn_permlane16_swap(fu(a), fu(a), false, false); return mx(uf(s[0]), uf(s[1])); }
DI void bar() {
  __builtin_amdgcn_sched_barrier(0);
  asm volatile("s_waitcnt lgkmcnt(0)\n\ts_barrier" ::: "memory");
  __builtin_amdgcn_sched_barrier(0);
}
DI void pin() { asm volatile("" ::: "memory"); __builtin_amdgcn_sched_barrier(0); }
DI u32 bad4(u32 acc, v4u v, u32 tag) {
  return __builtin_amdgcn_bitop3_b32(__builtin_amdgcn_bitop3_b32(acc, v.y, tag, 0xF6), v.w, tag, 0xF6);
}
DI bool none(u32 bad) { return __builtin_amdgcn_ballot_w64(bad != 0) == 0; }
DI v4u diag(u32 src, const u32 (&sel)[4]) {
  v4u a;
#pragma unroll
  for (int k = 0; k < 4; ++k) a[k] = __builtin_amdgcn_perm(src, src, sel[k]);
  return a;
}

#define ST(i) do { if ((SMASK >> (i)) & 1) asm volatile("s_memrealtime %0\n\ts_waitcnt lgkmcnt(0)" : "=s"(ts[i]) :: "memory"); } while (0)

DI void spin_flag(const char* p, u32 want = 1) {
  _Pragma("nounroll") for (int spin = 0; spin < SPIN_CAP * 8; ++spin) {
    if ((u32)__builtin_amdgcn_readfirstlane(*(volatile const LAS u32*)p) >= want) break;
    __builtin_amdgcn_s_sleep(1);
  }
  pin();
}
DI u32 arrive(u32 addr, int lane) {
  u32 old = 0;
  if (lane == 0) asm volatile("ds_add_rtn_u32 %0, %1, %2\n\ts_waitcnt lgkmcnt(0)" : "=v"(old) : "v"(addr), "v"(1u) : "memory");
  old = __builtin_amdgcn_readfirstlane(old);
  pin();
  return old;
}
DI void post(u32 addr, int lane) {
  if (lane == 0) asm volatile("ds_add_u32 %0, %1" ::"v"(addr), "v"(1u) : "memory");
  pin();
}
DI void spin_flag_asm(u32 addr, u32 want) {
  _Pragma("nounroll") for (int spin = 0; spin < SPIN_CAP * 8; ++spin) {
    u32 v;
    asm volatile("ds_read_b32 %0, %1\n\ts_waitcnt lgkmcnt(0)" : "=v"(v) : "v"(addr) : "memory");
    if ((u32)__builtin_amdgcn_readfirstlane(v) >= want) break;
    __builtin_amdgcn_s_sleep(1);
  }
  pin();
}
DI float quad_sum(float v) { v += dppf<XOR1>(v); return v + dppf<XOR2>(v); }
DI f2 sum8(const char* p) {
  f2 s = *(const f2*)p;
#pragma unroll
  for (int w = 1; w < 8; ++w) s += *(const f2*)(p + w * 1024);
  return s;
}
DI f4 rows16(u32 c01, u32 c23, const v4u (&w)[4], const u32 (&sel0)[4], const u32 (&sel1)[4]) {
  f4 acc = {0.f, 0.f, 0.f, 0.f};
  acc = mfma16(diag(c01, sel0), w[0], acc); acc = mfma16(diag(c01, sel1), w[1], acc);
  acc = mfma16(diag(c23, sel0), w[2], acc); acc = mfma16(diag(c23, sel1), w[3], acc);
  return acc;
}

__global__ void __launch_bounds__(512, 2) fused_decode(Params P) {
  constexpr int L_FLAG = 131072;
  __shared__ __attribute__((aligned(16))) char smem[L_FLAG + 64 + 4096];
  u64 ts[12];
#pragma unroll
  for (int i = 0; i < 12; ++i) ts[i] = 0;
  ST(0);
  const u32 t_in = (u32)__builtin_amdgcn_s_memrealtime();
  auto pace = [&](u32 t) { while ((u32)__builtin_amdgcn_s_memrealtime() - t_in < t) __builtin_amdgcn_s_sleep(1); pin(); };
  const int tid = threadIdx.x, lane = tid & 63, wave = __builtin_amdgcn_readfirstlane(tid >> 6);
  const int bid = blockIdx.x, x = bid & 7, jj = bid >> 3, j = jj & 15;
  const bool isX = jj < 16;
  const int g = lane >> 4, c16 = lane & 15, bsel = c16 >> 3;
  const int h = 4 * x + (j >> 2), r = j & 3, id = x * 16 + j;
  u32 pfc = 0;
  if (tid < 16) {
    u64 pc;
    asm volatile("s_getpc_b64 %0" : "=s"(pc));
    pfc = __builtin_amdgcn_raw_buffer_load_b32(mk((const void*)(pc & ~255ull), CODE_BYTES), tid * 64, jj * 1024, 0);
  }
  if (tid < 16) *(u32*)(smem + L_FLAG + tid * 4) = 0u;
  asm volatile("" ::"s"(P.xB), "s"(P.wqB), "s"(P.wuk), "s"(P.ckv), "s"(P.ws), "s"(P.out), "s"(P.xA), "s"(P.wqA), "s"(P.kc), "s"(P.vc), "s"(P.woA), "s"(P.woB), "s"(P.wuv));
  const int w4 = wave & 3;
  const int kq_voff = (c16 & 1) * 64 + g * 16;
  const int gk = 2 * x + (j >> 3), bx = (j >> 2) & 1;
  const int ro = isX ? 0 : 128 * SLOT;
  const rsrc_t ws = mk(P.ws, W_END);
  u32 tag, sel0[4], sel1[4];
  auto make_sel = [&]() {
#pragma unroll
    for (int k = 0; k < 4; ++k) {
      const bool on = ((c16 & 7) >> 1) == k;
      sel0[k] = on ? ((c16 & 1) ? 0x01000c0cu : 0x0c0c0100u) : 0x0c0c0c0cu;
      sel1[k] = on ? ((c16 & 1) ? 0x03020c0cu : 0x0c0c0302u) : 0x0c0c0c0cu;
    }
  };
  constexpr int L_QV = 1024, L_R1 = L_FLAG + 64;
  auto gather_q = [&](int gs, int f) {
    const int gv = (lane >> 5) * SLOT + (w4 & 1) * 1024 + (lane & 31) * 16;
    v4u a, c;
    _Pragma("nounroll") for (int spin = 0; spin < SPIN_CAP; ++spin) {
      pin();
      a = ld4<AUX_SC1>(ws, gv, gs); c = ld4<AUX_SC1>(ws, gv, gs + 2 * SLOT);
      if (none(bad4(bad4(0u, a, tag), c, tag))) break;
      __builtin_amdgcn_s_sleep(2);
    }
    const float s0 = uf(a.x) + uf(c.x), s1 = uf(a.z) + uf(c.z);
    const u32 pq = pk(swap32_add(s0, s0), swap32_add(s1, s1));
    if (lane < 32) *(u32*)(smem + L_QV + f * 512 + (w4 >> 1) * 256 + 16 * c16 + 8 * g + 4 * (w4 & 1)) = pq;
    post(L_FLAG + 12 + 4 * f, lane);
  };
  if (__builtin_expect(wave >= 4, 1)) {
    __builtin_amdgcn_s_setprio(2);
    if (T_Y0 && !isX) pace(T_Y0);
    v4u w1[9];
    const rsrc_t rx = mk(isX ? P.xB : P.xA, 2304);
    const rsrc_t rw = mk((const char*)(isX ? P.wqB : P.wqA) + (size_t)(r * 144 + w4 * 36) * 8192 + h * 256, 35 * 8192 + 256);
    const int xv = bsel * 1152 + r * 288 + w4 * 72;
    const v4u xc = ld4<0>(rx, xv + g * 16, 0);
    const u32 xe = __builtin_amdgcn_raw_buffer_load_b16(rx, xv + 64 + g * 2, 0, 0);
    pin();
#pragma unroll
    for (int i = 0; i < 8; ++i) { w1[i] = ld4<AUX_NT>(rw, g * 65536 + c16 * 16, i * 8192); pin(); }
    w1[8] = ld4<AUX_NT>(rw, (32 + g) * 8192 + c16 * 16, 0);
    const u32 ep = __builtin_amdgcn_raw_buffer_load_b32(ws, 0, W_EPOCH, AUX_SC1);
    bar();
    make_sel();
    pin();
    f4 acc = {0.f, 0.f, 0.f, 0.f};
#pragma unroll
    for (int i = 0; i < 4; ++i) { acc = mfma16(diag(xc[i], sel0), w1[2 * i], acc); acc = mfma16(diag(xc[i], sel1), w1[2 * i + 1], acc); }
    acc = mfma16(diag(xe, sel0), w1[8], acc);
    char* rp = smem + L_R1 + lane * 16;
    *(f4*)(rp + w4 * 1024) = acc;
    pin();
    tag = (u32)__builtin_amdgcn_readfirstlane(ep) + 0x9E3779B9u;
    if (arrive(L_FLAG + 48, lane) == 3) {
      acc = (*(const f4*)rp + *(const f4*)(rp + 1024)) + (*(const f4*)(rp + 2048) + *(const f4*)(rp + 3072));
      st4(v4u{fu(acc[0]), tag, fu(acc[1]), tag}, ws, lane * 16, S_H1 + ro + id * SLOT);
      st4(v4u{fu(acc[2]), tag, fu(acc[3]), tag}, ws, lane * 16 + 1024, S_H1 + ro + id * SLOT);
      *(u32*)(smem + L_FLAG + 28) = tag;
      pin();
      *(volatile LAS u32*)(smem + L_FLAG + 52) = 1;
      if (!isX) ST(10);
    }
  }
  if (__builtin_expect(isX, 1)) {
    if (__builtin_expect(wave >= 4, 1)) {
      spin_flag(smem + L_FLAG + 52);
      ST(1);
      __builtin_amdgcn_s_sleep(D_H1);
      _Pragma("nounroll") for (int f = 0; f < 2; ++f) {
        gather_q(S_H1 + (f ? (128 + (2 * gk + (w4 >> 1)) * 4) * SLOT + bx * 512 : h * 4 * SLOT + (w4 >> 1) * 512), f);
        if (f == 0) { ST(2); if (T_A1) pace(T_A1); }
      }
      ST(3);
    } else {
      bar();
      __builtin_amdgcn_s_setprio(3);
      v4u w2[8];
      const rsrc_t ru = mk((const char*)P.wuk + (r * 128 + w4 * 32) * 8192 + h * 256, 31 * 8192 + 256);
      const int voff = (4 * (c16 >> 2) + ((c16 >> 1) & 1)) * 8192 + kq_voff;
#pragma unroll
      for (int i = 0; i < 8; ++i) { w2[i] = ld4<AUX_NT>(ru, voff + (i & 1) * 128, ((i >> 1) & 1) * 16384 + (i >> 2) * 131072); pin(); }
      make_sel();
      spin_flag(smem + L_FLAG + 12, 4);
      tag = __builtin_amdgcn_readfirstlane(*(const u32*)(smem + L_FLAG + 28));
      const v4u q0 = *(const v4u*)(smem + L_QV + bsel * 256 + kq_voff), q1 = *(const v4u*)(smem + L_QV + bsel * 256 + 128 + kq_voff);
      u32 o[4];
#pragma unroll
      for (int be = 0; be < 4; ++be) {
        f4 d = {0.f, 0.f, 0.f, 0.f};
        d = mfma16(w2[2 * be], q0, d); d = mfma16(w2[2 * be + 1], q1, d);
        o[be] = pk(d[0] + dppf<XOR1>(d[1]), d[2] + dppf<XOR1>(d[3]));
      }
      const bool up = c16 & 2;
      if ((c16 & 5) == 0) st4(v4u{up ? o[2] : o[0], tag, up ? o[3] : o[1], tag}, ws, bsel * 512 + (c16 & 2) * 32 + g * 16, S_H2 + id * SLOT + w4 * 128);
      ST(3);
    }
    if (T_GO) pace(T_GO);
  } else if (wave < 4) {
    bar();
    make_sel();
  }
  __builtin_amdgcn_s_setprio(1);
  pin();
  const int hsh = (bid * 2654435761u) >> 20;
  const int rec_voff = 64 * ((lane >> 1) & 15) + 32 * (lane >> 5) + 16 * (lane & 1);
  constexpr int L_RED = 65536, L_REDE = L_RED + 8192;
  v4u wo[4], we[2];
  auto mk_wo = [&]() {
    const char* base = (const char*)(isX ? P.woA : P.woB) + (h * 128 + wave * 16) * 1152;
    const u64 u = ((u64)(u32)__builtin_amdgcn_readfirstlane((u32)((u64)base >> 32)) << 32) | (u32)__builtin_amdgcn_readfirstlane((u32)(u64)base);
    u64 v = u;
    asm volatile("" : "+s"(v));
    return mk((const void*)v, 16 * 1152);
  };
  auto load_wo = [&]() {
    const rsrc_t rwo = mk_wo();
    const int voff = g * 4608 + c16 * 16;
#pragma unroll
    for (int i = 0; i < 4; ++i) { wo[i] = ld4<AUX_NT>(rwo, voff + i * 1152, r * 256); pin(); }
  };
  auto load_we = [&]() {
    if (r == 3) {
      const rsrc_t rwo = mk_wo();
      const int voff = g * 4608 + bsel * 2304 + (c16 & 7) * 16;
#pragma unroll
      for (int i = 0; i < 2; ++i) { we[i] = ld4<AUX_NT>(rwo, voff + i * 1152, 1024); pin(); }
    }
  };
  constexpr int L_R2 = 53248;

  if (isX) {
    constexpr int L_ML = 0;
    v4u kk[16], vv[16];
    {
      const int chunk = SPREAD ? 2 * w4 + (((wave >> 2) ^ (hsh >> (2 + w4))) & 1) : wave;
      const size_t kvoff = ((size_t)(bx * 16 + gk) * 8192 + 6144 + r * 512 + chunk * 64) * 256;
      const int xo = ((SPREAD ? hsh + wave : hsh) & 3) * 4096;
      const rsrc_t rk = mk((const char*)P.kc + kvoff, 16384), rv = mk((const char*)P.vc + kvoff, 16384);
      const int k_voff = ((c16 & 2) * 2 + (c16 >> 2)) * 256 + kq_voff;
      if (WSYNC == 2) load_wo();
#pragma unroll
      for (int i = 0; i < 16; ++i) { kk[i] = ld4<AUX_NT>(rk, k_voff + (i & 1) * 128 + ((i >> 1) & 1) * 2048, ((i >> 2) * 4096) ^ xo); pin(); }
      if (KSYNC && wave >= 4) { post(L_FLAG + 40, lane); spin_flag(smem + L_FLAG + 40, 4); }
#pragma unroll
      for (int i = 0; i < 16; ++i) { vv[i] = ld4<AUX_NT>(rv, lane * 16 + (i & 3) * 1024, ((i >> 2) * 4096) ^ xo); pin(); }
      if (WSYNC == 1) post(L_FLAG + 44, lane);
    }
    ST(4);
    asm volatile("" ::"v"(pfc));
    spin_flag(smem + L_FLAG + 16, 4);
    ST(5);
    float sc[16];
    {
      const v4u q0 = *(const v4u*)(smem + L_QV + 512 + bsel * 256 + kq_voff), q1 = *(const v4u*)(smem + L_QV + 512 + bsel * 256 + 128 + kq_voff);
      const bool odd = c16 & 1;
#pragma unroll
      for (int be = 0; be < 8; ++be) {
        f4 d = {0.f, 0.f, 0.f, 0.f};
        d = mfma16(kk[2 * be], q0, d); d = mfma16(kk[2 * be + 1], q1, d);
        const float u0 = odd ? d[1] : d[0], u1 = odd ? d[3] : d[2];
        sc[2 * be] = u0 + dppf<XOR1>(u0); sc[2 * be + 1] = u1 + dppf<XOR1>(u1);
      }
    }
    float m = mx(mx(mx(sc[0], sc[1], sc[2]), mx(sc[3], sc[4], sc[5]), mx(sc[6], sc[7], sc[8])), mx(mx(sc[9], sc[10], sc[11]), mx(sc[12], sc[13], sc[14]), sc[15]));
    m = swap32_max(swap16_max(m)) * SC;
    float l = 0.f;
#pragma unroll
    for (int be = 0; be < 8; ++be) {
      const float p0 = ex2(fmaf(sc[2 * be], SC, -m)), p1 = ex2(fmaf(sc[2 * be + 1], SC, -m));
      l += p0 + p1;
      const u32 pp = pk(p0, p1);
      kk[2 * be] = diag(pp, sel0); kk[2 * be + 1] = diag(pp, sel1);
    }
    l = swap16_add(l, l); l = swap32_add(l, l);
    if ((lane & 0x37) == 0) { *(float*)(smem + L_ML + bsel * 64 + wave * 4) = m; *(float*)(smem + L_ML + bsel * 64 + 32 + wave * 4) = l; }
    post(L_FLAG + 36, lane);
#pragma unroll
    for (int i = 0; i < 16; i += 4) asm volatile("" : "+v"(kk[i]), "+v"(kk[i + 1]), "+v"(kk[i + 2]), "+v"(kk[i + 3]));
    if (WSYNC == 1) spin_flag(smem + L_FLAG + 44, 8);
    else __builtin_amdgcn_s_setprio(0);
    if (WSYNC != 2) load_wo();
    spin_flag(smem + L_FLAG + 36, 8);
    const char* p = smem + L_ML + (lane >> 5) * 64;
    const f4 m0 = *(const f4*)p, m1 = *(const f4*)(p + 16);
    const float M = mx(mx(mx(m0[0], m0[1]), mx(m0[2], m0[3])), mx(mx(m1[0], m1[1]), mx(m1[2], m1[3])));
    const float fo = ex2(*(const float*)(p + wave * 4) - M);
    float L = ex2(*(const float*)(p + (lane & 7) * 4) - M) * *(const float*)(p + 32 + (lane & 7) * 4);
    pin();
    f4 acc = {0.f, 0.f, 0.f, 0.f};
#pragma unroll
    for (int i = 0; i < 16; ++i) acc = mfma16(kk[i], vv[i], acc);
    *(f4*)(smem + L_R2 + wave * 1024 + lane * 16) = acc * fo;
    ST(6);
    if (wave == 2) {
      L = quad_sum(L); L += dppf<HMIR>(L);
      if ((lane & 24) == 0) st4(v4u{fu(M), tag, fu(L), tag}, ws, (lane >> 5) * 128 + (lane & 7) * 16, S_HS + id * SLOT);
    }
  } else {
    constexpr int L_Q = 0, L_P = 32768, L_MX = 40960, L_LS = 41472, L_LAT = 45056, L_ST = 49152, L_CNT = L_FLAG + 32;
    const int b = id >> 6;
    __builtin_amdgcn_s_waitcnt(0x0070);
    v4u fr[32], wv[4];
    const int r32 = lane & 31, hh = lane >> 5;
    const int row_base = r32 * 1024 + 16 * (hh ^ (((r32 & 3) << 2) | ((r32 >> 2) & 3)));
    const rsrc_t ruv = mk((const char*)P.wuv + (r * 128 + 64 * (wave >> 2) + 8 * w4) * 8192 + h * 256, 39 * 8192 + 256);
#define LDUV(i) wv[i] = ld4<AUX_NT>(ruv, g * 16384 + c16 * 16, (((i) >> 1) + 32 * ((i) & 1)) * 8192)
#define QCNT(rq) (L_FLAG + ((rq) < 3 ? 36 + 4 * (rq) : 56))
    f16v s;
    if (wave < 4) {
      const rsrc_t rc = SPREAD ? mk((const char*)P.ckv + (size_t)id * 131072 + wave * 32768, 32768) : mk((const char*)P.ckv + (size_t)id * 131072 + wave * 8192, 131072 - wave * 8192);
      __builtin_amdgcn_s_waitcnt(0xc07f);
      const int kz = ((hsh >> 3) + wave) & 3;
      const int xi = hsh & 4, lx = lane ^ ((2 * wave + (xi >> 2)) & 3);
#pragma unroll
      for (int i = 0; i < 32; ++i) {
        const int ro = (i >> 3) * 32768 + ((i & 7) ^ xi) * 1024;
        __builtin_amdgcn_raw_ptr_buffer_load_lds(rc, (LAS void*)(smem + wave * 8192 + ro), 16, 16 * (lx ^ (((i & 3) << 2) | ((i >> 2) & 1))), SPREAD ? ((i >> 3) ^ kz) * 8192 + ((i & 7) ^ xi) * 1024 : ro, 0, AUX_NT);
        pin();
        if (i >= WY - 1 && i < NY) asm volatile("s_waitcnt vmcnt(%0)" ::"i"(WY - 1) : "memory");
        if (i == 23 || i == 31) {
          asm volatile("s_waitcnt vmcnt(16)" ::: "memory");
          if (lane == 0) asm volatile("ds_add_u32 %0, %1" ::"v"(L_FLAG + 4 * ((i >> 3) - 2)), "v"(1u) : "memory");
          pin();
        }
      }
      int qa[8], fa[8];
#pragma unroll
      for (int t = 0; t < 8; ++t) { qa[t] = row_base ^ (32 * t); fa[t] = qa[t] + wave * 32768; asm volatile("" : "+v"(qa[t]), "+v"(fa[t])); }
      asm volatile("s_waitcnt vmcnt(8)" ::: "memory");
      post(L_FLAG + 8, lane);
      auto landed = [&]() {
        bar();
        asm volatile("s_waitcnt vmcnt(0)" ::: "memory");
        post(L_FLAG + 12, lane);
        ST(1);
      };
      if (wave == 3) landed();
      spin_flag_asm(L_FLAG + 4 * wave, 4);
#pragma unroll
      for (int t = 0; t < 32; ++t)
        asm volatile("ds_read_b128 %0, %1 offset:%2" : "=v"(fr[t]) : "v"(fa[t & 7]), "i"(256 * (t >> 3)) : "memory");
      pin();
      if (wave != 3) landed();
      __builtin_amdgcn_s_waitcnt(0x0f70);
      asm volatile("" ::"v"(pfc));
      v4u qb[4];
#define QRD(t) (*(const v4u*)(smem + L_Q + qa[(t) & 7] + 256 * ((t) >> 3)))
      spin_flag(smem + L_FLAG + 36, 4);
      ST(2);
      u32 rdy = 0;
#pragma unroll
      for (int t = 0; t < 4; ++t) qb[t] = QRD(t);
#pragma unroll
      for (int i = 0; i < 16; ++i) s[i] = 0.f;
      pin();
#pragma unroll
      for (int t = 0; t < 32; ++t) {
        s = mfma32(fr[t], qb[t & 3], s);
        __builtin_amdgcn_sched_barrier(0);
        if ((t & 7) == 4 && t < 24) {
          if (__builtin_expect((u32)__builtin_amdgcn_readfirstlane(rdy) < 4, 0)) spin_flag(smem + QCNT((t >> 3) + 1), 4);
          pin();
        }
        if ((t & 7) == 1 && t < 24) rdy = *(volatile const LAS u32*)(smem + QCNT((t >> 3) + 1));
        if (t + 4 < 32) qb[t & 3] = QRD(t + 4);
        if (t >= 21 && t < 25) LDUV(t - 21);
        __builtin_amdgcn_sched_barrier(0);
      }
      ST(3);
      float m = mx(mx(mx(s[0], s[1], s[2]), mx(s[3], s[4], s[5]), mx(s[6], s[7], s[8])), mx(mx(s[9], s[10], s[11]), mx(s[12], s[13], s[14]), s[15]));
      m = swap32_max(m);
      if (lane < 32) *(float*)(smem + L_MX + lane * 16 + wave * 4) = m;
    } else {
      const int q = c16 >> 2, p = lane & 3;
      const int base = (4 * hh + q) * 1024 + w4 * 256 + 16 * ((2 * (g & 1) + (p >> 1)) ^ (4 * q + hh)) + 8 * (p & 1);
      auto frags = [&](auto blk) {
        constexpr int kb = decltype(blk)::value;
        spin_flag(smem + L_FLAG + 4 * kb, 4);
#pragma unroll
        for (int ta = 0; ta < 4; ++ta)
#pragma unroll
          for (int sh = 0; sh < 2; ++sh) {
            const char* pa = smem + kb * 32768 + sh * 16384;
            const v2u a = __builtin_bit_cast(v2u, __builtin_amdgcn_ds_read_tr16_b64_v4i16((LAS s4v*)(pa + (base ^ (64 * ta)))));
            const v2u c = __builtin_bit_cast(v2u, __builtin_amdgcn_ds_read_tr16_b64_v4i16((LAS s4v*)(pa + (base ^ (64 * ta) ^ 32) + 8192)));
            fr[ta * 8 + 2 * kb + sh] = v4u{a.x, a.y, c.x, c.y};
          }
        pin();
      };
      v4u qg[16];
      int qvo = S_H2 + (32 * w4 + 4 * hh) * SLOT + b * 512 + r32 * 16;
      asm volatile("" : "+v"(qvo));
#define LDQ(rq, i) qg[4 * (rq) + (i)] = ld4<AUX_SC1>(ws, qvo, (8 * (i) + (rq)) * SLOT)
      int qw[4];
#pragma unroll
      for (int i = 0; i < 4; ++i) {
        const int hd = 8 * w4 + 2 * i + hh;
        qw[i] = hd * 1024 + 16 * ((r32 >> 1) ^ (((hd & 3) << 2) | ((hd >> 2) & 3))) + 8 * (lane & 1);
        asm volatile("" : "+v"(qw[i]));
      }
      frags(std::integral_constant<int, 0>{});
      frags(std::integral_constant<int, 1>{});
      frags(std::integral_constant<int, 2>{});
      if (!EARLYQ) { bar(); spin_flag(smem + L_FLAG + 12, 4); }
#pragma unroll
      for (int i = 0; i < 16; ++i) { LDQ(i >> 2, i & 3); pin(); }
      if (EARLYQ) bar();
#pragma unroll
      for (int rq = 0; rq < 4; ++rq) {
        u32 bad = 0;
#pragma unroll
        for (int i = 0; i < 4; ++i) bad = bad4(bad, qg[4 * rq + i], tag);
        if (__builtin_expect(!none(bad), 0)) {
          _Pragma("nounroll") for (int spin = 0; spin < SPIN_CAP; ++spin) {
            pin();
            bad = 0;
#pragma unroll
            for (int i = 0; i < 4; ++i) LDQ(rq, i);
#pragma unroll
            for (int i = 0; i < 4; ++i) bad = bad4(bad, qg[4 * rq + i], tag);
            if (none(bad)) break;
            __builtin_amdgcn_s_sleep(4);
          }
        }
#pragma unroll
        for (int i = 0; i < 4; ++i) *(v2u*)(smem + L_Q + qw[i] + 256 * rq) = v2u{qg[4 * rq + i].x, qg[4 * rq + i].z};
        post(QCNT(rq), lane);
      }
      ST(3);
#pragma unroll
      for (int i = 0; i < 4; ++i) { LDUV(i); pin(); }
      frags(std::integral_constant<int, 3>{});
    }
    bar();
    if (wave < 4) {
      const f4 mm = *(const f4*)(smem + L_MX + r32 * 16);
      const float m = mx(mx(mm[0], mm[1]), mx(mm[2], mm[3])) * SC;
      u32 pp[8];
#pragma unroll
      for (int i = 0; i < 8; ++i) {
        s[2 * i] = ex2(fmaf(s[2 * i], SC, -m)); s[2 * i + 1] = ex2(fmaf(s[2 * i + 1], SC, -m));
        pp[i] = pk(s[2 * i], s[2 * i + 1]);
      }
      *(v4u*)(smem + L_P + wave * 2048 + lane * 16) = v4u{pp[0], pp[1], pp[2], pp[3]};
      *(v4u*)(smem + L_P + wave * 2048 + 1024 + lane * 16) = v4u{pp[4], pp[5], pp[6], pp[7]};
    }
    bar();
    pin();
    if (wave < 4) {
      float l = 0.f;
#pragma unroll
      for (int i = 0; i < 8; ++i) l += s[2 * i] + s[2 * i + 1];
      l = swap32_add(l, l);
      if (lane < 32) *(float*)(smem + L_LS + lane * 16 + wave * 4) = l;
      tag = __builtin_amdgcn_readfirstlane(*(const u32*)(smem + L_FLAG + 28));
    }
    ST(4);
    if (ABL == 2) { load_wo(); load_we(); asm volatile("" ::"v"(wo[0]), "v"(wo[1]), "v"(wo[2]), "v"(wo[3]), "v"(wv[0]), "v"(wv[1]), "v"(wv[2]), "v"(wv[3])); goto done; }
    if (wave >= 4) {
      v4u pf[8];
#pragma unroll
      for (int ks = 0; ks < 8; ++ks) pf[ks] = *(const v4u*)(smem + L_P + ks * 1024 + lane * 16);
      f16v o0, o1, o2, o3;
#pragma unroll
      for (int i = 0; i < 16; ++i) { o0[i] = 0.f; o1[i] = 0.f; o2[i] = 0.f; o3[i] = 0.f; }
      const int voff = hh * 524288 + r32 * 4, soff = W_H3D + (wave - 4) * 32768 + id * 256;
      pin();
#pragma unroll
      for (int ks = 0; ks < 8; ++ks) { o0 = mfma32(pf[ks], fr[ks], o0); o1 = mfma32(pf[ks], fr[8 + ks], o1); }
      __builtin_amdgcn_sched_barrier(0);
#define OST(a, c, i, pi) __builtin_amdgcn_raw_buffer_store_b32(pk(a[i], c[i]), ws, voff + (pi) * 128, soff + (((i) & 3) + 8 * ((i) >> 2)) * 131072, AUX_SC1)
#pragma unroll
      for (int ks = 0; ks < 8; ++ks) {
        o2 = mfma32(pf[ks], fr[16 + ks], o2);
        __builtin_amdgcn_sched_barrier(0);
        if (ks) OST(o0, o1, 2 * ks - 2, 0);
        __builtin_amdgcn_sched_barrier(0);
        o3 = mfma32(pf[ks], fr[24 + ks], o3);
        __builtin_amdgcn_sched_barrier(0);
        if (ks) OST(o0, o1, 2 * ks - 1, 0);
        __builtin_amdgcn_sched_barrier(0);
      }
      OST(o0, o1, 14, 0); OST(o0, o1, 15, 0);
#pragma unroll
      for (int i = 0; i < 16; ++i) OST(o2, o3, i, 1);
    }
    bar();
    ST(5);
    if (wave >= 4) {
      __builtin_amdgcn_s_setprio(3);
      const int hd = (lane >> 3) * 4 + (lane & 3);
      const f4 mm = *(const f4*)(smem + L_MX + hd * 16), ll = *(const f4*)(smem + L_LS + hd * 16);
      v4u sv = {fu(mx(mx(mm[0], mm[1]), mx(mm[2], mm[3])) * SC), tag, fu((ll[0] + ll[1]) + (ll[2] + ll[3])), tag};
      asm volatile("s_waitcnt vmcnt(0)" : "+v"(sv)::"memory");
      if (arrive(L_CNT, lane) == 3) st4(sv, ws, lane * 16, S_HS + (128 + id) * SLOT);
      __builtin_amdgcn_s_setprio(0);
      ST(6);
    }
    pin();
    if (!WO_LATE) load_wo();
    if (ABL == 3) { asm volatile("" ::"v"(wo[0]), "v"(wo[1]), "v"(wo[2]), "v"(wo[3]), "v"(wv[0]), "v"(wv[1]), "v"(wv[2]), "v"(wv[3])); goto done; }
    if (wave < 4) __builtin_amdgcn_s_sleep(D_H3 + 14); else __builtin_amdgcn_s_sleep(D_H3);
    {
      v4u st;
      _Pragma("nounroll") for (int spin = 0; spin < SPIN_CAP; ++spin) {
        pin();
        st = ld4<AUX_SC1>(ws, (lane >> 2) * SLOT, S_HS + (128 + wave * 16) * SLOT + x * 128 + (h & 3) * 16);
        if (none(bad4(0u, st, tag))) break;
        __builtin_amdgcn_s_sleep(P_H3);
      }
      pin();
      ST(7);
      v4u d[4];
#pragma unroll
      for (int i = 0; i < 4; ++i) d[i] = ld4<AUX_SC1>(ws, g * 1024 + c16 * 16 + i * 256, W_H3D + id * 32768 + wave * 4096);
      pin();
      float M = uf(st.x);
      M = mx(M, dppf<ROR4>(M)); M = mx(M, dppf<ROR8>(M)); M = swap32_max(swap16_max(M));
      float wg = ex2(uf(st.x) - M);
      float L = wg * uf(st.z);
      L += dppf<ROR4>(L); L += dppf<ROR8>(L); L = swap16_add(L, L); L = swap32_add(L, L);
      if (lane == 0) *(f2*)(smem + L_ST + wave * 8) = f2{M, L};
      bar();
      const f4 s0 = *(const f4*)(smem + L_ST + (wave >> 2) * 32), s1 = *(const f4*)(smem + L_ST + (wave >> 2) * 32 + 16);
      const float MM = mx(mx(s0[0], s0[2], s1[0]), s1[2]);
      const float LL = fmaf(ex2(s0[0] - MM), s0[1], fmaf(ex2(s0[2] - MM), s0[3], fmaf(ex2(s1[0] - MM), s1[1], ex2(s1[2] - MM) * s1[3])));
      wg *= ex2(M - MM) * __builtin_amdgcn_rcpf(LL);
      const u32 w01 = pk(dppf<BCAST>(wg), dppf<BCAST + 4>(wg)), w23 = pk(dppf<BCAST + 8>(wg), dppf<BCAST + 12>(wg));
      v4u af[4] = {diag(w01, sel0), diag(w01, sel1), diag(w23, sel0), diag(w23, sel1)};
      asm volatile("" : "+v"(af[0]), "+v"(af[1]), "+v"(af[2]), "+v"(af[3]));
      pin();
      if (WO_LATE == 3) load_wo();
      f4 lat = {0.f, 0.f, 0.f, 0.f};
#pragma unroll
      for (int i = 0; i < 4; ++i) lat = mfma16(af[i], d[i], lat);
      if (lane < 32) *(f4*)(smem + L_LAT + wave * 512 + c16 * 32 + g * 16) = lat;
      pin();
    }
    bar();
    if (WO_LATE == 2) load_wo();
    {
      const char* p = smem + L_LAT + bsel * 2048 + wave * 64 + g * 16;
      const f4 c = (*(const f4*)p + *(const f4*)(p + 512)) + (*(const f4*)(p + 1024) + *(const f4*)(p + 1536));
      *(f4*)(smem + L_R2 + wave * 1024 + lane * 16) = rows16(pk(c[0], c[1]), pk(c[2], c[3]), wv, sel0, sel1);
    }
  }
  {
  bar();
  if (isX) ST(7);
  if (wave < 2) {
    const f2 sv = sum8(smem + L_R2 + tid * 8);
    st4(v4u{fu(sv.x), tag, fu(sv.y), tag}, ws, rec_voff, S_HD + ro + id * SLOT + wave * 1024);
  }
  pin();
  load_we();
  const int gsv = (c16 & 3) * SLOT + bsel * (isX ? 4 * SLOT : 1024), gso = isX ? (id & ~7) * SLOT : (128 + (id & ~3)) * SLOT;
  const int dv = gsv + g * 32 + (c16 & 4) * 4, dso = S_HD + gso + (isX ? (h & 1) * 1024 : 0) + wave * 128;
  if (isX) __builtin_amdgcn_s_sleep(D_A3); else __builtin_amdgcn_s_sleep(D_H4);
  ST(8);
  {
    v4u d0 = ld4<AUX_SC1>(ws, dv, dso);
    pin();
    float w = 1.f;
    if (isX) {
      v4u st;
      _Pragma("nounroll") for (int spin = 0; spin < SPIN_CAP; ++spin) {
        pin();
        st = ld4<AUX_SC1>(ws, gsv, S_HS + gso + (h & 1) * 128);
        if (none(bad4(0u, st, tag))) break;
        __builtin_amdgcn_s_sleep(4);
      }
      float M = uf(st.x);
      M = mx(M, dppf<XOR1>(M)); M = mx(M, dppf<XOR2>(M));
      w = ex2(uf(st.x) - M);
      w *= __builtin_amdgcn_rcpf(quad_sum(w * uf(st.z)));
      asm volatile("" : "+v"(w));
    }
    _Pragma("nounroll") for (int spin = 0; spin < SPIN_CAP; ++spin) {
      if (none(bad4(0u, d0, tag))) break;
      pin();
      d0 = ld4<AUX_SC1>(ws, dv, dso);
      if (P_HD) __builtin_amdgcn_s_sleep(P_HD);
    }
    ST(9);
    const u32 cm = pk(quad_sum(w * uf(d0.x)), quad_sum(w * uf(d0.z))), co = dppu<HMIR>(cm);
    const u32 c01 = (c16 & 4) ? co : cm, c23 = (c16 & 4) ? cm : co;
    *(f4*)(smem + L_RED + wave * 1024 + lane * 16) = rows16(c01, c23, wo, sel0, sel1);
    if (r == 3) {
      const f4 z = {0.f, 0.f, 0.f, 0.f};
      const f4 lo = mfma16(diag(c01, sel1), we[1], mfma16(diag(c01, sel0), we[0], z)), hi = mfma16(diag(c23, sel1), we[1], mfma16(diag(c23, sel0), we[0], z));
      *(f4*)(smem + L_REDE + wave * 1024 + lane * 16) = f4{lo[0] + dppf<ROR8>(hi[0]), lo[1] + dppf<ROR8>(hi[1]), lo[2] + dppf<ROR8>(hi[2]), lo[3] + dppf<ROR8>(hi[3])};
    }
  }
  bar();
  if (wave < 4) {
    if (wave < 2 || (r == 3 && (lane & 16) == 0)) {
      const f2 sv = sum8(smem + L_RED + (wave >> 1) * 8192 + (tid & 127) * 8);
      st4(v4u{fu(sv.x), tag, fu(sv.y), tag}, ws, lane * 16, S_H5 + ro + id * SLOT + wave * 1024);
    }
    if (isX) ST(10);
  } else if (wave == 4 && id < 72) {
    const int e = id - 64, L = id < 64 ? id : 64 + (e & 1) + 4 * (e >> 1);
    const int rso = S_H5 + ro + (id < 64 ? (L >> 4) * SLOT + (L & 15) * 128 : 3 * SLOT + (L - 48) * 128);
    v4u v[4];
    __builtin_amdgcn_s_sleep(D_H5);
    _Pragma("nounroll") for (int spin = 0; spin < SPIN_CAP; ++spin) {
      pin();
      u32 bad = 0;
#pragma unroll
      for (int i = 0; i < 4; ++i) v[i] = ld4<AUX_SC1>(ws, (lane >> 3) * 4 * SLOT + (lane & 7) * 16, rso + i * 32 * SLOT);
#pragma unroll
      for (int i = 0; i < 4; ++i) bad = bad4(bad, v[i], tag);
      if (none(bad)) break;
      __builtin_amdgcn_s_sleep(2);
    }
    float a = (uf(v[0].x) + uf(v[1].x)) + (uf(v[2].x) + uf(v[3].x)), c = (uf(v[0].z) + uf(v[1].z)) + (uf(v[2].z) + uf(v[3].z));
    a += dppf<ROR8>(a); c += dppf<ROR8>(c);
    a = swap16_add(a, a); c = swap16_add(c, c);
    a = swap32_add(a, a); c = swap32_add(c, c);
    if (lane < 8) {
      const int al = L & 15;
      const int n = (al >> 3) * 1152 + (isX ? 0 : 576) + 128 * (L >> 4) + 32 * (al & 3) + 8 * (lane >> 1) + 4 * ((al >> 2) & 1) + 2 * (lane & 1);
      ((u32*)P.out)[n >> 1] = pk(a, c);
    }
    if (!isX && id == 0 && lane == 0) __builtin_amdgcn_raw_buffer_store_b32(tag, ws, 0, W_EPOCH, AUX_SC1);
  }
  }
done:;
  ST(11);
  if (lane == 0) {
    u64* sp = (u64*)((char*)P.ws + W_STAMP) + (bid * 8 + wave) * 12;
#pragma unroll
    for (int i = 0; i < 12; ++i) __hip_atomic_store(sp + i, ts[i], __ATOMIC_RELAXED, __HIP_MEMORY_SCOPE_AGENT);
  }
  if (bid == 0 && wave == 7) {
    while (__builtin_amdgcn_s_memrealtime() - ts[0] < 7000) __builtin_amdgcn_s_sleep(8);
    u64* sp = (u64*)((char*)P.ws + W_STAMP);
    u64 mn = ~0ull;
    for (int e = lane; e < 2048; e += 64) { u64 v = __hip_atomic_load(sp + e * 12, __ATOMIC_RELAXED, __HIP_MEMORY_SCOPE_AGENT); mn = v < mn ? v : mn; }
    for (int o = 32; o; o >>= 1) { u64 t = __shfl_xor(mn, o); mn = t < mn ? t : mn; }
    float sum = 0.f, cnt = 0.f, mx = 0.f;
    for (int e = lane; e < 2048; e += 64) {
      u64 v = __hip_atomic_load(sp + e * 12 + REP_IDX, __ATOMIC_RELAXED, __HIP_MEMORY_SCOPE_AGENT);
      const int role = (e >> 6) >= 16;
      if (v && (REP_ROLE == 2 || role == REP_ROLE)) { float d = (float)(v - mn); sum += d; cnt += 1.f; mx = fmaxf(mx, d); }
    }
    for (int o = 32; o; o >>= 1) { sum += __shfl_xor(sum, o); cnt += __shfl_xor(cnt, o); mx = fmaxf(mx, __shfl_xor(mx, o)); }
    float q = REP_STAT == 0 ? 0.f : REP_STAT == 1 ? sum / cnt : mx;
    q = fminf(q, 6000.f);
    const u64 until = mn + 10000 + (u64)(4.f * q);
    while (__builtin_amdgcn_s_memrealtime() < until) __builtin_amdgcn_s_sleep(1);
  }
}

extern "C" void kernel_launch(void* const* d_in, const int* in_sizes, int n_in, void* d_out, int out_size, void* d_ws, size_t ws_size,
                              hipStream_t stream) {
  Params p;
  char* w = (char*)d_ws + (WS_AT_END ? ((ws_size - (18 << 20)) & ~(size_t)0x1fffff) : 0);
  p.xA = d_in[0]; p.kc = d_in[1]; p.vc = d_in[2]; p.wqA = d_in[3]; p.woA = d_in[4];
  p.xB = d_in[5]; p.ckv = d_in[6]; p.wqB = d_in[7]; p.wuk = d_in[8]; p.wuv = d_in[9]; p.woB = d_in[10];
  p.out = d_out; p.ws = w;
  fused_decode<<<256, 512, 0, stream>>>(p);
}
```

```cpp
#define STAMP 1
#define REP_IDX 4
#define REP_ROLE 1
#define REP_STAT 1
#define SMASK 2065
#include <hip/hip_runtime.h>
#include <hip/hip_bf16.h>
#include <stdint.h>
#include <type_traits>

typedef unsigned u32;
typedef unsigned long long u64;
typedef u32 v4u __attribute__((ext_vector_type(4)));
typedef u32 v2u __attribute__((ext_vector_type(2)));
typedef float f2 __attribute__((ext_vector_type(2)));
typedef float f4 __attribute__((ext_vector_type(4)));
typedef float f16v __attribute__((ext_vector_type(16)));
typedef __bf16 bf8 __attribute__((ext_vector_type(8)));
typedef __bf16 bf2 __attribute__((ext_vector_type(2)));
typedef short s4v __attribute__((ext_vector_type(4)));
typedef __amdgpu_buffer_rsrc_t rsrc_t;
#define LAS __attribute__((address_space(3)))
#define DI __device__ __forceinline__

constexpr int AUX_NT = 2, AUX_SC1 = 16;
constexpr int W_EPOCH = 0;
constexpr int SLOT = 16896, W_SLOT = 4096;
constexpr int S_H1 = W_SLOT;
constexpr int S_HD = W_SLOT + 2048;
constexpr int S_H5 = W_SLOT + 4096;
constexpr int S_HS = W_SLOT + 8192;
constexpr int S_H2 = W_SLOT + 9216;
constexpr int S_H1A = W_SLOT + 10240;
constexpr int W_H3D = 5 << 20;
constexpr int W_END = W_H3D + 4194304;
constexpr int W_STAMP = 16 << 20;
constexpr float SC = 0.08838834764831845f * 1.4426950408889634f;
#define KNOB(n, v) constexpr int n = v
#define K_D_H1 14
#define K_WX 2
#define K_NX 12
#define K_WY 2
#define K_NY 0
#define K_DXA 20
#define K_P_H3 6
#define P_HD 2
#define K_D_A3 12
#define K_D_H3 30
#define K_D_H4 14
#define K_D_H5 14
#define EARLYQ 1
#define K_D_H2 0
#define WS_AT_END 0
#define SPREAD 1
#define WE_EARLY 0
#define KSYNC 1
#define WSYNC 1
#define T_GO 0
#define T_A1 0
#define T_Y0 0
#define D_H1A 16
#define WO_LATE 3
#define K_DX0 0
#define ABL 0
#define CODE_BYTES 18176
constexpr int D_H1 = K_D_H1, WX = K_WX, NX = K_NX, WY = K_WY, NY = K_NY, DXA = K_DXA, P_H3 = K_P_H3, D_A3 = K_D_A3, D_H3 = K_D_H3, D_H4 = K_D_H4,
              D_H5 = K_D_H5, D_H2 = K_D_H2;
constexpr int SPIN_CAP = 1 << 17;

struct Params {
  const void *xB, *wqB, *wuk, *ckv;
  void *ws, *out;
  const void *xA, *wqA, *kc, *vc, *woA, *woB, *wuv;
};

DI rsrc_t mk(const void* p, u32 bytes) { return __builtin_amdgcn_make_buffer_rsrc((void*)p, 0, bytes, 0x00020000); }
template <int AUX> DI v4u ld4(rsrc_t r, int voff, int soff) { return __builtin_amdgcn_raw_buffer_load_b128(r, voff, soff, AUX); }
template <int AUX> DI v2u ld2(rsrc_t r, int voff, int soff) { return __builtin_amdgcn_raw_buffer_load_b64(r, voff, soff, AUX); }
DI void st4(v4u v, rsrc_t r, int voff, int soff) { __builtin_amdgcn_raw_buffer_store_b128(v, r, voff + soff, 0, AUX_SC1); }
DI u32 fu(float f) { return __builtin_bit_cast(u32, f); }
DI float uf(u32 u) { return __builtin_bit_cast(float, u); }
DI u32 pk(float lo, float hi) { f2 f = {lo, hi}; return __builtin_bit_cast(u32, __builtin_convertvector(f, bf2)); }
DI float blo(u32 u) { return uf(u << 16); }
DI float bhi(u32 u) { return uf(u & 0xffff0000u); }
template <int C> DI u32 dppu(u32 x) { return __builtin_amdgcn_mov_dpp(x, C, 0xf, 0xf, true); }
template <int C> DI float dppf(float x) { return uf(dppu<C>(fu(x))); }
constexpr int XOR1 = 0xB1, XOR2 = 0x4E, HMIR = 0x141, ROR4 = 0x124, ROR8 = 0x128, BCAST = 0x150;
DI f4 mfma16(v4u a, v4u b, f4 c) { return __builtin_amdgcn_mfma_f32_16x16x32_bf16(__builtin_bit_cast(bf8, a), __builtin_bit_cast(bf8, b), c, 0, 0, 0); }
DI f16v mfma32(v4u a, v4u b, f16v c) { return __builtin_amdgcn_mfma_f32_32x32x16_bf16(__builtin_bit_cast(bf8, a), __builtin_bit_cast(bf8, b), c, 0, 0, 0); }
DI float mx(float a, float b) { return __builtin_elementwise_maximum(a, b); }
DI float mx(float a, float b, float c) { return mx(mx(a, b), c); }
DI float mx4(f4 v) { return mx(mx(v[0], v[1], v[2]), v[3]); }
DI float ex2(float x) { return __builtin_amdgcn_exp2f(x); }
DI float swap32_add(float a, float b) { auto s = __builtin_amdgcn_permlane32_swap(fu(a), fu(b), false, false); return uf(s[0]) + uf(s[1]); }
DI float swap16_add(float a, float b) { auto s = __builtin_amdgcn_permlane16_swap(fu(a), fu(b), false, false); return uf(s[0]) + uf(s[1]); }
DI float swap32_max(float a) { auto s = __builtin_amdgcn_permlane32_swap(fu(a), fu(a), false, false); return mx(uf(s[0]), uf(s[1])); }
DI float swap16_max(float a) { auto s = __builtin_amdgcn_permlane16_swap(fu(a), fu(a), false, false); return mx(uf(s[0]), uf(s[1])); }
DI void bar() {
  __builtin_amdgcn_sched_barrier(0);
  asm volatile("s_waitcnt lgkmcnt(0)\n\ts_barrier" ::: "memory");
  __builtin_amdgcn_sched_barrier(0);
}
DI void pin() { asm volatile("" ::: "memory"); __builtin_amdgcn_sched_barrier(0); }
DI u32 bad4(u32 acc, v4u v, u32 tag) {
  return __builtin_amdgcn_bitop3_b32(__builtin_amdgcn_bitop3_b32(acc, v.y, tag, 0xF6), v.w, tag, 0xF6);
}
DI bool none(u32 bad) { return __builtin_amdgcn_ballot_w64(bad != 0) == 0; }
DI v4u diag(u32 src, const u32 (&sel)[4]) {
  v4u a;
#pragma unroll
  for (int k = 0; k < 4; ++k) a[k] = __builtin_amdgcn_perm(src, src, sel[k]);
  return a;
}

#define ST(i) do { if ((SMASK >> (i)) & 1) asm volatile("s_memrealtime %0\n\ts_waitcnt lgkmcnt(0)" : "=s"(ts[i]) :: "memory"); } while (0)

DI void spin_flag(const char* p, u32 want = 1) {
  _Pragma("nounroll") for (int spin = 0; spin < SPIN_CAP * 8; ++spin) {
    if ((u32)__builtin_amdgcn_readfirstlane(*(volatile const LAS u32*)p) >= want) break;
    __builtin_amdgcn_s_sleep(1);
  }
  pin();
}
DI u32 arrive(u32 addr, int lane) {
  u32 old = 0;
  if (lane == 0) asm volatile("ds_add_rtn_u32 %0, %1, %2\n\ts_waitcnt lgkmcnt(0)" : "=v"(old) : "v"(addr), "v"(1u) : "memory");
  old = __builtin_amdgcn_readfirstlane(old);
  pin();
  return old;
}
DI void post(u32 addr, int lane) {
  if (lane == 0) asm volatile("ds_add_u32 %0, %1" ::"v"(addr), "v"(1u) : "memory");
  pin();
}
DI void spin_flag_asm(u32 addr, u32 want) {
  _Pragma("nounroll") for (int spin = 0; spin < SPIN_CAP * 8; ++spin) {
    u32 v;
    asm volatile("ds_read_b32 %0, %1\n\ts_waitcnt lgkmcnt(0)" : "=v"(v) : "v"(addr) : "memory");
    if ((u32)__builtin_amdgcn_readfirstlane(v) >= want) break;
    __builtin_amdgcn_s_sleep(1);
  }
  pin();
}
DI float quad_sum(float v) { v += dppf<XOR1>(v); return v + dppf<XOR2>(v); }
DI f2 sum8(const char* p) {
  f2 s = *(const f2*)p;
#pragma unroll
  for (int w = 1; w < 8; ++w) s += *(const f2*)(p + w * 1024);
  return s;
}
DI f4 rows16(u32 c01, u32 c23, const v4u (&w)[4], const u32 (&sel0)[4], const u32 (&sel1)[4]) {
  f4 acc = {0.f, 0.f, 0.f, 0.f};
  acc = mfma16(diag(c01, sel0), w[0], acc); acc = mfma16(diag(c01, sel1), w[1], acc);
  acc = mfma16(diag(c23, sel0), w[2], acc); acc = mfma16(diag(c23, sel1), w[3], acc);
  return acc;
}

__global__ void __launch_bounds__(512, 2) fused_decode(Params P) {
  constexpr int L_FLAG = 131072;
  __shared__ __attribute__((aligned(16))) char smem[L_FLAG + 64 + 4096];
  u64 ts[12];
#pragma unroll
  for (int i = 0; i < 12; ++i) ts[i] = 0;
  ST(0);
  const u32 t_in = (u32)__builtin_amdgcn_s_memrealtime();
  auto pace = [&](u32 t) { while ((u32)__builtin_amdgcn_s_memrealtime() - t_in < t) __builtin_amdgcn_s_sleep(1); pin(); };
  const int tid = threadIdx.x, lane = tid & 63, wave = __builtin_amdgcn_readfirstlane(tid >> 6);
  const int bid = blockIdx.x, x = bid & 7, jj = bid >> 3, j = jj & 15;
  const bool isX = jj < 16;
  const int g = lane >> 4, c16 = lane & 15, bsel = c16 >> 3;
  const int h = 4 * x + (j >> 2), r = j & 3, id = x * 16 + j;
  u32 pfc = 0;
  if (tid < 16) {
    u64 pc;
    asm volatile("s_getpc_b64 %0" : "=s"(pc));
    pfc = __builtin_amdgcn_raw_buffer_load_b32(mk((const void*)(pc & ~255ull), CODE_BYTES), tid * 64, jj * 1024, 0);
  }
  if (tid < 16) *(u32*)(smem + L_FLAG + tid * 4) = 0u;
  asm volatile("" ::"s"(P.xB), "s"(P.wqB), "s"(P.wuk), "s"(P.ckv), "s"(P.ws), "s"(P.out), "s"(P.xA), "s"(P.wqA), "s"(P.kc), "s"(P.vc), "s"(P.woA), "s"(P.woB), "s"(P.wuv));
  const int w4 = wave & 3;
  const int kq_voff = (c16 & 1) * 64 + g * 16;
  const int gk = 2 * x + (j >> 3), bx = (j >> 2) & 1;
  const int ro = isX ? 0 : 128 * SLOT;
  const rsrc_t ws = mk(P.ws, W_END);
  u32 tag, sel0[4], sel1[4];
  auto make_sel = [&]() {
#pragma unroll
    for (int k = 0; k < 4; ++k) {
      const bool on = ((c16 & 7) >> 1) == k;
      sel0[k] = on ? ((c16 & 1) ? 0x01000c0cu : 0x0c0c0100u) : 0x0c0c0c0cu;
      sel1[k] = on ? ((c16 & 1) ? 0x03020c0cu : 0x0c0c0302u) : 0x0c0c0c0cu;
    }
  };
  constexpr int L_QV = 1024, L_R1 = L_FLAG + 64;
  auto gather_q = [&](int gs, int f) {
    const int gv = (lane >> 5) * SLOT + (w4 & 1) * 1024 + (lane & 31) * 16;
    v4u a, c;
    _Pragma("nounroll") for (int spin = 0; spin < SPIN_CAP; ++spin) {
      pin();
      a = ld4<AUX_SC1>(ws, gv, gs); c = ld4<AUX_SC1>(ws, gv, gs + 2 * SLOT);
      if (none(bad4(bad4(0u, a, tag), c, tag))) break;
      __builtin_amdgcn_s_sleep(2);
    }
    const float s0 = uf(a.x) + uf(c.x), s1 = uf(a.z) + uf(c.z);
    const u32 pq = pk(swap32_add(s0, s0), swap32_add(s1, s1));
    if (lane < 32) *(u32*)(smem + L_QV + f * 512 + (w4 >> 1) * 256 + 16 * c16 + 8 * g + 4 * (w4 & 1)) = pq;
    post(L_FLAG + 12 + 4 * f, lane);
  };
  if (__builtin_expect(wave >= 4, 1)) {
    __builtin_amdgcn_s_setprio(2);
    if (T_Y0 && !isX) pace(T_Y0);
    v4u w1[9];
    const rsrc_t rx = mk(isX ? P.xB : P.xA, 2304);
    const rsrc_t rw = mk((const char*)(isX ? P.wqB : P.wqA) + (size_t)(r * 144 + w4 * 36) * 8192 + h * 256, 35 * 8192 + 256);
    const int xv = bsel * 1152 + r * 288 + w4 * 72;
    const v4u xc = ld4<0>(rx, xv + g * 16, 0);
    const u32 xe = __builtin_amdgcn_raw_buffer_load_b16(rx, xv + 64 + g * 2, 0, 0);
    pin();
#pragma unroll
    for (int i = 0; i < 8; ++i) { w1[i] = ld4<AUX_NT>(rw, g * 65536 + c16 * 16, i * 8192); pin(); }
    w1[8] = ld4<AUX_NT>(rw, (32 + g) * 8192 + c16 * 16, 0);
    const u32 ep = __builtin_amdgcn_raw_buffer_load_b32(ws, 0, W_EPOCH, AUX_SC1);
    bar();
    make_sel();
    pin();
    f4 acc = {0.f, 0.f, 0.f, 0.f};
#pragma unroll
    for (int i = 0; i < 4; ++i) { acc = mfma16(diag(xc[i], sel0), w1[2 * i], acc); acc = mfma16(diag(xc[i], sel1), w1[2 * i + 1], acc); }
    acc = mfma16(diag(xe, sel0), w1[8], acc);
    char* rp = smem + L_R1 + lane * 16;
    *(f4*)(rp + w4 * 1024) = acc;
    pin();
    tag = (u32)__builtin_amdgcn_readfirstlane(ep) + 0x9E3779B9u;
    if (arrive(L_FLAG + 48, lane) == 3) {
      acc = (*(const f4*)rp + *(const f4*)(rp + 1024)) + (*(const f4*)(rp + 2048) + *(const f4*)(rp + 3072));
      st4(v4u{fu(acc[0]), tag, fu(acc[1]), tag}, ws, lane * 16, S_H1 + ro + id * SLOT);
      st4(v4u{fu(acc[2]), tag, fu(acc[3]), tag}, ws, lane * 16 + 1024, S_H1 + ro + id * SLOT);
      *(u32*)(smem + L_FLAG + 28) = tag;
      pin();
      *(volatile LAS u32*)(smem + L_FLAG + 52) = 1;
      if (!isX) ST(10);
    }
  }
  if (__builtin_expect(isX, 1)) {
    if (__builtin_expect(wave >= 4, 1)) {
      spin_flag(smem + L_FLAG + 52);
      ST(1);
      __builtin_amdgcn_s_sleep(D_H1);
      _Pragma("nounroll") for (int f = 0; f < 2; ++f) {
        gather_q(S_H1 + (f ? (128 + (2 * gk + (w4 >> 1)) * 4) * SLOT + bx * 512 : h * 4 * SLOT + (w4 >> 1) * 512), f);
        if (f == 0) { ST(2); if (T_A1) pace(T_A1); }
      }
      ST(3);
    } else {
      bar();
      __builtin_amdgcn_s_setprio(3);
      v4u w2[8];
      const rsrc_t ru = mk((const char*)P.wuk + (r * 128 + w4 * 32) * 8192 + h * 256, 31 * 8192 + 256);
      const int voff = (4 * (c16 >> 2) + ((c16 >> 1) & 1)) * 8192 + kq_voff;
#pragma unroll
      for (int i = 0; i < 8; ++i) { w2[i] = ld4<AUX_NT>(ru, voff + (i & 1) * 128, ((i >> 1) & 1) * 16384 + (i >> 2) * 131072); pin(); }
      make_sel();
      spin_flag(smem + L_FLAG + 12, 4);
      tag = __builtin_amdgcn_readfirstlane(*(const u32*)(smem + L_FLAG + 28));
      const v4u q0 = *(const v4u*)(smem + L_QV + bsel * 256 + kq_voff), q1 = *(const v4u*)(smem + L_QV + bsel * 256 + 128 + kq_voff);
      u32 o[4];
#pragma unroll
      for (int be = 0; be < 4; ++be) {
        f4 d = {0.f, 0.f, 0.f, 0.f};
        d = mfma16(w2[2 * be], q0, d); d = mfma16(w2[2 * be + 1], q1, d);
        o[be] = pk(d[0] + dppf<XOR1>(d[1]), d[2] + dppf<XOR1>(d[3]));
      }
      const bool up = c16 & 2;
      if ((c16 & 5) == 0) st4(v4u{up ? o[2] : o[0], tag, up ? o[3] : o[1], tag}, ws, bsel * 512 + (c16 & 2) * 32 + g * 16, S_H2 + id * SLOT + w4 * 128);
      ST(3);
    }
    if (T_GO) pace(T_GO);
  } else if (wave < 4) {
    bar();
    make_sel();
  }
  __builtin_amdgcn_s_setprio(1);
  pin();
  const int hsh = (bid * 2654435761u) >> 20;
  const int rec_voff = 64 * ((lane >> 1) & 15) + 32 * (lane >> 5) + 16 * (lane & 1);
  constexpr int L_RED = 65536, L_REDE = L_RED + 8192;
  v4u wo[4], we[2];
  auto mk_wo = [&]() {
    const char* base = (const char*)(isX ? P.woA : P.woB) + (h * 128 + wave * 16) * 1152;
    const u64 u = ((u64)(u32)__builtin_amdgcn_readfirstlane((u32)((u64)base >> 32)) << 32) | (u32)__builtin_amdgcn_readfirstlane((u32)(u64)base);
    u64 v = u;
    asm volatile("" : "+s"(v));
    return mk((const void*)v, 16 * 1152);
  };
  auto load_wo = [&]() {
    const rsrc_t rwo = mk_wo();
    const int voff = g * 4608 + c16 * 16;
#pragma unroll
    for (int i = 0; i < 4; ++i) { wo[i] = ld4<AUX_NT>(rwo, voff + i * 1152, r * 256); pin(); }
  };
  auto load_we = [&]() {
    if (r == 3) {
      const rsrc_t rwo = mk_wo();
      const int voff = g * 4608 + bsel * 2304 + (c16 & 7) * 16;
#pragma unroll
      for (int i = 0; i < 2; ++i) { we[i] = ld4<AUX_NT>(rwo, voff + i * 1152, 1024); pin(); }
    }
  };
  constexpr int L_R2 = 53248;

  if (isX) {
    constexpr int L_ML = 0;
    v4u kk[16], vv[16];
    {
      const int chunk = SPREAD ? 2 * w4 + (((wave >> 2) ^ (hsh >> (2 + w4))) & 1) : wave;
      const size_t kvoff = ((size_t)(bx * 16 + gk) * 8192 + 6144 + r * 512 + chunk * 64) * 256;
      const int xo = ((SPREAD ? hsh + wave : hsh) & 3) * 4096;
      const rsrc_t rk = mk((const char*)P.kc + kvoff, 16384), rv = mk((const char*)P.vc + kvoff, 16384);
      const int k_voff = ((c16 & 2) * 2 + (c16 >> 2)) * 256 + kq_voff;
      if (WSYNC == 2) load_wo();
#pragma unroll
      for (int i = 0; i < 16; ++i) { kk[i] = ld4<AUX_NT>(rk, k_voff + (i & 1) * 128 + ((i >> 1) & 1) * 2048, ((i >> 2) * 4096) ^ xo); pin(); }
      if (KSYNC && wave >= 4) { post(L_FLAG + 40, lane); spin_flag(smem + L_FLAG + 40, 4); }
#pragma unroll
      for (int i = 0; i < 16; ++i) { vv[i] = ld4<AUX_NT>(rv, lane * 16 + (i & 3) * 1024, ((i >> 2) * 4096) ^ xo); pin(); }
      if (WSYNC == 1) post(L_FLAG + 44, lane);
    }
    ST(4);
    asm volatile("" ::"v"(pfc));
    spin_flag(smem + L_FLAG + 16, 4);
    ST(5);
    float sc[16];
    {
      const v4u q0 = *(const v4u*)(smem + L_QV + 512 + bsel * 256 + kq_voff), q1 = *(const v4u*)(smem + L_QV + 512 + bsel * 256 + 128 + kq_voff);
      const bool odd = c16 & 1;
#pragma unroll
      for (int be = 0; be < 8; ++be) {
        f4 d = {0.f, 0.f, 0.f, 0.f};
        d = mfma16(kk[2 * be], q0, d); d = mfma16(kk[2 * be + 1], q1, d);
        const float u0 = odd ? d[1] : d[0], u1 = odd ? d[3] : d[2];
        sc[2 * be] = u0 + dppf<XOR1>(u0); sc[2 * be + 1] = u1 + dppf<XOR1>(u1);
      }
    }
    float m = mx(mx(mx(sc[0], sc[1], sc[2]), mx(sc[3], sc[4], sc[5]), mx(sc[6], sc[7], sc[8])), mx(mx(sc[9], sc[10], sc[11]), mx(sc[12], sc[13], sc[14]), sc[15]));
    m = swap32_max(swap16_max(m)) * SC;
    float l = 0.f;
#pragma unroll
    for (int be = 0; be < 8; ++be) {
      const float p0 = ex2(fmaf(sc[2 * be], SC, -m)), p1 = ex2(fmaf(sc[2 * be + 1], SC, -m));
      l += p0 + p1;
      const u32 pp = pk(p0, p1);
      kk[2 * be] = diag(pp, sel0); kk[2 * be + 1] = diag(pp, sel1);
    }
    l = swap16_add(l, l); l = swap32_add(l, l);
    if ((lane & 0x37) == 0) { *(float*)(smem + L_ML + bsel * 64 + wave * 4) = m; *(float*)(smem + L_ML + bsel * 64 + 32 + wave * 4) = l; }
    post(L_FLAG + 36, lane);
#pragma unroll
    for (int i = 0; i < 16; i += 4) asm volatile("" : "+v"(kk[i]), "+v"(kk[i + 1]), "+v"(kk[i + 2]), "+v"(kk[i + 3]));
    if (WSYNC == 1) spin_flag(smem + L_FLAG + 44, 8);
    else __builtin_amdgcn_s_setprio(0);
    if (WSYNC != 2) load_wo();
    spin_flag(smem + L_FLAG + 36, 8);
    const char* p = smem + L_ML + (lane >> 5) * 64;
    const f4 m0 = *(const f4*)p, m1 = *(const f4*)(p + 16);
    const float M = mx(mx(mx(m0[0], m0[1]), mx(m0[2], m0[3])), mx(mx(m1[0], m1[1]), mx(m1[2], m1[3])));
    const float fo = ex2(*(const float*)(p + wave * 4) - M);
    float L = ex2(*(const float*)(p + (lane & 7) * 4) - M) * *(const float*)(p + 32 + (lane & 7) * 4);
    pin();
    f4 acc = {0.f, 0.f, 0.f, 0.f};
#pragma unroll
    for (int i = 0; i < 16; ++i) acc = mfma16(kk[i], vv[i], acc);
    *(f4*)(smem + L_R2 + wave * 1024 + lane * 16) = acc * fo;
    ST(6);
    if (wave == 2) {
      L = quad_sum(L); L += dppf<HMIR>(L);
      if ((lane & 24) == 0) st4(v4u{fu(M), tag, fu(L), tag}, ws, (lane >> 5) * 128 + (lane & 7) * 16, S_HS + id * SLOT);
    }
  } else {
    constexpr int L_Q = 0, L_P = 32768, L_MX = 40960, L_LS = 41472, L_LAT = 45056, L_ST = 49152, L_CNT = L_FLAG + 32;
    const int b = id >> 6;
    __builtin_amdgcn_s_waitcnt(0x0070);
    v4u fr[32], wv[4];
    const int r32 = lane & 31, hh = lane >> 5;
    const int row_base = r32 * 1024 + 16 * (hh ^ (((r32 & 3) << 2) | ((r32 >> 2) & 3)));
    const rsrc_t ruv = mk((const char*)P.wuv + (r * 128 + 64 * (wave >> 2) + 8 * w4) * 8192 + h * 256, 39 * 8192 + 256);
#define LDUV(i) wv[i] = ld4<AUX_NT>(ruv, g * 16384 + c16 * 16, (((i) >> 1) + 32 * ((i) & 1)) * 8192)
#define QCNT(rq) (L_FLAG + ((rq) < 3 ? 36 + 4 * (rq) : 56))
    f16v s;
    if (wave < 4) {
      const rsrc_t rc = SPREAD ? mk((const char*)P.ckv + (size_t)id * 131072 + wave * 32768, 32768) : mk((const char*)P.ckv + (size_t)id * 131072 + wave * 8192, 131072 - wave * 8192);
      __builtin_amdgcn_s_waitcnt(0xc07f);
      const int kz = ((hsh >> 3) + wave) & 3;
      const int xi = hsh & 4, lx = lane ^ ((2 * wave + (xi >> 2)) & 3);
#pragma unroll
      for (int i = 0; i < 32; ++i) {
        const int ro = (i >> 3) * 32768 + ((i & 7) ^ xi) * 1024;
        __builtin_amdgcn_raw_ptr_buffer_load_lds(rc, (LAS void*)(smem + wave * 8192 + ro), 16, 16 * (lx ^ (((i & 3) << 2) | ((i >> 2) & 1))), SPREAD ? ((i >> 3) ^ kz) * 8192 + ((i & 7) ^ xi) * 1024 : ro, 0, AUX_NT);
        pin();
        if (i >= WY - 1 && i < NY) asm volatile("s_waitcnt vmcnt(%0)" ::"i"(WY - 1) : "memory");
        if (i == 23 || i == 31) {
          asm volatile("s_waitcnt vmcnt(16)" ::: "memory");
          if (lane == 0) asm volatile("ds_add_u32 %0, %1" ::"v"(L_FLAG + 4 * ((i >> 3) - 2)), "v"(1u) : "memory");
          pin();
        }
      }
      int qa[8], fa[8];
#pragma unroll
      for (int t = 0; t < 8; ++t) { qa[t] = row_base ^ (32 * t); fa[t] = qa[t] + wave * 32768; asm volatile("" : "+v"(qa[t]), "+v"(fa[t])); }
      asm volatile("s_waitcnt vmcnt(8)" ::: "memory");
      post(L_FLAG + 8, lane);
      auto landed = [&]() {
        bar();
        asm volatile("s_waitcnt vmcnt(0)" ::: "memory");
        post(L_FLAG + 12, lane);
        ST(1);
      };
      if (wave == 3) landed();
      spin_flag_asm(L_FLAG + 4 * wave, 4);
#pragma unroll
      for (int t = 0; t < 32; ++t)
        asm volatile("ds_read_b128 %0, %1 offset:%2" : "=v"(fr[t]) : "v"(fa[t & 7]), "i"(256 * (t >> 3)) : "memory");
      pin();
      if (wave != 3) landed();
      __builtin_amdgcn_s_waitcnt(0x0f70);
      asm volatile("" ::"v"(pfc));
      v4u qb[4];
#define QRD(t) (*(const v4u*)(smem + L_Q + qa[(t) & 7] + 256 * ((t) >> 3)))
      spin_flag(smem + L_FLAG + 36, 4);
      ST(2);
      u32 rdy = 0;
#pragma unroll
      for (int t = 0; t < 4; ++t) qb[t] = QRD(t);
#pragma unroll
      for (int i = 0; i < 16; ++i) s[i] = 0.f;
      pin();
#pragma unroll
      for (int t = 0; t < 32; ++t) {
        s = mfma32(fr[t], qb[t & 3], s);
        __builtin_amdgcn_sched_barrier(0);
        if ((t & 7) == 4 && t < 24) {
          if (__builtin_expect((u32)__builtin_amdgcn_readfirstlane(rdy) < 4, 0)) spin_flag(smem + QCNT((t >> 3) + 1), 4);
          pin();
        }
        if ((t & 7) == 1 && t < 24) rdy = *(volatile const LAS u32*)(smem + QCNT((t >> 3) + 1));
        if (t + 4 < 32) qb[t & 3] = QRD(t + 4);
        if (t >= 21 && t < 25) LDUV(t - 21);
        __builtin_amdgcn_sched_barrier(0);
      }
      ST(3);
      float m = mx(mx(mx(s[0], s[1], s[2]), mx(s[3], s[4], s[5]), mx(s[6], s[7], s[8])), mx(mx(s[9], s[10], s[11]), mx(s[12], s[13], s[14]), s[15]));
      m = swap32_max(m);
      if (lane < 32) *(float*)(smem + L_MX + lane * 16 + wave * 4) = m;
    } else {
      const int q = c16 >> 2, p = lane & 3;
      const int base = (4 * hh + q) * 1024 + w4 * 256 + 16 * ((2 * (g & 1) + (p >> 1)) ^ (4 * q + hh)) + 8 * (p & 1);
      auto frags = [&](auto blk) {
        constexpr int kb = decltype(blk)::value;
        spin_flag(smem + L_FLAG + 4 * kb, 4);
#pragma unroll
        for (int ta = 0; ta < 4; ++ta)
#pragma unroll
          for (int sh = 0; sh < 2; ++sh) {
            const char* pa = smem + kb * 32768 + sh * 16384;
            const v2u a = __builtin_bit_cast(v2u, __builtin_amdgcn_ds_read_tr16_b64_v4i16((LAS s4v*)(pa + (base ^ (64 * ta)))));
            const v2u c = __builtin_bit_cast(v2u, __builtin_amdgcn_ds_read_tr16_b64_v4i16((LAS s4v*)(pa + (base ^ (64 * ta) ^ 32) + 8192)));
            fr[ta * 8 + 2 * kb + sh] = v4u{a.x, a.y, c.x, c.y};
          }
        pin();
      };
      v4u qg[16];
      int qvo = S_H2 + (32 * w4 + 4 * hh) * SLOT + b * 512 + r32 * 16;
      asm volatile("" : "+v"(qvo));
#define LDQ(rq, i) qg[4 * (rq) + (i)] = ld4<AUX_SC1>(ws, qvo, (8 * (i) + (rq)) * SLOT)
      int qw[4];
#pragma unroll
      for (int i = 0; i < 4; ++i) {
        const int hd = 8 * w4 + 2 * i + hh;
        qw[i] = hd * 1024 + 16 * ((r32 >> 1) ^ (((hd & 3) << 2) | ((hd >> 2) & 3))) + 8 * (lane & 1);
        asm volatile("" : "+v"(qw[i]));
      }
      frags(std::integral_constant<int, 0>{});
      frags(std::integral_constant<int, 1>{});
      frags(std::integral_constant<int, 2>{});
      if (!EARLYQ) { bar(); spin_flag(smem + L_FLAG + 12, 4); }
#pragma unroll
      for (int i = 0; i < 16; ++i) { LDQ(i >> 2, i & 3); pin(); }
      if (EARLYQ) bar();
#pragma unroll
      for (int rq = 0; rq < 4; ++rq) {
        u32 bad = 0;
#pragma unroll
        for (int i = 0; i < 4; ++i) bad = bad4(bad, qg[4 * rq + i], tag);
        if (__builtin_expect(!none(bad), 0)) {
          _Pragma("nounroll") for (int spin = 0; spin < SPIN_CAP; ++spin) {
            pin();
            bad = 0;
#pragma unroll
            for (int i = 0; i < 4; ++i) LDQ(rq, i);
#pragma unroll
            for (int i = 0; i < 4; ++i) bad = bad4(bad, qg[4 * rq + i], tag);
            if (none(bad)) break;
            __builtin_amdgcn_s_sleep(4);
          }
        }
#pragma unroll
        for (int i = 0; i < 4; ++i) *(v2u*)(smem + L_Q + qw[i] + 256 * rq) = v2u{qg[4 * rq + i].x, qg[4 * rq + i].z};
        post(QCNT(rq), lane);
      }
      ST(3);
#pragma unroll
      for (int i = 0; i < 4; ++i) { LDUV(i); pin(); }
      frags(std::integral_constant<int, 3>{});
    }
    bar();
    if (wave < 4) {
      const f4 mm = *(const f4*)(smem + L_MX + r32 * 16);
      const float m = mx(mx(mm[0], mm[1]), mx(mm[2], mm[3])) * SC;
      u32 pp[8];
#pragma unroll
      for (int i = 0; i < 8; ++i) {
        s[2 * i] = ex2(fmaf(s[2 * i], SC, -m)); s[2 * i + 1] = ex2(fmaf(s[2 * i + 1], SC, -m));
        pp[i] = pk(s[2 * i], s[2 * i + 1]);
      }
      *(v4u*)(smem + L_P + wave * 2048 + lane * 16) = v4u{pp[0], pp[1], pp[2], pp[3]};
      *(v4u*)(smem + L_P + wave * 2048 + 1024 + lane * 16) = v4u{pp[4], pp[5], pp[6], pp[7]};
    }
    bar();
    pin();
    if (wave < 4) {
      float l = 0.f;
#pragma unroll
      for (int i = 0; i < 8; ++i) l += s[2 * i] + s[2 * i + 1];
      l = swap32_add(l, l);
      if (lane < 32) *(float*)(smem + L_LS + lane * 16 + wave * 4) = l;
      tag = __builtin_amdgcn_readfirstlane(*(const u32*)(smem + L_FLAG + 28));
    }
    ST(4);
    if (ABL == 2) { load_wo(); load_we(); asm volatile("" ::"v"(wo[0]), "v"(wo[1]), "v"(wo[2]), "v"(wo[3]), "v"(wv[0]), "v"(wv[1]), "v"(wv[2]), "v"(wv[3])); goto done; }
    if (wave >= 4) {
      v4u pf[8];
#pragma unroll
      for (int ks = 0; ks < 8; ++ks) pf[ks] = *(const v4u*)(smem + L_P + ks * 1024 + lane * 16);
      f16v o0, o1, o2, o3;
#pragma unroll
      for (int i = 0; i < 16; ++i) { o0[i] = 0.f; o1[i] = 0.f; o2[i] = 0.f; o3[i] = 0.f; }
      const int voff = hh * 524288 + r32 * 4, soff = W_H3D + (wave - 4) * 32768 + id * 256;
      pin();
#pragma unroll
      for (int ks = 0; ks < 8; ++ks) { o0 = mfma32(pf[ks], fr[ks], o0); o1 = mfma32(pf[ks], fr[8 + ks], o1); }
      __builtin_amdgcn_sched_barrier(0);
#define OST(a, c, i, pi) __builtin_amdgcn_raw_buffer_store_b32(pk(a[i], c[i]), ws, voff + (pi) * 128, soff + (((i) & 3) + 8 * ((i) >> 2)) * 131072, AUX_SC1)
#pragma unroll
      for (int ks = 0; ks < 8; ++ks) {
        o2 = mfma32(pf[ks], fr[16 + ks], o2);
        __builtin_amdgcn_sched_barrier(0);
        if (ks) OST(o0, o1, 2 * ks - 2, 0);
        __builtin_amdgcn_sched_barrier(0);
        o3 = mfma32(pf[ks], fr[24 + ks], o3);
        __builtin_amdgcn_sched_barrier(0);
        if (ks) OST(o0, o1, 2 * ks - 1, 0);
        __builtin_amdgcn_sched_barrier(0);
      }
      OST(o0, o1, 14, 0); OST(o0, o1, 15, 0);
#pragma unroll
      for (int i = 0; i < 16; ++i) OST(o2, o3, i, 1);
    }
    bar();
    ST(5);
    if (wave >= 4) {
      __builtin_amdgcn_s_setprio(3);
      const int hd = (lane >> 3) * 4 + (lane & 3);
      const f4 mm = *(const f4*)(smem + L_MX + hd * 16), ll = *(const f4*)(smem + L_LS + hd * 16);
      v4u sv = {fu(mx(mx(mm[0], mm[1]), mx(mm[2], mm[3])) * SC), tag, fu((ll[0] + ll[1]) + (ll[2] + ll[3])), tag};
      asm volatile("s_waitcnt vmcnt(0)" : "+v"(sv)::"memory");
      if (arrive(L_CNT, lane) == 3) st4(sv, ws, lane * 16, S_HS + (128 + id) * SLOT);
      __builtin_amdgcn_s_setprio(0);
      ST(6);
    }
    pin();
    if (!WO_LATE) load_wo();
    if (ABL == 3) { asm volatile("" ::"v"(wo[0]), "v"(wo[1]), "v"(wo[2]), "v"(wo[3]), "v"(wv[0]), "v"(wv[1]), "v"(wv[2]), "v"(wv[3])); goto done; }
    if (wave < 4) __builtin_amdgcn_s_sleep(D_H3 + 14); else __builtin_amdgcn_s_sleep(D_H3);
    {
      v4u st;
      _Pragma("nounroll") for (int spin = 0; spin < SPIN_CAP; ++spin) {
        pin();
        st = ld4<AUX_SC1>(ws, (lane >> 2) * SLOT, S_HS + (128 + wave * 16) * SLOT + x * 128 + (h & 3) * 16);
        if (none(bad4(0u, st, tag))) break;
        __builtin_amdgcn_s_sleep(P_H3);
      }
      pin();
      ST(7);
      v4u d[4];
#pragma unroll
      for (int i = 0; i < 4; ++i) d[i] = ld4<AUX_SC1>(ws, g * 1024 + c16 * 16 + i * 256, W_H3D + id * 32768 + wave * 4096);
      pin();
      float M = uf(st.x);
      M = mx(M, dppf<ROR4>(M)); M = mx(M, dppf<ROR8>(M)); M = swap32_max(swap16_max(M));
      float wg = ex2(uf(st.x) - M);
      float L = wg * uf(st.z);
      L += dppf<ROR4>(L); L += dppf<ROR8>(L); L = swap16_add(L, L); L = swap32_add(L, L);
      if (lane == 0) *(f2*)(smem + L_ST + wave * 8) = f2{M, L};
      bar();
      const f4 s0 = *(const f4*)(smem + L_ST + (wave >> 2) * 32), s1 = *(const f4*)(smem + L_ST + (wave >> 2) * 32 + 16);
      const float MM = mx(mx(s0[0], s0[2], s1[0]), s1[2]);
      const float LL = fmaf(ex2(s0[0] - MM), s0[1], fmaf(ex2(s0[2] - MM), s0[3], fmaf(ex2(s1[0] - MM), s1[1], ex2(s1[2] - MM) * s1[3])));
      wg *= ex2(M - MM) * __builtin_amdgcn_rcpf(LL);
      const u32 w01 = pk(dppf<BCAST>(wg), dppf<BCAST + 4>(wg)), w23 = pk(dppf<BCAST + 8>(wg), dppf<BCAST + 12>(wg));
      v4u af[4] = {diag(w01, sel0), diag(w01, sel1), diag(w23, sel0), diag(w23, sel1)};
      asm volatile("" : "+v"(af[0]), "+v"(af[1]), "+v"(af[2]), "+v"(af[3]));
      pin();
      if (WO_LATE == 3) load_wo();
      f4 lat = {0.f, 0.f, 0.f, 0.f};
#pragma unroll
      for (int i = 0; i < 4; ++i) lat = mfma16(af[i], d[i], lat);
      if (lane < 32) *(f4*)(smem + L_LAT + wave * 512 + c16 * 32 + g * 16) = lat;
      pin();
    }
    bar();
    if (WO_LATE == 2) load_wo();
    {
      const char* p = smem + L_LAT + bsel * 2048 + wave * 64 + g * 16;
      const f4 c = (*(const f4*)p + *(const f4*)(p + 512)) + (*(const f4*)(p + 1024) + *(const f4*)(p + 1536));
      *(f4*)(smem + L_R2 + wave * 1024 + lane * 16) = rows16(pk(c[0], c[1]), pk(c[2], c[3]), wv, sel0, sel1);
    }
  }
  {
  bar();
  if (isX) ST(7);
  if (wave < 2) {
    const f2 sv = sum8(smem + L_R2 + tid * 8);
    st4(v4u{fu(sv.x), tag, fu(sv.y), tag}, ws, rec_voff, S_HD + ro + id * SLOT + wave * 1024);
  }
  pin();
  load_we();
  const int gsv = (c16 & 3) * SLOT + bsel * (isX ? 4 * SLOT : 1024), gso = isX ? (id & ~7) * SLOT : (128 + (id & ~3)) * SLOT;
  const int dv = gsv + g * 32 + (c16 & 4) * 4, dso = S_HD + gso + (isX ? (h & 1) * 1024 : 0) + wave * 128;
  if (isX) __builtin_amdgcn_s_sleep(D_A3); else __builtin_amdgcn_s_sleep(D_H4);
  ST(8);
  {
    v4u d0 = ld4<AUX_SC1>(ws, dv, dso);
    pin();
    float w = 1.f;
    if (isX) {
      v4u st;
      _Pragma("nounroll") for (int spin = 0; spin < SPIN_CAP; ++spin) {
        pin();
        st = ld4<AUX_SC1>(ws, gsv, S_HS + gso + (h & 1) * 128);
        if (none(bad4(0u, st, tag))) break;
        __builtin_amdgcn_s_sleep(4);
      }
      float M = uf(st.x);
      M = mx(M, dppf<XOR1>(M)); M = mx(M, dppf<XOR2>(M));
      w = ex2(uf(st.x) - M);
      w *= __builtin_amdgcn_rcpf(quad_sum(w * uf(st.z)));
      asm volatile("" : "+v"(w));
    }
    _Pragma("nounroll") for (int spin = 0; spin < SPIN_CAP; ++spin) {
      if (none(bad4(0u, d0, tag))) break;
      pin();
      d0 = ld4<AUX_SC1>(ws, dv, dso);
      if (P_HD) __builtin_amdgcn_s_sleep(P_HD);
    }
    ST(9);
    const u32 cm = pk(quad_sum(w * uf(d0.x)), quad_sum(w * uf(d0.z))), co = dppu<HMIR>(cm);
    const u32 c01 = (c16 & 4) ? co : cm, c23 = (c16 & 4) ? cm : co;
    *(f4*)(smem + L_RED + wave * 1024 + lane * 16) = rows16(c01, c23, wo, sel0, sel1);
    if (r == 3) {
      const f4 z = {0.f, 0.f, 0.f, 0.f};
      const f4 lo = mfma16(diag(c01, sel1), we[1], mfma16(diag(c01, sel0), we[0], z)), hi = mfma16(diag(c23, sel1), we[1], mfma16(diag(c23, sel0), we[0], z));
      *(f4*)(smem + L_REDE + wave * 1024 + lane * 16) = f4{lo[0] + dppf<ROR8>(hi[0]), lo[1] + dppf<ROR8>(hi[1]), lo[2] + dppf<ROR8>(hi[2]), lo[3] + dppf<ROR8>(hi[3])};
    }
  }
  bar();
  if (wave < 4) {
    if (wave < 2 || (r == 3 && (lane & 16) == 0)) {
      const f2 sv = sum8(smem + L_RED + (wave >> 1) * 8192 + (tid & 127) * 8);
      st4(v4u{fu(sv.x), tag, fu(sv.y), tag}, ws, lane * 16, S_H5 + ro + id * SLOT + wave * 1024);
    }
    if (isX) ST(10);
  } else if (wave == 4 && id < 72) {
    const int e = id - 64, L = id < 64 ? id : 64 + (e & 1) + 4 * (e >> 1);
    const int rso = S_H5 + ro + (id < 64 ? (L >> 4) * SLOT + (L & 15) * 128 : 3 * SLOT + (L - 48) * 128);
    v4u v[4];
    __builtin_amdgcn_s_sleep(D_H5);
    _Pragma("nounroll") for (int spin = 0; spin < SPIN_CAP; ++spin) {
      pin();
      u32 bad = 0;
#pragma unroll
      for (int i = 0; i < 4; ++i) v[i] = ld4<AUX_SC1>(ws, (lane >> 3) * 4 * SLOT + (lane & 7) * 16, rso + i * 32 * SLOT);
#pragma unroll
      for (int i = 0; i < 4; ++i) bad = bad4(bad, v[i], tag);
      if (none(bad)) break;
      __builtin_amdgcn_s_sleep(2);
    }
    float a = (uf(v[0].x) + uf(v[1].x)) + (uf(v[2].x) + uf(v[3].x)), c = (uf(v[0].z) + uf(v[1].z)) + (uf(v[2].z) + uf(v[3].z));
    a += dppf<ROR8>(a); c += dppf<ROR8>(c);
    a = swap16_add(a, a); c = swap16_add(c, c);
    a = swap32_add(a, a); c = swap32_add(c, c);
    if (lane < 8) {
      const int al = L & 15;
      const int n = (al >> 3) * 1152 + (isX ? 0 : 576) + 128 * (L >> 4) + 32 * (al & 3) + 8 * (lane >> 1) + 4 * ((al >> 2) & 1) + 2 * (lane & 1);
      ((u32*)P.out)[n >> 1] = pk(a, c);
    }
    if (!isX && id == 0 && lane == 0) __builtin_amdgcn_raw_buffer_store_b32(tag, ws, 0, W_EPOCH, AUX_SC1);
  }
  }
done:;
  ST(11);
  if (lane == 0) {
    u64* sp = (u64*)((char*)P.ws + W_STAMP) + (bid * 8 + wave) * 12;
#pragma unroll
    for (int i = 0; i < 12; ++i) __hip_atomic_store(sp + i, ts[i], __ATOMIC_RELAXED, __HIP_MEMORY_SCOPE_AGENT);
  }
  if (bid == 0 && wave == 7) {
    while (__builtin_amdgcn_s_memrealtime() - ts[0] < 7000) __builtin_amdgcn_s_sleep(8);
    u64* sp = (u64*)((char*)P.ws + W_STAMP);
    u64 mn = ~0ull;
    for (int e = lane; e < 2048; e += 64) { u64 v = __hip_atomic_load(sp + e * 12, __ATOMIC_RELAXED, __HIP_MEMORY_SCOPE_AGENT); mn = v < mn ? v : mn; }
    for (int o = 32; o; o >>= 1) { u64 t = __shfl_xor(mn, o); mn = t < mn ? t : mn; }
    float sum = 0.f, cnt = 0.f, mx = 0.f;
    for (int e = lane; e < 2048; e += 64) {
      u64 v = __hip_atomic_load(sp + e * 12 + REP_IDX, __ATOMIC_RELAXED, __HIP_MEMORY_SCOPE_AGENT);
      const int role = (e >> 6) >= 16;
      if (v && (REP_ROLE == 2 || role == REP_ROLE)) { float d = (float)(v - mn); sum += d; cnt += 1.f; mx = fmaxf(mx, d); }
    }
    for (int o = 32; o; o >>= 1) { sum += __shfl_xor(sum, o); cnt += __shfl_xor(cnt, o); mx = fmaxf(mx, __shfl_xor(mx, o)); }
    float q = REP_STAT == 0 ? 0.f : REP_STAT == 1 ? sum / cnt : mx;
    q = fminf(q, 6000.f);
    const u64 until = mn + 10000 + (u64)(4.f * q);
    while (__builtin_amdgcn_s_memrealtime() < until) __builtin_amdgcn_s_sleep(1);
  }
}

extern "C" void kernel_launch(void* const* d_in, const int* in_sizes, int n_in, void* d_out, int out_size, void* d_ws, size_t ws_size,
                              hipStream_t stream) {
  Params p;
  char* w = (char*)d_ws + (WS_AT_END ? ((ws_size - (18 << 20)) & ~(size_t)0x1fffff) : 0);
  p.xA = d_in[0]; p.kc = d_in[1]; p.vc = d_in[2]; p.wqA = d_in[3]; p.woA = d_in[4];
  p.xB = d_in[5]; p.ckv = d_in[6]; p.wqB = d_in[7]; p.wuk = d_in[8]; p.wuv = d_in[9]; p.woB = d_in[10];
  p.out = d_out; p.ws = w;
  fused_decode<<<256, 512, 0, stream>>>(p);
}
```

```cpp
#include <hip/hip_runtime.h>
#include <hip/hip_bf16.h>
#include <stdint.h>
#include <type_traits>

typedef unsigned u32;
typedef unsigned long long u64;
typedef u32 v4u __attribute__((ext_vector_type(4)));
typedef u32 v2u __attribute__((ext_vector_type(2)));
typedef float f2 __attribute__((ext_vector_type(2)));
typedef float f4 __attribute__((ext_vector_type(4)));
typedef float f16v __attribute__((ext_vector_type(16)));
typedef __bf16 bf8 __attribute__((ext_vector_type(8)));
typedef __bf16 bf2 __attribute__((ext_vector_type(2)));
typedef short s4v __attribute__((ext_vector_type(4)));
typedef __amdgpu_buffer_rsrc_t rsrc_t;
#define LAS __attribute__((address_space(3)))
#define DI __device__ __forceinline__

constexpr int AUX_NT = 2, AUX_SC1 = 16;
constexpr int W_EPOCH = 0;
constexpr int SLOT = 16896, W_SLOT = 4096;
constexpr int O_HD = 0;
constexpr int O_H5 = 2048;
constexpr int S_H1 = W_SLOT + 2048;
constexpr int S_H2 = W_SLOT + 5120;
constexpr int S_HY = W_SLOT + 5120;
constexpr int S_HX = W_SLOT + 6144;
constexpr int INB = 33280;
constexpr int W_H3D = 5 << 20;
constexpr int W_END = W_H3D + 128 * INB;
constexpr int W_STAMP = 16 << 20;
constexpr float SC = 0.08838834764831845f * 1.4426950408889634f;
#define KNOB(n, v) constexpr int n = v
#define K_D_H1 20
#define K_WX 2
#define K_NX 12
#define K_WY 2
#define K_NY 0
#define K_DXA 20
#define K_P_H3 6
#define P_HD 2
#define K_D_A3 22
#define K_D_H3 30
#define K_D_H4 16
#define K_D_H5 14
#define EARLYQ 1
#define K_D_H2 0
#define WS_AT_END 0
#define SPREAD 1
#define WE_EARLY 0
#define NK 0
#define T_WO 0
#define T_WOY T_WO
#define D_WO 4
#define D_WOY 12
#define D_G3 0
#define PWT 1
#define WEY 1
#define PWL 1
#define PREW 2
#define HX 0
#define KSYNC 1
#define WSYNC 1
#define T_GO 0
#define T_A1 0
#define T_Y0 0
#define D_H1A 16
#define WO_LATE 5
#define K_DX0 0
#define ABL 0
#define CODE_BYTES 20736
constexpr int D_H1 = K_D_H1, WX = K_WX, NX = K_NX, WY = K_WY, NY = K_NY, DXA = K_DXA, P_H3 = K_P_H3, D_A3 = K_D_A3, D_H3 = K_D_H3, D_H4 = K_D_H4,
              D_H5 = K_D_H5, D_H2 = K_D_H2;
constexpr int SPIN_CAP = 1 << 17;

struct Role {
  const void *x, *wq;
  void* ws;
  const void *p0, *p1, *p2, *wo;
  void* out;
};
struct Params { Role role[2]; };

DI rsrc_t mk(const void* p, u32 bytes) { return __builtin_amdgcn_make_buffer_rsrc((void*)p, 0, bytes, 0x00020000); }
template <int AUX> DI v4u ld4(rsrc_t r, int voff, int soff) { return __builtin_amdgcn_raw_buffer_load_b128(r, voff, soff, AUX); }
template <int AUX> DI v2u ld2(rsrc_t r, int voff, int soff) { return __builtin_amdgcn_raw_buffer_load_b64(r, voff, soff, AUX); }
DI void st4(v4u v, rsrc_t r, int voff, int soff) { __builtin_amdgcn_raw_buffer_store_b128(v, r, voff + soff, 0, AUX_SC1); }
DI u32 fu(float f) { return __builtin_bit_cast(u32, f); }
DI float uf(u32 u) { return __builtin_bit_cast(float, u); }
DI u32 pk(float lo, float hi) { f2 f = {lo, hi}; return __builtin_bit_cast(u32, __builtin_convertvector(f, bf2)); }
DI float blo(u32 u) { return uf(u << 16); }
DI float bhi(u32 u) { return uf(u & 0xffff0000u); }
template <int C> DI u32 dppu(u32 x) { return __builtin_amdgcn_mov_dpp(x, C, 0xf, 0xf, true); }
template <int C> DI float dppf(float x) { return uf(dppu<C>(fu(x))); }
constexpr int XOR1 = 0xB1, XOR2 = 0x4E, HMIR = 0x141, ROR4 = 0x124, ROR8 = 0x128, BCAST = 0x150;
DI f4 mfma16(v4u a, v4u b, f4 c) { return __builtin_amdgcn_mfma_f32_16x16x32_bf16(__builtin_bit_cast(bf8, a), __builtin_bit_cast(bf8, b), c, 0, 0, 0); }
DI f16v mfma32(v4u a, v4u b, f16v c) { return __builtin_amdgcn_mfma_f32_32x32x16_bf16(__builtin_bit_cast(bf8, a), __builtin_bit_cast(bf8, b), c, 0, 0, 0); }
DI float mx(float a, float b) { return __builtin_elementwise_maximum(a, b); }
DI float mx(float a, float b, float c) { return mx(mx(a, b), c); }
DI float mx4(f4 v) { return mx(mx(v[0], v[1], v[2]), v[3]); }
DI float ex2(float x) { return __builtin_amdgcn_exp2f(x); }
DI float swap32_add(float a, float b) { auto s = __builtin_amdgcn_permlane32_swap(fu(a), fu(b), false, false); return uf(s[0]) + uf(s[1]); }
DI float swap16_add(float a, float b) { auto s = __builtin_amdgcn_permlane16_swap(fu(a), fu(b), false, false); return uf(s[0]) + uf(s[1]); }
DI float swap32_max(float a) { auto s = __builtin_amdgcn_permlane32_swap(fu(a), fu(a), false, false); return mx(uf(s[0]), uf(s[1])); }
DI float swap16_max(float a) { auto s = __builtin_amdgcn_permlane16_swap(fu(a), fu(a), false, false); return mx(uf(s[0]), uf(s[1])); }
DI void bar() {
  __builtin_amdgcn_sched_barrier(0);
  asm volatile("s_waitcnt lgkmcnt(0)\n\ts_barrier" ::: "memory");
  __builtin_amdgcn_sched_barrier(0);
}
DI void pin() { asm volatile("" ::: "memory"); __builtin_amdgcn_sched_barrier(0); }
DI u32 bad4(u32 acc, v4u v, u32 tag) {
  return __builtin_amdgcn_bitop3_b32(__builtin_amdgcn_bitop3_b32(acc, v.y, tag, 0xF6), v.w, tag, 0xF6);
}
DI bool none(u32 bad) { return __builtin_amdgcn_ballot_w64(bad != 0) == 0; }
DI v4u diag(u32 src, const u32 (&sel)[4]) {
  v4u a;
#pragma unroll
  for (int k = 0; k < 4; ++k) a[k] = __builtin_amdgcn_perm(src, src, sel[k]);
  return a;
}

#define ST(i) do {} while (0)
#define CNT(i, n) do {} while (0)

DI void spin_flag(const char* p, u32 want = 1) {
  _Pragma("nounroll") for (int spin = 0; spin < SPIN_CAP * 8; ++spin) {
    if ((u32)__builtin_amdgcn_readfirstlane(*(volatile const LAS u32*)p) >= want) break;
    __builtin_amdgcn_s_sleep(1);
  }
  pin();
}
DI u32 arrive(u32 addr, int lane) {
  u32 old = 0;
  if (lane == 0) asm volatile("ds_add_rtn_u32 %0, %1, %2\n\ts_waitcnt lgkmcnt(0)" : "=v"(old) : "v"(addr), "v"(1u) : "memory");
  old = __builtin_amdgcn_readfirstlane(old);
  pin();
  return old;
}
DI void post(u32 addr, int lane) {
  if (lane == 0) asm volatile("ds_add_u32 %0, %1" ::"v"(addr), "v"(1u) : "memory");
  pin();
}
DI void spin_flag_asm(u32 addr, u32 want) {
  _Pragma("nounroll") for (int spin = 0; spin < SPIN_CAP * 8; ++spin) {
    u32 v;
    asm volatile("ds_read_b32 %0, %1\n\ts_waitcnt lgkmcnt(0)" : "=v"(v) : "v"(addr) : "memory");
    if ((u32)__builtin_amdgcn_readfirstlane(v) >= want) break;
    __builtin_amdgcn_s_sleep(1);
  }
  pin();
}
DI float row4_sum(float v) { v += dppf<ROR4>(v); return v + dppf<ROR8>(v); }
DI float quad_sum(float v) { v += dppf<XOR1>(v); return v + dppf<XOR2>(v); }
DI f2 sum8(const char* p) {
  f2 s = *(const f2*)p;
#pragma unroll
  for (int w = 1; w < 8; ++w) s += *(const f2*)(p + w * 1024);
  return s;
}
DI f4 rows16(u32 c01, u32 c23, const v4u (&w)[4], const u32 (&sel0)[4], const u32 (&sel1)[4]) {
  f4 acc = {0.f, 0.f, 0.f, 0.f};
  acc = mfma16(diag(c01, sel0), w[0], acc); acc = mfma16(diag(c01, sel1), w[1], acc);
  acc = mfma16(diag(c23, sel0), w[2], acc); acc = mfma16(diag(c23, sel1), w[3], acc);
  return acc;
}

__global__ void __launch_bounds__(512, 2) fused_decode(Params P) {
  constexpr int L_FLAG = 131072;
  __shared__ __attribute__((aligned(16))) char smem[L_FLAG + 64 + 4096];
  const Role R = P.role[blockIdx.x >> 7];
  const u32 t_in = (T_GO | T_A1 | T_Y0 | T_WO) ? (u32)__builtin_amdgcn_s_memrealtime() : 0u;
  auto pace = [&](u32 t) { while ((u32)__builtin_amdgcn_s_memrealtime() - t_in < t) __builtin_amdgcn_s_sleep(1); pin(); };
  const int tid = threadIdx.x, lane = tid & 63, wave = __builtin_amdgcn_readfirstlane(tid >> 6);
  const int bid = blockIdx.x, x = bid & 7, jj = bid >> 3, j = jj & 15;
  const bool isX = jj < 16;
  const int g = lane >> 4, c16 = lane & 15, bsel = c16 >> 3;
  const int h = 4 * x + (j >> 2), r = j & 3, id = x * 16 + j;
  u32 pfc = 0;
  if (tid < 16) {
    u64 pc;
    asm volatile("s_getpc_b64 %0" : "=s"(pc));
    pfc = __builtin_amdgcn_raw_buffer_load_b32(mk((const void*)(pc & ~255ull), CODE_BYTES), tid * 64, jj * 1024, 0);
  }
  if (tid < 16) *(u32*)(smem + L_FLAG + tid * 4) = 0u;
  const int w4 = wave & 3;
  int xv = bsel * 1152 + r * 288 + w4 * 72 + g * 16, xv2 = xv + 64 - g * 14, wv8 = (r * 144 + w4 * 36 + g * 8) * 8192 + h * 256 + c16 * 16;
  int wv9 = wv8 + (32 - 7 * g) * 8192;
  asm volatile("" : "+v"(xv), "+v"(xv2), "+v"(wv8), "+v"(wv9));
  pin();
  const int kq_voff = (c16 & 1) * 64 + g * 16;
  const int gk = 2 * x + (j >> 3), bx = (j >> 2) & 1;
  const int ro = isX ? 0 : 128 * SLOT;
  const rsrc_t ws = mk(R.ws, W_END);
  u32 tag, sel0[4], sel1[4];
  auto make_sel = [&]() {
#pragma unroll
    for (int k = 0; k < 4; ++k) {
      const bool on = ((c16 & 7) >> 1) == k;
      sel0[k] = on ? ((c16 & 1) ? 0x01000c0cu : 0x0c0c0100u) : 0x0c0c0c0cu;
      sel1[k] = on ? ((c16 & 1) ? 0x03020c0cu : 0x0c0c0302u) : 0x0c0c0c0cu;
    }
  };
  constexpr int L_QV = 1024, L_R1 = L_FLAG + 64;
  auto gather_q = [&](int gs, int f) {
    const int gv = (lane >> 5) * SLOT + (w4 & 1) * 1024 + (lane & 31) * 16;
    v4u a, c;
    _Pragma("nounroll") for (int spin = 0; spin < SPIN_CAP; ++spin) {
      pin();
      a = ld4<AUX_SC1>(ws, gv, gs); c = ld4<AUX_SC1>(ws, gv, gs + 2 * SLOT);
      if (none(bad4(bad4(0u, a, tag), c, tag))) break;
      if (f) CNT(1, 1); else CNT(0, 1);
      __builtin_amdgcn_s_sleep(2);
    }
    const float s0 = uf(a.x) + uf(c.x), s1 = uf(a.z) + uf(c.z);
    const u32 pq = pk(swap32_add(s0, s0), swap32_add(s1, s1));
    if (lane < 32) *(u32*)(smem + L_QV + f * 512 + (w4 >> 1) * 256 + 16 * c16 + 8 * g + 4 * (w4 & 1)) = pq;
    post(L_FLAG + 12 + 4 * f, lane);
  };
  auto late = [](int v) { asm volatile("" : "+s"(v)); return v; };
  v4u kk[16];
  size_t kvoff;
  int xo;
  auto kv_setup = [&]() {
    const int hsh = (late(bid) * 2654435761u) >> 20;
    kvoff = ((size_t)(bx * 16 + late(gk)) * 8192 + 6144 + r * 512 + (2 * w4 + (((wave >> 2) ^ (hsh >> (2 + w4))) & 1)) * 64) * 256;
    xo = ((hsh + wave) & 3) * 4096;
  };
  auto load_k = [&](auto lo, auto hi) {
    u64 base = (u64)R.p1 + kvoff;
    asm volatile("" : "+s"(base));
    const rsrc_t rk = mk((const void*)base, 16384);
    const int k_voff = ((c16 & 2) * 2 + (c16 >> 2)) * 256 + kq_voff;
#pragma unroll
    for (int i = decltype(lo)::value; i < decltype(hi)::value; ++i) { kk[i] = ld4<AUX_NT>(rk, k_voff + (i & 1) * 128 + ((i >> 1) & 1) * 2048, ((i >> 2) * 4096) ^ xo); pin(); }
  };
  using I0 = std::integral_constant<int, 0>; using INK = std::integral_constant<int, NK>; using I16 = std::integral_constant<int, 16>;
  const v4u zero4 = {0u, 0u, 0u, 0u};
  auto prewrite = [&]() {
    if (PREW & 2) {
      if (isX ? (w4 < 2 || (w4 == 2 ? r == 3 : lane < 16)) : w4 == 0)
        st4(zero4, ws, lane * 16, W_SLOT + ro + id * SLOT + (isX ? (w4 < 2 ? w4 * 1024 : w4 * 2048) : 5120));
      pin();
    }
  };
  if (__builtin_expect(wave >= 4, 1)) {
    __builtin_amdgcn_s_setprio(2);
    if (T_Y0 && !isX) pace(T_Y0);
    if ((PREW & 1) && (isX || (PREW & 4)) && (w4 < 2 || (w4 == 3 && isX && (PREW & 8)))) st4(zero4, ws, lane * 16, S_H1 + ro + id * SLOT + w4 * 1024);
    v4u w1[9];
    const rsrc_t rx = mk(R.x, 2304), rw = mk(R.wq, 576 * 8192);
    const v4u xc = ld4<0>(rx, xv, 0);
    const u32 xe = __builtin_amdgcn_raw_buffer_load_b16(rx, xv2, 0, 0);
    pin();
#pragma unroll
    for (int i = 0; i < 8; ++i) { w1[i] = ld4<AUX_NT>(rw, wv8, i * 8192); pin(); }
    w1[8] = ld4<AUX_NT>(rw, wv9, 0);
    const u32 ep = __builtin_amdgcn_raw_buffer_load_b32(ws, 0, W_EPOCH, AUX_SC1);
    bar();
    make_sel();
    pin();
    f4 acc = {0.f, 0.f, 0.f, 0.f};
#pragma unroll
    for (int i = 0; i < 4; ++i) { acc = mfma16(diag(xc[i], sel0), w1[2 * i], acc); acc = mfma16(diag(xc[i], sel1), w1[2 * i + 1], acc); }
    acc = mfma16(diag(xe, sel0), w1[8], acc);
    char* rp = smem + L_R1 + lane * 16;
    *(f4*)(rp + w4 * 1024) = acc;
    pin();
    tag = (u32)__builtin_amdgcn_readfirstlane(ep) + 0x9E3779B9u;
    if (tag + 1 < 2) tag += 0x9E3779B9u;
    if (arrive(L_FLAG + 48, lane) == 3) {
      acc = (*(const f4*)rp + *(const f4*)(rp + 1024)) + (*(const f4*)(rp + 2048) + *(const f4*)(rp + 3072));
      st4(v4u{fu(acc[0]), tag, fu(acc[1]), tag}, ws, lane * 16, S_H1 + ro + id * SLOT);
      st4(v4u{fu(acc[2]), tag, fu(acc[3]), tag}, ws, lane * 16 + 1024, S_H1 + ro + id * SLOT);
      *(u32*)(smem + L_FLAG + 28) = tag;
      pin();
      *(volatile LAS u32*)(smem + L_FLAG + 52) = 1;
      if (!isX) ST(10);
    }
    if (__builtin_expect(isX, 1)) {
      spin_flag(smem + L_FLAG + 52);
      ST(1);
      __builtin_amdgcn_s_sleep(D_H1);
      gather_q(S_H1 + (h ^ HX) * 4 * SLOT + (w4 >> 1) * 512, 0);
      ST(2);
      if (T_A1) pace(T_A1);
      if (PWL) spin_flag(smem + L_FLAG + 20, 4);
      if (!PWT) prewrite();
      gather_q(S_H1 + (128 + (2 * gk + (w4 >> 1)) * 4) * SLOT + bx * 512, 1);
      ST(3);
    } else if (!PWT) {
      prewrite();
    }
  } else {
    bar();
    if (__builtin_expect(isX, 1)) {
      __builtin_amdgcn_s_setprio(3);
      const int h2v = bsel * 512 + (c16 & 2) * 32 + g * 16 + S_H2 + id * SLOT + w4 * 128;
      if ((PREW & 16) && (c16 & 5) == 0) st4(zero4, ws, h2v, 0);
      v4u w2[8];
      const rsrc_t ru = mk((const char*)R.p0 + (r * 128 + w4 * 32) * 8192 + (h ^ HX) * 256, 31 * 8192 + 256);
      const int voff = (4 * (c16 >> 2) + ((c16 >> 1) & 1)) * 8192 + kq_voff;
#pragma unroll
      for (int i = 0; i < 8; ++i) { w2[i] = ld4<AUX_NT>(ru, voff + (i & 1) * 128, ((i >> 1) & 1) * 16384 + (i >> 2) * 131072); pin(); }
      make_sel();
      spin_flag(smem + L_FLAG + 12, 4);
      tag = __builtin_amdgcn_readfirstlane(*(const u32*)(smem + L_FLAG + 28));
      const v4u q0 = *(const v4u*)(smem + L_QV + bsel * 256 + kq_voff), q1 = *(const v4u*)(smem + L_QV + bsel * 256 + 128 + kq_voff);
      u32 o[4];
#pragma unroll
      for (int be = 0; be < 4; ++be) {
        f4 d = {0.f, 0.f, 0.f, 0.f};
        d = mfma16(w2[2 * be], q0, d); d = mfma16(w2[2 * be + 1], q1, d);
        o[be] = pk(d[0] + dppf<XOR1>(d[1]), d[2] + dppf<XOR1>(d[3]));
      }
      const bool up = c16 & 2;
      if ((c16 & 5) == 0) st4(v4u{up ? o[2] : o[0], tag, up ? o[3] : o[1], tag}, ws, h2v, 0);
      post(L_FLAG + 20, lane);
      ST(3);
      if (T_GO) pace(T_GO);
    } else {
      make_sel();
    }
  }
  __builtin_amdgcn_s_setprio(1);
  pin();
  constexpr int L_RED = 16384, L_REDE = L_RED + 8192;
  v4u wo[4], we[2];
  auto mk_wo = [&](auto role) {
    u64 base = (u64)R.wo + (h * 128 + wave * 16) * 1152;
    asm volatile("" : "+s"(base));
    return mk((const void*)base, 16 * 1152);
  };
  auto load_wo = [&](auto role) {
    const rsrc_t rwo = mk_wo(role);
    const int voff = g * 4608 + c16 * 16;
#pragma unroll
    for (int i = 0; i < 4; ++i) { wo[i] = ld4<AUX_NT>(rwo, voff + i * 1152, r * 256); pin(); }
  };
  auto load_we = [&](auto role) {
    if (r == 3) {
      const rsrc_t rwo = mk_wo(role);
      const int voff = g * 4608 + bsel * 2304 + (c16 & 7) * 16;
#pragma unroll
      for (int i = 0; i < 2; ++i) { we[i] = ld4<AUX_NT>(rwo, voff + i * 1152, 1024); pin(); }
    }
  };
  constexpr int L_R2 = 53248;

  int hdv, h5v, gsv, dv, redv;
  u32 tl0[4], tl1[4];
  auto tail_setup = [&](auto role) {
    constexpr bool isX = decltype(role)::value;
    constexpr int TS = isX ? SLOT : INB, TB = isX ? W_SLOT : W_H3D;
    hdv = 64 * ((lane >> 1) & 15) + 32 * (lane >> 5) + 16 * (lane & 1) + TB + O_HD + id * TS + wave * 1024;
    h5v = (wave < 2 ? lane * 16 + wave * 1024 : (lane >> 5) * 256 + (lane & 15) * 16 + 1024 + wave * 512) + TB + O_H5 + id * TS;
    gsv = ((c16 >> 2) + (id & (isX ? ~7 : ~3))) * TS + (c16 & 2) * (isX ? 2 * SLOT : 512);
    dv = gsv + g * 32 + (c16 & 1) * 16 + TB + O_HD + (isX ? (h & 1) * 1024 : 0) + wave * 128;
    gsv += S_HX + (h & 1) * 128;
    redv = wave * 1024 + lane * 16;
    asm volatile("" : "+v"(hdv), "+v"(h5v), "+v"(dv), "+v"(redv));
    if (isX) asm volatile("" : "+v"(gsv));
#pragma unroll
    for (int k = 0; k < 4; ++k) {
      const bool on = (c16 >> 2) == k;
      tl0[k] = on ? ((c16 & 1) ? 0x01000c0cu : 0x0c0c0100u) : 0x0c0c0c0cu;
      tl1[k] = on ? ((c16 & 1) ? 0x03020c0cu : 0x0c0c0302u) : 0x0c0c0c0cu;
      asm volatile("" : "+v"(tl0[k]), "+v"(tl1[k]));
    }
    pin();
  };
  auto tail = [&](auto role) {
  constexpr bool isX = decltype(role)::value;
  constexpr int TS = isX ? SLOT : INB, TB = isX ? W_SLOT : W_H3D;
  bar();
  if (isX) ST(7);
  if (wave < 2) {
    const f2 sv = sum8(smem + L_R2 + tid * 8);
    st4(v4u{fu(sv.x), tag, fu(sv.y), tag}, ws, hdv, 0);
  }
  pin();
  if (isX ? WSYNC == 3 : WO_LATE == 5) {
    if (wave >= 2) __builtin_amdgcn_s_sleep(D_WO);
    if (T_WO) pace(isX ? T_WO : T_WOY);
    if (D_WOY) __builtin_amdgcn_s_sleep(D_WOY);
    load_wo(role);
  }
  if (isX && r == 3 && wave >= 2) __builtin_amdgcn_s_sleep(D_WO);
  if (isX || !WEY) load_we(role);
  {
    v4u d0;
    float w = 1.f;
    bool have_w = !isX;
    __builtin_amdgcn_s_sleep(isX ? D_A3 : D_H4);
    ST(8);
    _Pragma("nounroll") for (int spin = 0; spin < SPIN_CAP; ++spin) {
      pin();
      d0 = ld4<AUX_SC1>(ws, dv, 0);
      if (isX && !have_w) {
        const v4u st = ld4<AUX_SC1>(ws, gsv, 0);
        if (none(bad4(0u, st, tag))) {
          float M = uf(st.x);
          M = mx(M, dppf<ROR4>(M)); M = mx(M, dppf<ROR8>(M));
          w = ex2(uf(st.x) - M);
          w *= __builtin_amdgcn_rcpf(row4_sum(w * uf(st.z)));
          have_w = true;
        }
      }
      if (have_w && none(bad4(0u, d0, tag))) break;
      if (isX) CNT(5, 1); else CNT(6, 1);
      if (P_HD) __builtin_amdgcn_s_sleep(P_HD);
    }
    ST(9);
    const u32 cm = pk(row4_sum(w * uf(d0.x)), row4_sum(w * uf(d0.z)));
    const u32 c01 = dppu<0xA0>(cm), c23 = dppu<0xF5>(cm);
    *(f4*)(smem + L_RED + redv) = rows16(c01, c23, wo, tl0, tl1);
    if (r == 3) {
      const f4 z = {0.f, 0.f, 0.f, 0.f};
      const f4 lo = mfma16(diag(c01, tl1), we[1], mfma16(diag(c01, tl0), we[0], z)), hi = mfma16(diag(c23, tl1), we[1], mfma16(diag(c23, tl0), we[0], z));
      *(f4*)(smem + L_REDE + redv) = f4{lo[0] + dppf<ROR8>(hi[0]), lo[1] + dppf<ROR8>(hi[1]), lo[2] + dppf<ROR8>(hi[2]), lo[3] + dppf<ROR8>(hi[3])};
    }
  }
  bar();
  if (wave < 4) {
    if (wave < 2 || (r == 3 && (lane & 16) == 0)) {
      const f2 sv = sum8(smem + L_RED + (wave >> 1) * 8192 + (tid & 127) * 8);
      st4(v4u{fu(sv.x), tag + 1, fu(sv.y), tag + 1}, ws, h5v, 0);
    }
    if (isX) ST(10);
  } else if (wave == 4 && id < 72) {
    const int e = id - 64, L = id < 64 ? id : 64 + (e & 1) + 4 * (e >> 1);
    const int rso = TB + O_H5 + (id < 64 ? (L >> 4) * TS + (L & 15) * 128 : 3 * TS + 2048 + e * 128);
    v4u v[4];
    __builtin_amdgcn_s_sleep(D_H5);
    _Pragma("nounroll") for (int spin = 0; spin < SPIN_CAP; ++spin) {
      pin();
      u32 bad = 0;
#pragma unroll
      for (int i = 0; i < 4; ++i) v[i] = ld4<AUX_SC1>(ws, (lane >> 3) * 4 * TS + (lane & 7) * 16, rso + i * 32 * TS);
#pragma unroll
      for (int i = 0; i < 4; ++i) bad = bad4(bad, v[i], tag + 1);
      if (none(bad)) break;
      CNT(7, 1);
      __builtin_amdgcn_s_sleep(2);
    }
    float a = (uf(v[0].x) + uf(v[1].x)) + (uf(v[2].x) + uf(v[3].x)), c = (uf(v[0].z) + uf(v[1].z)) + (uf(v[2].z) + uf(v[3].z));
    a += dppf<ROR8>(a); c += dppf<ROR8>(c);
    a = swap16_add(a, a); c = swap16_add(c, c);
    a = swap32_add(a, a); c = swap32_add(c, c);
    if (lane < 8) {
      const int al = L & 15;
      const int n = (lane & 1) * 1152 + 128 * (L >> 4) + 32 * (al & 3) + 8 * (lane >> 1) + 2 * (al >> 2);
      ((u32*)R.out)[n >> 1] = pk(a, c);
    }
    if (!isX && id == 0 && lane == 0) __builtin_amdgcn_raw_buffer_store_b32(tag, ws, 0, W_EPOCH, AUX_SC1);
  }
  };

  if (isX) {
    constexpr int L_ML = 0;
    v4u vv[16];
    kv_setup();
    if (wave >= 4) tail_setup(std::true_type{});
    {
      const rsrc_t rv = mk((const char*)R.p2 + kvoff, 16384);
      load_k(I0{}, I16{});
      if (KSYNC && wave >= 4) { post(L_FLAG + 40, lane); spin_flag(smem + L_FLAG + 40, 4); }
#pragma unroll
      for (int i = 0; i < 16; ++i) { vv[i] = ld4<AUX_NT>(rv, lane * 16 + (i & 3) * 1024, ((i >> 2) * 4096) ^ xo); pin(); }
      if (WSYNC == 1) post(L_FLAG + 44, lane);
    }
    ST(4);
    if (wave < 4) {
      if (PWT) prewrite();
      tail_setup(std::true_type{});
      if (PWT) asm volatile("s_waitcnt vmcnt(0)" ::: "memory");
    }
    asm volatile("" ::"v"(pfc));
    spin_flag(smem + L_FLAG + 16, 4);
    ST(5);
    float sc[16];
    {
      const v4u q0 = *(const v4u*)(smem + L_QV + 512 + bsel * 256 + kq_voff), q1 = *(const v4u*)(smem + L_QV + 512 + bsel * 256 + 128 + kq_voff);
      const bool odd = c16 & 1;
#pragma unroll
      for (int be = 0; be < 8; ++be) {
        f4 d = {0.f, 0.f, 0.f, 0.f};
        d = mfma16(kk[2 * be], q0, d); d = mfma16(kk[2 * be + 1], q1, d);
        const float u0 = odd ? d[1] : d[0], u1 = odd ? d[3] : d[2];
        sc[2 * be] = u0 + dppf<XOR1>(u0); sc[2 * be + 1] = u1 + dppf<XOR1>(u1);
      }
    }
    float m = mx(mx(mx(sc[0], sc[1], sc[2]), mx(sc[3], sc[4], sc[5]), mx(sc[6], sc[7], sc[8])), mx(mx(sc[9], sc[10], sc[11]), mx(sc[12], sc[13], sc[14]), sc[15]));
    m = swap32_max(swap16_max(m)) * SC;
    float l = 0.f;
#pragma unroll
    for (int be = 0; be < 8; ++be) {
      const float p0 = ex2(fmaf(sc[2 * be], SC, -m)), p1 = ex2(fmaf(sc[2 * be + 1], SC, -m));
      l += p0 + p1;
      const u32 pp = pk(p0, p1);
      kk[2 * be] = diag(pp, sel0); kk[2 * be + 1] = diag(pp, sel1);
    }
    l = swap16_add(l, l); l = swap32_add(l, l);
    if ((lane & 0x37) == 0) { *(float*)(smem + L_ML + bsel * 64 + wave * 4) = m; *(float*)(smem + L_ML + bsel * 64 + 32 + wave * 4) = l; }
    post(L_FLAG + 36, lane);
#pragma unroll
    for (int i = 0; i < 16; i += 4) asm volatile("" : "+v"(kk[i]), "+v"(kk[i + 1]), "+v"(kk[i + 2]), "+v"(kk[i + 3]));
    if (WSYNC == 1) spin_flag(smem + L_FLAG + 44, 8);
    else __builtin_amdgcn_s_setprio(0);
    if (WSYNC < 2) load_wo(std::true_type{});
    spin_flag(smem + L_FLAG + 36, 8);
    const char* p = smem + L_ML + (lane >> 5) * 64;
    const f4 m0 = *(const f4*)p, m1 = *(const f4*)(p + 16);
    const float M = mx(mx(mx(m0[0], m0[1]), mx(m0[2], m0[3])), mx(mx(m1[0], m1[1]), mx(m1[2], m1[3])));
    const float fo = ex2(*(const float*)(p + wave * 4) - M);
    float L = ex2(*(const float*)(p + (lane & 7) * 4) - M) * *(const float*)(p + 32 + (lane & 7) * 4);
    pin();
    f4 acc = {0.f, 0.f, 0.f, 0.f};
#pragma unroll
    for (int i = 0; i < 16; ++i) acc = mfma16(kk[i], vv[i], acc);
    *(f4*)(smem + L_R2 + redv) = acc * fo;
    ST(6);
    if (wave == 2) {
      L = quad_sum(L); L += dppf<HMIR>(L);
      if ((lane & 24) == 0) st4(v4u{fu(M), tag, fu(L), tag}, ws, (lane >> 5) * 128 + (lane & 7) * 16, S_HX + id * SLOT);
    }
    tail(std::true_type{});
  } else {
    constexpr int L_Q = 0, L_P = 32768, L_MX = 40960, L_LS = 41472, L_LAT = 45056, L_ST = 49152, L_CNT = L_FLAG + 32;
    const int b = id >> 6;
    __builtin_amdgcn_s_waitcnt(0x0070);
    v4u fr[32], wv[4];
    const int r32 = lane & 31, hh = lane >> 5;
    const int row_base = r32 * 1024 + 16 * (hh ^ (((r32 & 3) << 2) | ((r32 >> 2) & 3)));
    const rsrc_t ruv = mk((const char*)R.p1 + (r * 128 + 64 * (wave >> 2) + 8 * w4) * 8192 + h * 256, 39 * 8192 + 256);
#define LDUV(i) wv[i] = ld4<AUX_NT>(ruv, g * 16384 + c16 * 16, (((i) >> 1) + 32 * ((i) & 1)) * 8192)
#define QCNT(rq) (L_FLAG + ((rq) < 3 ? 36 + 4 * (rq) : 56))
    f16v s;
    if (wave < 4) {
      const rsrc_t rc = SPREAD ? mk((const char*)R.p0 + (size_t)id * 131072 + wave * 32768, 32768) : mk((const char*)R.p0 + (size_t)id * 131072 + wave * 8192, 131072 - wave * 8192);
      __builtin_amdgcn_s_waitcnt(0xc07f);
      const int hsh = (late(bid) * 2654435761u) >> 20;
      const int kz = ((hsh >> 3) + wave) & 3;
      const int xi = hsh & 4, lx = lane ^ ((2 * wave + (xi >> 2)) & 3);
#pragma unroll
      for (int i = 0; i < 32; ++i) {
        const int ro = (i >> 3) * 32768 + ((i & 7) ^ xi) * 1024;
        __builtin_amdgcn_raw_ptr_buffer_load_lds(rc, (LAS void*)(smem + wave * 8192 + ro), 16, 16 * (lx ^ (((i & 3) << 2) | ((i >> 2) & 1))), SPREAD ? ((i >> 3) ^ kz) * 8192 + ((i & 7) ^ xi) * 1024 : ro, 0, AUX_NT);
        pin();
        if (i >= WY - 1 && i < NY) asm volatile("s_waitcnt vmcnt(%0)" ::"i"(WY - 1) : "memory");
        if (i == 23 || i == 31) {
          asm volatile("s_waitcnt vmcnt(16)" ::: "memory");
          if (lane == 0) asm volatile("ds_add_u32 %0, %1" ::"v"(L_FLAG + 4 * ((i >> 3) - 2)), "v"(1u) : "memory");
          pin();
        }
      }
      int qa[8], fa[8];
#pragma unroll
      for (int t = 0; t < 8; ++t) { qa[t] = row_base ^ (32 * t); fa[t] = qa[t] + wave * 32768; asm volatile("" : "+v"(qa[t]), "+v"(fa[t])); }
      asm volatile("s_waitcnt vmcnt(8)" ::: "memory");
      post(L_FLAG + 8, lane);
      auto landed = [&]() {
        bar();
        asm volatile("s_waitcnt vmcnt(0)" ::: "memory");
        post(L_FLAG + 12, lane);
        ST(1);
      };
      if (wave == 3) landed();
      spin_flag_asm(L_FLAG + 4 * wave, 4);
#pragma unroll
      for (int t = 0; t < 32; ++t)
        asm volatile("ds_read_b128 %0, %1 offset:%2" : "=v"(fr[t]) : "v"(fa[t & 7]), "i"(256 * (t >> 3)) : "memory");
      pin();
      if (wave != 3) landed();
      __builtin_amdgcn_s_waitcnt(0x0f70);
      asm volatile("" ::"v"(pfc));
      v4u qb[4];
#define QRD(t) (*(const v4u*)(smem + L_Q + qa[(t) & 7] + 256 * ((t) >> 3)))
      spin_flag(smem + L_FLAG + 36, 4);
      ST(2);
      u32 rdy = 0;
#pragma unroll
      for (int t = 0; t < 4; ++t) qb[t] = QRD(t);
#pragma unroll
      for (int i = 0; i < 16; ++i) s[i] = 0.f;
      pin();
#pragma unroll
      for (int t = 0; t < 32; ++t) {
        s = mfma32(fr[t], qb[t & 3], s);
        __builtin_amdgcn_sched_barrier(0);
        if ((t & 7) == 4 && t < 24) {
          if (__builtin_expect((u32)__builtin_amdgcn_readfirstlane(rdy) < 4, 0)) spin_flag(smem + QCNT((t >> 3) + 1), 4);
          pin();
        }
        if ((t & 7) == 1 && t < 24) rdy = *(volatile const LAS u32*)(smem + QCNT((t >> 3) + 1));
        if (t + 4 < 32) qb[t & 3] = QRD(t + 4);
        if (t >= 21 && t < 25) LDUV(t - 21);
        __builtin_amdgcn_sched_barrier(0);
      }
      ST(3);
      float m = mx(mx(mx(s[0], s[1], s[2]), mx(s[3], s[4], s[5]), mx(s[6], s[7], s[8])), mx(mx(s[9], s[10], s[11]), mx(s[12], s[13], s[14]), s[15]));
      m = swap32_max(m);
      if (lane < 32) *(float*)(smem + L_MX + lane * 16 + wave * 4) = m;
    } else {
      const int q = c16 >> 2, p = lane & 3;
      const int base = (4 * hh + q) * 1024 + w4 * 256 + 16 * ((2 * (g & 1) + (p >> 1)) ^ (4 * q + hh)) + 8 * (p & 1);
      auto frags = [&](auto blk) {
        constexpr int kb = decltype(blk)::value;
        spin_flag(smem + L_FLAG + 4 * kb, 4);
#pragma unroll
        for (int ta = 0; ta < 4; ++ta)
#pragma unroll
          for (int sh = 0; sh < 2; ++sh) {
            const char* pa = smem + kb * 32768 + sh * 16384;
            const v2u a = __builtin_bit_cast(v2u, __builtin_amdgcn_ds_read_tr16_b64_v4i16((LAS s4v*)(pa + (base ^ (64 * ta)))));
            const v2u c = __builtin_bit_cast(v2u, __builtin_amdgcn_ds_read_tr16_b64_v4i16((LAS s4v*)(pa + (base ^ (64 * ta) ^ 32) + 8192)));
            fr[ta * 8 + 2 * kb + sh] = v4u{a.x, a.y, c.x, c.y};
          }
        pin();
      };
      v4u qg[16];
      int qvo = S_H2 + (32 * w4 + 4 * hh) * SLOT + b * 512 + r32 * 16;
      asm volatile("" : "+v"(qvo));
#define LDQ(rq, i) qg[4 * (rq) + (i)] = ld4<AUX_SC1>(ws, qvo, (8 * (i) + (rq)) * SLOT)
      int qw[4];
#pragma unroll
      for (int i = 0; i < 4; ++i) {
        const int hd = (8 * w4 + 2 * i + hh) ^ HX;
        qw[i] = hd * 1024 + 16 * ((r32 >> 1) ^ (((hd & 3) << 2) | ((hd >> 2) & 3))) + 8 * (lane & 1);
        asm volatile("" : "+v"(qw[i]));
      }
      frags(std::integral_constant<int, 0>{});
      frags(std::integral_constant<int, 1>{});
      frags(std::integral_constant<int, 2>{});
      if (!EARLYQ) { bar(); spin_flag(smem + L_FLAG + 12, 4); }
#pragma unroll
      for (int i = 0; i < 16; ++i) { LDQ(i >> 2, i & 3); pin(); }
      if (EARLYQ) bar();
#pragma unroll
      for (int rq = 0; rq < 4; ++rq) {
        u32 bad = 0;
#pragma unroll
        for (int i = 0; i < 4; ++i) bad = bad4(bad, qg[4 * rq + i], tag);
        if (__builtin_expect(!none(bad), 0)) {
          _Pragma("nounroll") for (int spin = 0; spin < SPIN_CAP; ++spin) {
            pin();
            CNT(2, 1);
            bad = 0;
#pragma unroll
            for (int i = 0; i < 4; ++i) LDQ(rq, i);
#pragma unroll
            for (int i = 0; i < 4; ++i) bad = bad4(bad, qg[4 * rq + i], tag);
            if (none(bad)) break;
            __builtin_amdgcn_s_sleep(4);
          }
        }
#pragma unroll
        for (int i = 0; i < 4; ++i) *(v2u*)(smem + L_Q + qw[i] + 256 * rq) = v2u{qg[4 * rq + i].x, qg[4 * rq + i].z};
        post(QCNT(rq), lane);
      }
      ST(3);
#pragma unroll
      for (int i = 0; i < 4; ++i) { LDUV(i); pin(); }
      frags(std::integral_constant<int, 3>{});
    }
    bar();
    if (wave < 4) {
      const f4 mm = *(const f4*)(smem + L_MX + r32 * 16);
      const float m = mx(mx(mm[0], mm[1]), mx(mm[2], mm[3])) * SC;
      u32 pp[8];
#pragma unroll
      for (int i = 0; i < 8; ++i) {
        s[2 * i] = ex2(fmaf(s[2 * i], SC, -m)); s[2 * i + 1] = ex2(fmaf(s[2 * i + 1], SC, -m));
        pp[i] = pk(s[2 * i], s[2 * i + 1]);
      }
      *(v4u*)(smem + L_P + wave * 2048 + lane * 16) = v4u{pp[0], pp[1], pp[2], pp[3]};
      *(v4u*)(smem + L_P + wave * 2048 + 1024 + lane * 16) = v4u{pp[4], pp[5], pp[6], pp[7]};
    }
    bar();
    pin();
    if (wave < 4) {
      float l = 0.f;
#pragma unroll
      for (int i = 0; i < 8; ++i) l += s[2 * i] + s[2 * i + 1];
      l = swap32_add(l, l);
      if (lane < 32) *(float*)(smem + L_LS + lane * 16 + wave * 4) = l;
      tag = __builtin_amdgcn_readfirstlane(*(const u32*)(smem + L_FLAG + 28));
      if (PWT) { prewrite(); asm volatile("s_waitcnt vmcnt(0)" ::: "memory"); }
    }
    ST(4);
    if (wave >= 4) {
      v4u pf[8];
#pragma unroll
      for (int ks = 0; ks < 8; ++ks) pf[ks] = *(const v4u*)(smem + L_P + ks * 1024 + lane * 16);
      f16v o0, o1, o2, o3;
#pragma unroll
      for (int i = 0; i < 16; ++i) { o0[i] = 0.f; o1[i] = 0.f; o2[i] = 0.f; o3[i] = 0.f; }
      const int voff = hh * 16 * INB + r32 * 4, soff = W_H3D + w4 * INB + id * 256;
      pin();
#pragma unroll
      for (int ks = 0; ks < 8; ++ks) { o0 = mfma32(pf[ks], fr[ks], o0); o1 = mfma32(pf[ks], fr[8 + ks], o1); }
      __builtin_amdgcn_sched_barrier(0);
#define OST(a, c, i, pi) __builtin_amdgcn_raw_buffer_store_b32(pk(a[i], c[i]), ws, voff + (pi) * 128, soff + (((i) & 3) + 8 * ((i) >> 2)) * 4 * INB, AUX_SC1)
#pragma unroll
      for (int ks = 0; ks < 8; ++ks) {
        o2 = mfma32(pf[ks], fr[16 + ks], o2);
        __builtin_amdgcn_sched_barrier(0);
        if (ks) OST(o0, o1, 2 * ks - 2, 0);
        __builtin_amdgcn_sched_barrier(0);
        o3 = mfma32(pf[ks], fr[24 + ks], o3);
        __builtin_amdgcn_sched_barrier(0);
        if (ks) OST(o0, o1, 2 * ks - 1, 0);
        __builtin_amdgcn_sched_barrier(0);
      }
      OST(o0, o1, 14, 0); OST(o0, o1, 15, 0);
#pragma unroll
      for (int i = 0; i < 16; ++i) OST(o2, o3, i, 1);
    }
    bar();
    ST(5);
    if (wave >= 4) {
      __builtin_amdgcn_s_setprio(3);
      const int hd = (lane >> 3) * 4 + (lane & 3);
      const f4 mm = *(const f4*)(smem + L_MX + hd * 16), ll = *(const f4*)(smem + L_LS + hd * 16);
      v4u sv = {fu(mx(mx(mm[0], mm[1]), mx(mm[2], mm[3])) * SC), tag, fu((ll[0] + ll[1]) + (ll[2] + ll[3])), tag};
      asm volatile("s_waitcnt vmcnt(0)" : "+v"(sv)::"memory");
      if (arrive(L_CNT, lane) == 3) st4(sv, ws, lane * 16, S_HY + (128 + id) * SLOT);
      __builtin_amdgcn_s_setprio(0);
      ST(6);
    }
    pin();
    if (!WO_LATE) load_wo(std::false_type{});
    if (WO_LATE == 4) { spin_flag(smem + L_CNT, 4); if (wave < 4) __builtin_amdgcn_s_sleep(2); load_wo(std::false_type{}); }
    tail_setup(std::false_type{});
    if (wave < 4 && WO_LATE != 4) __builtin_amdgcn_s_sleep(D_H3 + 14); else __builtin_amdgcn_s_sleep(D_H3);
    {
      v4u st;
      _Pragma("nounroll") for (int spin = 0; spin < SPIN_CAP; ++spin) {
        pin();
        st = ld4<AUX_SC1>(ws, (lane >> 2) * SLOT, S_HY + (128 + wave * 16) * SLOT + x * 128 + (h & 3) * 16);
        if (none(bad4(0u, st, tag))) break;
        CNT(3, 1);
        __builtin_amdgcn_s_sleep(P_H3);
      }
      pin();
      ST(7);
      if (D_G3) __builtin_amdgcn_s_sleep(D_G3);
      v4u d[4];
#pragma unroll
      for (int i = 0; i < 4; ++i) d[i] = ld4<AUX_SC1>(ws, g * 1024 + c16 * 16 + i * 256, W_H3D + id * INB + wave * 4096);
      pin();
      float M = uf(st.x);
      M = mx(M, dppf<ROR4>(M)); M = mx(M, dppf<ROR8>(M)); M = swap32_max(swap16_max(M));
      float wg = ex2(uf(st.x) - M);
      float L = wg * uf(st.z);
      L += dppf<ROR4>(L); L += dppf<ROR8>(L); L = swap16_add(L, L); L = swap32_add(L, L);
      if (lane == 0) *(f2*)(smem + L_ST + wave * 8) = f2{M, L};
      bar();
      const f4 s0 = *(const f4*)(smem + L_ST + (wave >> 2) * 32), s1 = *(const f4*)(smem + L_ST + (wave >> 2) * 32 + 16);
      const float MM = mx(mx(s0[0], s0[2], s1[0]), s1[2]);
      const float LL = fmaf(ex2(s0[0] - MM), s0[1], fmaf(ex2(s0[2] - MM), s0[3], fmaf(ex2(s1[0] - MM), s1[1], ex2(s1[2] - MM) * s1[3])));
      wg *= ex2(M - MM) * __builtin_amdgcn_rcpf(LL);
      const u32 w01 = pk(dppf<BCAST>(wg), dppf<BCAST + 4>(wg)), w23 = pk(dppf<BCAST + 8>(wg), dppf<BCAST + 12>(wg));
      v4u af[4] = {diag(w01, sel0), diag(w01, sel1), diag(w23, sel0), diag(w23, sel1)};
      asm volatile("" : "+v"(af[0]), "+v"(af[1]), "+v"(af[2]), "+v"(af[3]));
      pin();
      if (WO_LATE == 3) load_wo(std::false_type{});
      f4 lat = {0.f, 0.f, 0.f, 0.f};
#pragma unroll
      for (int i = 0; i < 4; ++i) lat = mfma16(af[i], d[i], lat);
      if (lane < 32) *(f4*)(smem + L_LAT + wave * 512 + c16 * 32 + g * 16) = lat;
      pin();
      if (WEY) load_we(std::false_type{});
    }
    bar();
    if (WO_LATE == 2) load_wo(std::false_type{});
    {
      const char* p = smem + L_LAT + bsel * 2048 + wave * 64 + g * 16;
      const f4 c = (*(const f4*)p + *(const f4*)(p + 512)) + (*(const f4*)(p + 1024) + *(const f4*)(p + 1536));
      *(f4*)(smem + L_R2 + redv) = rows16(pk(c[0], c[1]), pk(c[2], c[3]), wv, sel0, sel1);
    }
    tail(std::false_type{});
  }
}

extern "C" void kernel_launch(void* const* d_in, const int* in_sizes, int n_in, void* d_out, int out_size, void* d_ws, size_t ws_size,
                              hipStream_t stream) {
  Params p;
  char* w = (char*)d_ws + (WS_AT_END ? ((ws_size - (18 << 20)) & ~(size_t)0x1fffff) : 0);
  p.role[0] = Role{d_in[5], d_in[7], w, d_in[8], d_in[1], d_in[2], d_in[4], d_out};
  p.role[1] = Role{d_in[0], d_in[3], w, d_in[6], d_in[9], d_in[9], d_in[10], (char*)d_out + 1152};
  fused_decode<<<256, 512, 0, stream>>>(p);
}
```

```cpp
#include <hip/hip_runtime.h>
#include <hip/hip_bf16.h>
#include <stdint.h>
#include <type_traits>

typedef unsigned u32;
typedef unsigned long long u64;
typedef u32 v4u __attribute__((ext_vector_type(4)));
typedef u32 v2u __attribute__((ext_vector_type(2)));
typedef float f2 __attribute__((ext_vector_type(2)));
typedef float f4 __attribute__((ext_vector_type(4)));
typedef float f16v __attribute__((ext_vector_type(16)));
typedef __bf16 bf8 __attribute__((ext_vector_type(8)));
typedef __bf16 bf2 __attribute__((ext_vector_type(2)));
typedef short s4v __attribute__((ext_vector_type(4)));
typedef __amdgpu_buffer_rsrc_t rsrc_t;
#define LAS __attribute__((address_space(3)))
#define DI __device__ __forceinline__
#define NOUNROLL _Pragma("nounroll")
template <int N> using IC = std::integral_constant<int, N>;

constexpr int AUX_NT = 2, AUX_SC1 = 16;
constexpr int W_EPOCH = 0;
constexpr int SLOT = 16896, W_SLOT = 4096;
constexpr int S_HD = W_SLOT;
constexpr int S_H1 = W_SLOT + 2048;
constexpr int S_H5 = S_H1;
constexpr int S_H2 = W_SLOT + 5120;
constexpr int S_HY = W_SLOT + 5120;
constexpr int S_HX = W_SLOT + 6144;
constexpr int INB = 4352, PLANE = 128 * INB, W_H3 = 5 << 20;
constexpr int W_END = W_H3 + 8 * PLANE;
constexpr float SC = 0.08838834764831845f * 1.4426950408889634f;
constexpr int CODE_BYTES = 20224;
constexpr int SPIN_GLOBAL = 1 << 20, SPIN_LDS = 1 << 24;
constexpr int D_H1 = 20, D_H3 = 30, P_H3 = 6, D_HX = 26, D_HY = 16, D_H5 = 14, D_WO = 4, D_WOY = 2;

struct Role {
  const void *x, *wq;
  void* ws;
  const void *p0, *p1, *p2, *wo;
  void* out;
};
struct Params { Role role[2]; };

DI rsrc_t mk(const void* p, u32 bytes) { return __builtin_amdgcn_make_buffer_rsrc((void*)p, 0, bytes, 0x00020000); }
DI rsrc_t mk_here(const void* p, size_t off, u32 bytes) {
  u64 base = (u64)p + off;
  asm volatile("" : "+s"(base));
  return mk((const void*)base, bytes);
}
template <int AUX> DI v4u ld4(rsrc_t r, int voff, int soff) { return __builtin_amdgcn_raw_buffer_load_b128(r, voff, soff, AUX); }
DI void st4(v4u v, rsrc_t r, int voff) { __builtin_amdgcn_raw_buffer_store_b128(v, r, voff, 0, AUX_SC1); }
DI u32 fu(float f) { return __builtin_bit_cast(u32, f); }
DI float uf(u32 u) { return __builtin_bit_cast(float, u); }
DI v4u gran(float a, float b, u32 tag) { return v4u{fu(a), tag, fu(b), tag}; }
DI u32 pk(float lo, float hi) { f2 f = {lo, hi}; return __builtin_bit_cast(u32, __builtin_convertvector(f, bf2)); }
template <int C> DI u32 dppu(u32 x) { return __builtin_amdgcn_mov_dpp(x, C, 0xf, 0xf, true); }
template <int C> DI float dppf(float x) { return uf(dppu<C>(fu(x))); }
constexpr int XOR1 = 0xB1, XOR2 = 0x4E, EVEN = 0xA0, ODD = 0xF5, HMIR = 0x141, ROR4 = 0x124, ROR8 = 0x128, BCAST = 0x150;
DI f4 mfma16(v4u a, v4u b, f4 c) { return __builtin_amdgcn_mfma_f32_16x16x32_bf16(__builtin_bit_cast(bf8, a), __builtin_bit_cast(bf8, b), c, 0, 0, 0); }
DI f16v mfma32(v4u a, v4u b, f16v c) { return __builtin_amdgcn_mfma_f32_32x32x16_bf16(__builtin_bit_cast(bf8, a), __builtin_bit_cast(bf8, b), c, 0, 0, 0); }
DI float mx(float a, float b) { return __builtin_elementwise_maximum(a, b); }
DI float mx(float a, float b, float c) { return mx(mx(a, b), c); }
DI float mx4(f4 v) { return mx(mx(v[0], v[1], v[2]), v[3]); }
template <class T> DI float mx16(const T& s) {
  return mx(mx(mx(s[0], s[1], s[2]), mx(s[3], s[4], s[5]), mx(s[6], s[7], s[8])), mx(mx(s[9], s[10], s[11]), mx(s[12], s[13], s[14]), s[15]));
}
DI float ex2(float x) { return __builtin_amdgcn_exp2f(x); }
DI float swap32_add(float a, float b) { auto s = __builtin_amdgcn_permlane32_swap(fu(a), fu(b), false, false); return uf(s[0]) + uf(s[1]); }
DI float swap16_add(float a, float b) { auto s = __builtin_amdgcn_permlane16_swap(fu(a), fu(b), false, false); return uf(s[0]) + uf(s[1]); }
DI float swap32_max(float a) { auto s = __builtin_amdgcn_permlane32_swap(fu(a), fu(a), false, false); return mx(uf(s[0]), uf(s[1])); }
DI float swap16_max(float a) { auto s = __builtin_amdgcn_permlane16_swap(fu(a), fu(a), false, false); return mx(uf(s[0]), uf(s[1])); }
DI float row4_sum(float v) { v += dppf<ROR4>(v); return v + dppf<ROR8>(v); }
DI float row4_max(float v) { v = mx(v, dppf<ROR4>(v)); return mx(v, dppf<ROR8>(v)); }
DI void pin() { asm volatile("" ::: "memory"); __builtin_amdgcn_sched_barrier(0); }
DI void bar() {
  __builtin_amdgcn_sched_barrier(0);
  asm volatile("s_waitcnt lgkmcnt(0)\n\ts_barrier" ::: "memory");
  __builtin_amdgcn_sched_barrier(0);
}
template <int N> DI void wait_vm() { asm volatile("s_waitcnt vmcnt(%0)" ::"i"(N) : "memory"); }
DI u32 bad4(u32 acc, v4u v, u32 tag) {
  return __builtin_amdgcn_bitop3_b32(__builtin_amdgcn_bitop3_b32(acc, v.y, tag, 0xF6), v.w, tag, 0xF6);
}
DI bool none(u32 bad) { return __builtin_amdgcn_ballot_w64(bad != 0) == 0; }

DI void post(u32 addr) {
  __builtin_amdgcn_sched_barrier(0);
  asm volatile("s_mov_b64 exec, 1\n\tds_add_u32 %0, %1\n\ts_mov_b64 exec, -1" ::"v"(addr), "v"(1u) : "memory");
  __builtin_amdgcn_sched_barrier(0);
}
DI u32 arrive(u32 addr) {
  u32 v, old;
  __builtin_amdgcn_sched_barrier(0);
  asm volatile("s_mov_b64 exec, 1\n\tds_add_rtn_u32 %0, %2, %3\n\ts_mov_b64 exec, -1\n\ts_waitcnt lgkmcnt(0)\n\tv_readfirstlane_b32 %1, %0"
               : "=&v"(v), "=s"(old) : "v"(addr), "v"(1u) : "memory");
  __builtin_amdgcn_sched_barrier(0);
  return old;
}
template <int WANT> DI void await(u32 addr) {
  u32 v, t, n;
  __builtin_amdgcn_sched_barrier(0);
  asm volatile(
      "s_mov_b32 %2, %5\n"
      "1:\n\tds_read_b32 %0, %3\n\ts_waitcnt lgkmcnt(0)\n\tv_readfirstlane_b32 %1, %0\n\ts_cmp_ge_u32 %1, %4\n\ts_cbranch_scc1 2f\n\t"
      "s_sleep 1\n\ts_sub_u32 %2, %2, 1\n\ts_cbranch_scc0 1b\n"
      "2:"
      : "=&v"(v), "=&s"(t), "=&s"(n) : "v"(addr), "i"(WANT), "i"(SPIN_LDS) : "memory", "scc");
  __builtin_amdgcn_sched_barrier(0);
}

DI v4u diag(u32 pair, const u32 (&sel)[4]) {
  v4u a;
#pragma unroll
  for (int k = 0; k < 4; ++k) a[k] = __builtin_amdgcn_perm(pair, pair, sel[k]);
  return a;
}
DI void make_sel(int dw, bool odd, u32 (&lo)[4], u32 (&hi)[4]) {
  const u32 l = odd ? 0x01000c0cu : 0x0c0c0100u, u = odd ? 0x03020c0cu : 0x0c0c0302u;
#pragma unroll
  for (int k = 0; k < 4; ++k) { lo[k] = dw == k ? l : 0x0c0c0c0cu; hi[k] = dw == k ? u : 0x0c0c0c0cu; }
}
DI f4 rows16(u32 c01, u32 c23, const v4u (&w)[4], const u32 (&lo)[4], const u32 (&hi)[4]) {
  f4 acc = {0.f, 0.f, 0.f, 0.f};
  acc = mfma16(diag(c01, lo), w[0], acc); acc = mfma16(diag(c01, hi), w[1], acc);
  acc = mfma16(diag(c23, lo), w[2], acc); acc = mfma16(diag(c23, hi), w[3], acc);
  return acc;
}
DI f2 sum8(const char* p) {
  f2 s[8];
#pragma unroll
  for (int w = 0; w < 8; ++w) s[w] = *(const f2*)(p + w * 1024);
  __builtin_amdgcn_sched_barrier(0);
  return ((s[0] + s[1]) + (s[2] + s[3])) + ((s[4] + s[5]) + (s[6] + s[7]));
}

__global__ void __launch_bounds__(512, 2) fused_decode(Params P) {
  constexpr int L_C = 131072, L_R1 = L_C + 64;
  constexpr int C_TAG = L_C + 28, C_PROJ = L_C + 48, C_PUB = L_C + 52;
  constexpr int C_QB = L_C + 12, C_QA = L_C + 16, C_ABS = L_C + 20, C_SCORE = L_C + 36, C_VAL = L_C + 44;
  constexpr int C_BLK = L_C, C_FREE = L_C + 16, C_SUMS = L_C + 24, C_DRAIN = L_C + 32;
#define C_QUART(q) (L_C + ((q) < 3 ? 36 + 4 * (q) : 56))
  constexpr int L_QV = 1024, L_RED = 16384, L_REDE = L_RED + 8192, L_R2 = 53248;
  __shared__ __attribute__((aligned(16))) char smem[L_R1 + 4096];

  const Role R = P.role[blockIdx.x >> 7];
  const int tid = threadIdx.x, lane = tid & 63, wave = __builtin_amdgcn_readfirstlane(tid >> 6);
  const int bid = blockIdx.x, x = bid & 7, jj = bid >> 3, j = jj & 15;
  const bool isX = jj < 16;
  const int g = lane >> 4, c16 = lane & 15, bsel = c16 >> 3;
  const int h = 4 * x + (j >> 2), r = j & 3, id = x * 16 + j;
  u32 pfc = 0;
  if (tid < 16) {
    u64 pc;
    asm volatile("s_getpc_b64 %0" : "=s"(pc));
    pfc = __builtin_amdgcn_raw_buffer_load_b32(mk((const void*)(pc & ~255ull), CODE_BYTES), tid * 64, jj * 1024, 0);
  }
  if (tid < 16) *(u32*)(smem + L_C + tid * 4) = 0u;
  const int w4 = wave & 3;
  int xv = bsel * 1152 + r * 288 + w4 * 72 + g * 16, xv2 = xv + 64 - g * 14;
  int wv8 = (r * 144 + w4 * 36 + g * 8) * 8192 + h * 256 + c16 * 16, wv9 = wv8 + (32 - 7 * g) * 8192;
  asm volatile("" : "+v"(xv), "+v"(xv2), "+v"(wv8), "+v"(wv9));
  pin();
  auto late = [](int v) { asm volatile("" : "+s"(v)); return v; };
  const int kq_voff = (c16 & 1) * 64 + g * 16;
  const int gk = 2 * x + (j >> 3), bx = (j >> 2) & 1;
  const rsrc_t ws = mk(R.ws, W_END);
  const v4u zero4 = {0u, 0u, 0u, 0u};
  u32 tag, sel0[4], sel1[4];

  if (__builtin_expect(wave >= 4, 1)) {
    __builtin_amdgcn_s_setprio(2);
    v4u w1[9];
    const rsrc_t rx = mk(R.x, 2304), rw = mk(R.wq, 576 * 8192);
    const v4u xc = ld4<0>(rx, xv, 0);
    const u32 xe = __builtin_amdgcn_raw_buffer_load_b16(rx, xv2, 0, 0);
    pin();
#pragma unroll
    for (int i = 0; i < 8; ++i) { w1[i] = ld4<AUX_NT>(rw, wv8, i * 8192); pin(); }
    w1[8] = ld4<AUX_NT>(rw, wv9, 0);
    const u32 ep = __builtin_amdgcn_raw_buffer_load_b32(ws, 0, W_EPOCH, AUX_SC1);
    bar();
    make_sel((c16 & 7) >> 1, c16 & 1, sel0, sel1);
    pin();
    f4 acc = {0.f, 0.f, 0.f, 0.f};
#pragma unroll
    for (int i = 0; i < 4; ++i) { acc = mfma16(diag(xc[i], sel0), w1[2 * i], acc); acc = mfma16(diag(xc[i], sel1), w1[2 * i + 1], acc); }
    acc = mfma16(diag(xe, sel0), w1[8], acc);
    char* rp = smem + L_R1 + lane * 16;
    *(f4*)(rp + w4 * 1024) = acc;
    pin();
    tag = (u32)__builtin_amdgcn_readfirstlane(ep) + 0x9E3779B9u;
    if (tag + 1 < 2) tag += 0x9E3779B9u;
    if (arrive(C_PROJ) == 3) {
      acc = (*(const f4*)rp + *(const f4*)(rp + 1024)) + (*(const f4*)(rp + 2048) + *(const f4*)(rp + 3072));
      const int so = S_H1 + ((isX ? 0 : 128) + id) * SLOT + lane * 16;
      st4(gran(acc[0], acc[1], tag), ws, so);
      st4(gran(acc[2], acc[3], tag), ws, so + 1024);
      *(u32*)(smem + C_TAG) = tag;
      post(C_PUB);
    }
    if (__builtin_expect(isX, 1)) {
      auto gather_q = [&](int gs, int f) {
        const int gv = (lane >> 5) * SLOT + (w4 & 1) * 1024 + (lane & 31) * 16;
        v4u a, c;
        NOUNROLL for (int spin = 0; spin < SPIN_GLOBAL; ++spin) {
          pin();
          a = ld4<AUX_SC1>(ws, gv, gs); c = ld4<AUX_SC1>(ws, gv, gs + 2 * SLOT);
          if (none(bad4(bad4(0u, a, tag), c, tag))) break;
          __builtin_amdgcn_s_sleep(2);
        }
        const float s0 = uf(a.x) + uf(c.x), s1 = uf(a.z) + uf(c.z);
        const u32 pq = pk(swap32_add(s0, s0), swap32_add(s1, s1));
        if (lane < 32) *(u32*)(smem + L_QV + f * 512 + (w4 >> 1) * 256 + 16 * c16 + 8 * g + 4 * (w4 & 1)) = pq;
        post(f ? C_QA : C_QB);
      };
      await<1>(C_PUB);
      __builtin_amdgcn_s_sleep(D_H1);
      gather_q(S_H1 + h * 4 * SLOT + (w4 >> 1) * 512, 0);
      await<4>(C_ABS);
      gather_q(S_H1 + (128 + (2 * gk + (w4 >> 1)) * 4) * SLOT + bx * 512, 1);
    }
  } else {
    bar();
    if (__builtin_expect(isX, 1)) {
      __builtin_amdgcn_s_setprio(3);
      v4u w2[8];
      const rsrc_t ru = mk((const char*)R.p0 + (r * 128 + w4 * 32) * 8192 + h * 256, 31 * 8192 + 256);
      const int voff = (4 * (c16 >> 2) + ((c16 >> 1) & 1)) * 8192 + kq_voff;
#pragma unroll
      for (int i = 0; i < 8; ++i) { w2[i] = ld4<AUX_NT>(ru, voff + (i & 1) * 128, ((i >> 1) & 1) * 16384 + (i >> 2) * 131072); pin(); }
      make_sel((c16 & 7) >> 1, c16 & 1, sel0, sel1);
      int h2v = (c16 & 5) ? 0x40000000 : bsel * 512 + (c16 & 2) * 32 + g * 16 + S_H2 + id * SLOT + w4 * 128, qv = bsel * 256 + kq_voff;
      asm volatile("" : "+v"(h2v), "+v"(qv));
#pragma unroll
      for (int k = 0; k < 4; ++k) asm volatile("" : "+v"(sel0[k]), "+v"(sel1[k]));
      await<1>(C_PUB);
      tag = __builtin_amdgcn_readfirstlane(*(const u32*)(smem + C_TAG));
      asm volatile("" : "+s"(tag));
      await<4>(C_QB);
      const v4u q0 = *(const v4u*)(smem + L_QV + qv), q1 = *(const v4u*)(smem + L_QV + 128 + qv);
      u32 o[4];
#pragma unroll
      for (int be = 0; be < 4; ++be) {
        f4 d = {0.f, 0.f, 0.f, 0.f};
        d = mfma16(w2[2 * be], q0, d); d = mfma16(w2[2 * be + 1], q1, d);
        o[be] = pk(d[0] + dppf<XOR1>(d[1]), d[2] + dppf<XOR1>(d[3]));
      }
      const bool up = c16 & 2;
      st4(v4u{up ? o[2] : o[0], tag, up ? o[3] : o[1], tag}, ws, h2v);
      post(C_ABS);
    } else {
      make_sel((c16 & 7) >> 1, c16 & 1, sel0, sel1);
    }
  }
  __builtin_amdgcn_s_setprio(1);
  pin();

  v4u wo[4], we[2];
  auto load_wo = [&]() {
    const rsrc_t rwo = mk_here(R.wo, (h * 128 + wave * 16) * 1152, 16 * 1152);
    const int voff = g * 4608 + c16 * 16;
#pragma unroll
    for (int i = 0; i < 4; ++i) { wo[i] = ld4<AUX_NT>(rwo, voff + i * 1152, r * 256); pin(); }
  };
  auto load_we = [&]() {
    const rsrc_t rwo = mk_here(R.wo, (h * 128 + wave * 16) * 1152, 16 * 1152);
    const int voff = g * 4608 + bsel * 2304 + (c16 & 7) * 16;
#pragma unroll
    for (int i = 0; i < 2; ++i) { we[i] = ld4<AUX_NT>(rwo, voff + i * 1152, 1024); pin(); }
  };
  int hdv, h5v, gsv, dv, redv, sumv;
  u32 tl0[4], tl1[4];
  auto tail_setup = [&](auto role) {
    constexpr bool X = decltype(role)::value;
    constexpr int TS = X ? SLOT : INB;
    const int bd = X ? S_HD : W_H3 + (h & 7) * PLANE, b5 = X ? S_H5 : W_H3 + ((h + 4) & 7) * PLANE;
    hdv = 64 * ((lane >> 1) & 15) + 32 * (lane >> 5) + 16 * (lane & 1) + bd + id * TS + wave * 1024;
    h5v = (wave < 2 ? lane * 16 + wave * 1024 : (lane & 16) * 0x4000000 + (lane >> 5) * 256 + c16 * 16 + 1024 + wave * 512) + b5 + id * TS;
    sumv = (wave >> 1) * 8192 + (tid & 127) * 8;
    gsv = ((c16 >> 2) + (id & (X ? ~7 : ~3))) * TS + (c16 & 2) * (X ? 2 * SLOT : 512);
    dv = gsv + g * 32 + (c16 & 1) * 16 + bd + (X ? (h & 1) * 1024 : 0) + wave * 128;
    gsv += S_HX + (h & 1) * 128;
    redv = wave * 1024 + lane * 16;
    asm volatile("" : "+v"(hdv), "+v"(h5v), "+v"(dv), "+v"(redv), "+v"(sumv));
    if (X) asm volatile("" : "+v"(gsv));
    make_sel(c16 >> 2, c16 & 1, tl0, tl1);
#pragma unroll
    for (int k = 0; k < 4; ++k) asm volatile("" : "+v"(tl0[k]), "+v"(tl1[k]));
    pin();
  };
  auto tail = [&](auto role) {
    constexpr bool X = decltype(role)::value;
    constexpr int TS = X ? SLOT : INB;
    bar();
    if (wave < 2) {
      const f2 sv = sum8(smem + L_R2 + sumv);
      st4(gran(sv.x, sv.y, tag), ws, hdv);
    }
    pin();
    if (!X || r == 3) {
      if (wave >= 2) __builtin_amdgcn_s_sleep(D_WO);
      if (X) load_we();
      else { __builtin_amdgcn_s_sleep(D_WOY); load_wo(); }
    }
    __builtin_amdgcn_s_sleep(X ? D_HX : D_HY);
    v4u d0 = ld4<AUX_SC1>(ws, dv, 0);
    float w = 1.f;
    if (X) {
      v4u st;
      NOUNROLL for (int spin = 0; spin < SPIN_GLOBAL; ++spin) {
        pin();
        st = ld4<AUX_SC1>(ws, gsv, 0);
        if (none(bad4(0u, st, tag))) break;
        __builtin_amdgcn_s_sleep(3);
      }
      w = ex2(uf(st.x) - row4_max(uf(st.x)));
      w *= __builtin_amdgcn_rcpf(row4_sum(w * uf(st.z)));
      asm volatile("" : "+v"(w));
    }
    NOUNROLL for (int spin = 0; spin < SPIN_GLOBAL; ++spin) {
      if (none(bad4(0u, d0, tag))) break;
      if (spin) __builtin_amdgcn_s_sleep(2);
      pin();
      d0 = ld4<AUX_SC1>(ws, dv, 0);
    }
    const u32 cm = pk(row4_sum(w * uf(d0.x)), row4_sum(w * uf(d0.z)));
    const u32 c01 = dppu<EVEN>(cm), c23 = dppu<ODD>(cm);
    *(f4*)(smem + L_RED + redv) = rows16(c01, c23, wo, tl0, tl1);
    if (r == 3) {
      const f4 z = {0.f, 0.f, 0.f, 0.f};
      const f4 lo = mfma16(diag(c01, tl1), we[1], mfma16(diag(c01, tl0), we[0], z));
      const f4 hi = mfma16(diag(c23, tl1), we[1], mfma16(diag(c23, tl0), we[0], z));
      *(f4*)(smem + L_REDE + redv) = f4{lo[0] + dppf<ROR8>(hi[0]), lo[1] + dppf<ROR8>(hi[1]), lo[2] + dppf<ROR8>(hi[2]), lo[3] + dppf<ROR8>(hi[3])};
    }
    bar();
    if (wave < (r == 3 ? 4 : 2)) {
      const f2 sv = sum8(smem + L_RED + sumv);
      st4(gran(sv.x, sv.y, tag + 1), ws, h5v);
    }
    pin();
    if (wave == 4 && id < 72) {
      const int e = id - 64, L = id < 64 ? id : 64 + (e & 1) + 4 * (e >> 1), al = L & 15;
      int rv = (X ? S_H5 : W_H3 + ((lane >> 3) ^ 4) * PLANE) + (id < 64 ? (L >> 4) * TS + al * 128 : 3 * TS + 2048 + e * 128) +
               (lane >> 3) * 4 * TS + (lane & 7) * 16;
      int ov = lane < 8 ? 2 * ((lane & 1) * 1152 + 128 * (L >> 4) + 32 * (al & 3) + 8 * (lane >> 1) + 2 * (al >> 2)) : 0x40000000;
      asm volatile("" : "+v"(rv), "+v"(ov));
      v4u v[4];
      __builtin_amdgcn_s_sleep(D_H5);
      NOUNROLL for (int spin = 0; spin < SPIN_GLOBAL; ++spin) {
        pin();
        u32 bad = 0;
#pragma unroll
        for (int i = 0; i < 4; ++i) v[i] = ld4<AUX_SC1>(ws, rv, i * 32 * TS);
#pragma unroll
        for (int i = 0; i < 4; ++i) bad = bad4(bad, v[i], tag + 1);
        if (none(bad)) break;
      }
      float a = swap32_add((uf(v[0].x) + uf(v[1].x)) + (uf(v[2].x) + uf(v[3].x)), (uf(v[0].z) + uf(v[1].z)) + (uf(v[2].z) + uf(v[3].z)));
      a += dppf<ROR8>(a);
      a = swap16_add(a, a);
      const auto pr = __builtin_amdgcn_permlane32_swap(fu(a), fu(a), false, false);
      __builtin_amdgcn_raw_buffer_store_b32(pk(uf(pr[0]), uf(pr[1])), mk(R.out, 3456), ov, 0, 0);
      if (!X && id == 0 && lane == 0) __builtin_amdgcn_raw_buffer_store_b32(tag, ws, 0, W_EPOCH, AUX_SC1);
    }
  };

  if (isX) {
    constexpr int L_ML = 0;
    v4u kk[16], vv[16];
    const int hsh = (late(bid) * 2654435761u) >> 20;
    const size_t kvoff = ((size_t)(bx * 16 + late(gk)) * 8192 + 6144 + r * 64 + (2 * w4 + (((wave >> 2) ^ (hsh >> (2 + w4))) & 1)) * 256) * 256;
    const int xo = ((hsh + wave) & 3) * 4096;
    if (wave >= 4) tail_setup(std::true_type{});
    auto load_v = [&]() {
      const rsrc_t rv = mk_here(R.p2, kvoff, 16384);
#pragma unroll
      for (int i = 0; i < 16; ++i) { vv[i] = ld4<AUX_NT>(rv, lane * 16 + (i & 3) * 1024, ((i >> 2) * 4096) ^ xo); pin(); }
      post(C_VAL);
    };
    __builtin_amdgcn_s_setprio(2);
    {
      const rsrc_t rk = mk_here(R.p1, kvoff, 16384);
      const int voff = ((c16 & 2) * 2 + (c16 >> 2)) * 256 + kq_voff;
#pragma unroll
      for (int i = 0; i < 16; ++i) { kk[i] = ld4<AUX_NT>(rk, voff + (i & 1) * 128 + ((i >> 1) & 1) * 2048, ((i >> 2) * 4096) ^ xo); pin(); }
    }
    __builtin_amdgcn_s_setprio(1);
    if (wave < 4) {
      if (w4 < 2 || (w4 == 2 ? r == 3 : lane < 16)) st4(zero4, ws, lane * 16 + W_SLOT + id * SLOT + (w4 < 2 ? w4 * 1024 : w4 * 2048));
      pin();
      load_v();
      tail_setup(std::true_type{});
      wait_vm<0>();
    }
    asm volatile("" ::"v"(pfc));
    await<4>(C_QA);
    float sc[16];
    {
      const v4u q0 = *(const v4u*)(smem + L_QV + 512 + bsel * 256 + kq_voff), q1 = *(const v4u*)(smem + L_QV + 512 + bsel * 256 + 128 + kq_voff);
      const bool odd = c16 & 1;
#pragma unroll
      for (int be = 0; be < 8; ++be) {
        f4 d = {0.f, 0.f, 0.f, 0.f};
        d = mfma16(kk[2 * be], q0, d); d = mfma16(kk[2 * be + 1], q1, d);
        const float u0 = odd ? d[1] : d[0], u1 = odd ? d[3] : d[2];
        sc[2 * be] = u0 + dppf<XOR1>(u0); sc[2 * be + 1] = u1 + dppf<XOR1>(u1);
      }
    }
    const float m = swap32_max(swap16_max(mx16(sc))) * SC;
    float l = 0.f;
#pragma unroll
    for (int be = 0; be < 8; ++be) {
      const float p0 = ex2(fmaf(sc[2 * be], SC, -m)), p1 = ex2(fmaf(sc[2 * be + 1], SC, -m));
      l += p0 + p1;
      const u32 pp = pk(p0, p1);
      kk[2 * be] = diag(pp, sel0); kk[2 * be + 1] = diag(pp, sel1);
    }
    l = swap16_add(l, l); l = swap32_add(l, l);
    if ((lane & 0x37) == 0) { *(float*)(smem + L_ML + bsel * 64 + wave * 4) = m; *(float*)(smem + L_ML + bsel * 64 + 32 + wave * 4) = l; }
    post(C_SCORE);
#pragma unroll
    for (int i = 0; i < 16; i += 4) asm volatile("" : "+v"(kk[i]), "+v"(kk[i + 1]), "+v"(kk[i + 2]), "+v"(kk[i + 3]));
    const char* p = smem + L_ML + (lane >> 5) * 64;
    float M, fo;
    auto scale = [&]() {
      await<8>(C_SCORE);
      M = mx(mx4(*(const f4*)p), mx4(*(const f4*)(p + 16)));
      fo = ex2(*(const float*)(p + wave * 4) - M);
      asm volatile("" : "+v"(fo));
      pin();
    };
    f4 acc = {0.f, 0.f, 0.f, 0.f};
    if (wave < 4) {
#pragma unroll
      for (int i = 0; i < 16; ++i) acc = mfma16(kk[i], vv[i], acc);
      asm volatile("" : "+v"(acc));
      scale();
      await<8>(C_VAL);
      load_wo();
      if (wave == 2) {
        float L = ex2(*(const float*)(p + (lane & 7) * 4) - M) * *(const float*)(p + 32 + (lane & 7) * 4);
        L += dppf<XOR1>(L); L += dppf<XOR2>(L); L += dppf<HMIR>(L);
        if ((lane & 24) == 0) st4(gran(M, L, tag), ws, (lane >> 5) * 128 + (lane & 7) * 16 + S_HX + id * SLOT);
      }
    } else {
      load_v();
      scale();
#pragma unroll
      for (int i = 0; i < 16; ++i) {
        if (i == 12) { asm volatile("" : "+v"(acc)); pin(); load_wo(); }
        acc = mfma16(kk[i], vv[i], acc);
      }
    }
    *(f4*)(smem + L_R2 + redv) = acc * fo;
    tail(std::true_type{});
  } else {
    constexpr int L_Q = 0, L_P = 32768, L_MX = 40960, L_LS = 41472, L_LAT = 45056, L_ST = 49152;
    const int b = id >> 6;
    __builtin_amdgcn_s_waitcnt(0x0070);
    v4u fr[32], wv[4];
    const int r32 = lane & 31, hh = lane >> 5;
    const rsrc_t ruv = mk((const char*)R.p1 + (r * 128 + 64 * (wave >> 2) + 8 * w4) * 8192 + h * 256, 39 * 8192 + 256);
#define LDUV(i) wv[i] = ld4<AUX_NT>(ruv, g * 16384 + c16 * 16, (((i) >> 1) + 32 * ((i) & 1)) * 8192)
    f16v s;
    int voff[4];
    if (wave < 4) {
      const rsrc_t rc = mk((const char*)R.p0 + (id >> 3) * 1048576 + wave * 262144 + (id & 7) * 32768, 32768);
      __builtin_amdgcn_s_waitcnt(0xc07f);
      const int hsh = (late(bid) * 2654435761u) >> 20;
      const int kz = ((hsh >> 3) + wave) & 3, xi = hsh & 4, lx = lane ^ ((2 * wave + (xi >> 2)) & 3);
#pragma unroll
      for (int i = 0; i < 32; ++i) {
        const int row = ((i & 7) ^ xi) * 1024;
        __builtin_amdgcn_raw_ptr_buffer_load_lds(rc, (LAS void*)(smem + (i >> 3) * 32768 + wave * 8192 + row), 16,
                                                 16 * (lx ^ (((i & 3) << 2) | ((i >> 2) & 1))), ((i >> 3) ^ kz) * 8192 + row, 0, AUX_NT);
        pin();
        if (i == 23 || i == 31) { wait_vm<16>(); post(C_BLK + 4 * ((i >> 3) - 2)); }
      }
      int qa[8], fa[8];
#pragma unroll
      for (int t = 0; t < 8; ++t) {
        qa[t] = (r32 * 1024 + 16 * (hh ^ (((r32 & 3) << 2) | ((r32 >> 2) & 3)))) ^ (32 * t);
        fa[t] = qa[t] + wave * 32768;
        asm volatile("" : "+v"(qa[t]), "+v"(fa[t]));
      }
      wait_vm<8>();
      post(C_BLK + 8);
      if (wave == 3) { wait_vm<0>(); post(C_BLK + 12); }
      await<4>(C_BLK + 4 * wave);
#pragma unroll
      for (int t = 0; t < 32; ++t) asm volatile("ds_read_b128 %0, %1 offset:%2" : "=v"(fr[t]) : "v"(fa[t & 7]), "i"(256 * (t >> 3)) : "memory");
      pin();
      if (wave < 2) post(C_FREE);
      if (wave != 3) { wait_vm<0>(); post(C_BLK + 12); }
      __builtin_amdgcn_s_waitcnt(0x0f70);
      asm volatile("" ::"v"(pfc));
      v4u qb[4];
#define QRD(t) (*(const v4u*)(smem + L_Q + qa[(t) & 7] + 256 * ((t) >> 3)))
      await<4>(C_QUART(0));
      u32 rdy = 0;
#pragma unroll
      for (int t = 0; t < 4; ++t) qb[t] = QRD(t);
#pragma unroll
      for (int i = 0; i < 16; ++i) s[i] = 0.f;
      pin();
#pragma unroll
      for (int t = 0; t < 32; ++t) {
        s = mfma32(fr[t], qb[t & 3], s);
        __builtin_amdgcn_sched_barrier(0);
        if ((t & 7) == 4 && t < 24) {
          if (__builtin_expect((u32)__builtin_amdgcn_readfirstlane(rdy) < 4, 0)) await<4>(C_QUART((t >> 3) + 1));
          pin();
        }
        if ((t & 7) == 2 && t < 24) rdy = *(volatile const LAS u32*)(smem + C_QUART((t >> 3) + 1));
        if (t + 4 < 32) qb[t & 3] = QRD(t + 4);
        if (t >= 21 && t < 25) LDUV(t - 21);
        __builtin_amdgcn_sched_barrier(0);
      }
      const float m = swap32_max(mx16(s));
      if (lane < 32) *(float*)(smem + L_MX + lane * 16 + wave * 4) = m;
    } else {
      const int q = c16 >> 2, p = lane & 3;
      const int base = (4 * hh + q) * 1024 + w4 * 256 + 16 * ((2 * (g & 1) + (p >> 1)) ^ (4 * q + hh)) + 8 * (p & 1);
      auto frags = [&](auto blk) {
        constexpr int kb = decltype(blk)::value;
        await<4>(C_BLK + 4 * kb);
#pragma unroll
        for (int ta = 0; ta < 4; ++ta)
#pragma unroll
          for (int sh = 0; sh < 2; ++sh) {
            const char* pa = smem + kb * 32768 + sh * 16384;
            const v2u a = __builtin_bit_cast(v2u, __builtin_amdgcn_ds_read_tr16_b64_v4i16((LAS s4v*)(pa + (base ^ (64 * ta)))));
            const v2u c = __builtin_bit_cast(v2u, __builtin_amdgcn_ds_read_tr16_b64_v4i16((LAS s4v*)(pa + (base ^ (64 * ta) ^ 32) + 8192)));
            fr[ta * 8 + 2 * kb + sh] = v4u{a.x, a.y, c.x, c.y};
          }
        pin();
      };
      v4u qg[16];
      int qvo = S_H2 + (32 * w4 + 4 * hh) * SLOT + b * 512 + r32 * 16;
      asm volatile("" : "+v"(qvo));
#define LDQ(rq, i) qg[4 * (rq) + (i)] = ld4<AUX_SC1>(ws, qvo, (8 * (i) + (rq)) * SLOT)
      int qw[4];
#pragma unroll
      for (int i = 0; i < 4; ++i) {
        const int hd = 8 * w4 + 2 * i + hh;
        qw[i] = hd * 1024 + 16 * ((r32 >> 1) ^ (((hd & 3) << 2) | ((hd >> 2) & 3))) + 8 * (lane & 1);
        asm volatile("" : "+v"(qw[i]));
      }
      frags(IC<0>{});
      frags(IC<1>{});
      post(C_FREE);
      frags(IC<2>{});
#pragma unroll
      for (int i = 0; i < 16; ++i) { LDQ(i >> 2, i & 3); pin(); }
      await<6>(C_FREE);
#pragma unroll
      for (int rq = 0; rq < 4; ++rq) {
        u32 bad = 0;
#pragma unroll
        for (int i = 0; i < 4; ++i) bad = bad4(bad, qg[4 * rq + i], tag);
        if (__builtin_expect(!none(bad), 0)) {
          NOUNROLL for (int spin = 0; spin < SPIN_GLOBAL; ++spin) {
            pin();
            bad = 0;
#pragma unroll
            for (int i = 0; i < 4; ++i) LDQ(rq, i);
#pragma unroll
            for (int i = 0; i < 4; ++i) bad = bad4(bad, qg[4 * rq + i], tag);
            if (none(bad)) break;
            __builtin_amdgcn_s_sleep(4);
          }
        }
#pragma unroll
        for (int i = 0; i < 4; ++i) *(v2u*)(smem + L_Q + qw[i] + 256 * rq) = v2u{qg[4 * rq + i].x, qg[4 * rq + i].z};
        post(C_QUART(rq));
      }
#pragma unroll
      for (int i = 0; i < 4; ++i) { LDUV(i); pin(); }
      frags(IC<3>{});
#pragma unroll
      for (int k = 0; k < 4; ++k) { voff[k] = ((x + k + 4 * hh) & 7) * PLANE + (4 * hh + k) * 4 * INB + r32 * 4; asm volatile("" : "+v"(voff[k])); }
    }
    bar();
    if (wave < 4) {
      const float m = mx4(*(const f4*)(smem + L_MX + r32 * 16)) * SC;
      u32 pp[8];
#pragma unroll
      for (int i = 0; i < 8; ++i) {
        s[2 * i] = ex2(fmaf(s[2 * i], SC, -m)); s[2 * i + 1] = ex2(fmaf(s[2 * i + 1], SC, -m));
        pp[i] = pk(s[2 * i], s[2 * i + 1]);
      }
      *(v4u*)(smem + L_P + wave * 2048 + lane * 16) = v4u{pp[0], pp[1], pp[2], pp[3]};
      *(v4u*)(smem + L_P + wave * 2048 + 1024 + lane * 16) = v4u{pp[4], pp[5], pp[6], pp[7]};
    }
    bar();
    if (wave < 4) {
      float l = 0.f;
#pragma unroll
      for (int i = 0; i < 8; ++i) l += s[2 * i] + s[2 * i + 1];
      l = swap32_add(l, l);
      if (lane < 32) *(float*)(smem + L_LS + lane * 16 + wave * 4) = l;
      tag = __builtin_amdgcn_readfirstlane(*(const u32*)(smem + C_TAG));
      if (wave == 0) { st4(zero4, ws, lane * 16 + S_HY + (128 + id) * SLOT); wait_vm<0>(); }
      post(C_SUMS);
    }
    pin();
    if (wave >= 4) {
      v4u pf[8];
#pragma unroll
      for (int ks = 0; ks < 8; ++ks) pf[ks] = *(const v4u*)(smem + L_P + ks * 1024 + lane * 16);
      f16v o0, o1, o2, o3;
#pragma unroll
      for (int i = 0; i < 16; ++i) { o0[i] = 0.f; o1[i] = 0.f; o2[i] = 0.f; o3[i] = 0.f; }
      const int soff = W_H3 + w4 * INB + j * 256;
      pin();
#pragma unroll
      for (int ks = 0; ks < 8; ++ks) { o0 = mfma32(pf[ks], fr[ks], o0); o1 = mfma32(pf[ks], fr[8 + ks], o1); }
      __builtin_amdgcn_sched_barrier(0);
#define OST(a, c, i, pi) __builtin_amdgcn_raw_buffer_store_b32(pk(a[i], c[i]), ws, voff[(i) & 3] + (pi) * 128, soff + ((i) >> 2) * 32 * INB, AUX_SC1)
#pragma unroll
      for (int ks = 0; ks < 8; ++ks) {
        o2 = mfma32(pf[ks], fr[16 + ks], o2);
        __builtin_amdgcn_sched_barrier(0);
        if (ks) OST(o0, o1, 2 * ks - 2, 0);
        __builtin_amdgcn_sched_barrier(0);
        o3 = mfma32(pf[ks], fr[24 + ks], o3);
        __builtin_amdgcn_sched_barrier(0);
        if (ks) OST(o0, o1, 2 * ks - 1, 0);
        __builtin_amdgcn_sched_barrier(0);
      }
      OST(o0, o1, 14, 0); OST(o0, o1, 15, 0);
#pragma unroll
      for (int i = 0; i < 16; ++i) OST(o2, o3, i, 1);
      await<4>(C_SUMS);
      const int hd = (lane >> 3) * 4 + (lane & 3);
      const f4 ll = *(const f4*)(smem + L_LS + hd * 16);
      v4u sv = gran(mx4(*(const f4*)(smem + L_MX + hd * 16)) * SC, (ll[0] + ll[1]) + (ll[2] + ll[3]), tag);
      int fv = lane * 16 + S_HY + (128 + id) * SLOT;
      asm volatile("s_waitcnt vmcnt(0)" : "+v"(sv), "+v"(fv)::"memory");
      if (arrive(C_DRAIN) == 3) st4(sv, ws, fv);
    }
    pin();
    tail_setup(std::false_type{});
    await<4>(C_DRAIN);
    __builtin_amdgcn_s_sleep(D_H3);
    {
      v4u st;
      NOUNROLL for (int spin = 0; spin < SPIN_GLOBAL; ++spin) {
        pin();
        st = ld4<AUX_SC1>(ws, (lane >> 2) * SLOT, S_HY + (128 + wave * 16) * SLOT + x * 128 + (h & 3) * 16);
        if (none(bad4(0u, st, tag))) break;
        __builtin_amdgcn_s_sleep(P_H3);
      }
      pin();
      v4u d[4];
#pragma unroll
      for (int i = 0; i < 4; ++i) d[i] = ld4<AUX_SC1>(ws, g * 1024 + c16 * 16 + i * 256, W_H3 + ((wave + h) & 7) * PLANE + id * INB);
      pin();
      const float M = swap32_max(swap16_max(row4_max(uf(st.x))));
      float wg = ex2(uf(st.x) - M);
      float L = row4_sum(wg * uf(st.z));
      L = swap16_add(L, L); L = swap32_add(L, L);
      if (lane == 0) *(f2*)(smem + L_ST + wave * 8) = f2{M, L};
      bar();
      const f4 s0 = *(const f4*)(smem + L_ST + (wave >> 2) * 32), s1 = *(const f4*)(smem + L_ST + (wave >> 2) * 32 + 16);
      const float MM = mx(mx(s0[0], s0[2], s1[0]), s1[2]);
      const float LL = fmaf(ex2(s0[0] - MM), s0[1], fmaf(ex2(s0[2] - MM), s0[3], fmaf(ex2(s1[0] - MM), s1[1], ex2(s1[2] - MM) * s1[3])));
      wg *= ex2(M - MM) * __builtin_amdgcn_rcpf(LL);
      const u32 w01 = pk(dppf<BCAST>(wg), dppf<BCAST + 4>(wg)), w23 = pk(dppf<BCAST + 8>(wg), dppf<BCAST + 12>(wg));
      v4u af[4] = {diag(w01, sel0), diag(w01, sel1), diag(w23, sel0), diag(w23, sel1)};
      asm volatile("" : "+v"(af[0]), "+v"(af[1]), "+v"(af[2]), "+v"(af[3]));
      pin();
      f4 lat = {0.f, 0.f, 0.f, 0.f};
#pragma unroll
      for (int i = 0; i < 4; ++i) lat = mfma16(af[i], d[i], lat);
      if (lane < 32) *(f4*)(smem + L_LAT + wave * 512 + c16 * 32 + g * 16) = lat;
      pin();
      if (r == 3) load_we();
    }
    bar();
    {
      const char* p = smem + L_LAT + bsel * 2048 + wave * 64 + g * 16;
      const f4 c = (*(const f4*)p + *(const f4*)(p + 512)) + (*(const f4*)(p + 1024) + *(const f4*)(p + 1536));
      *(f4*)(smem + L_R2 + redv) = rows16(pk(c[0], c[1]), pk(c[2], c[3]), wv, sel0, sel1);
    }
    tail(std::false_type{});
  }
}

extern "C" void kernel_launch(void* const* d_in, const int* in_sizes, int n_in, void* d_out, int out_size, void* d_ws, size_t ws_size,
                              hipStream_t stream) {
  Params p;
  p.role[0] = Role{d_in[5], d_in[7], d_ws, d_in[8], d_in[1], d_in[2], d_in[4], d_out};
  p.role[1] = Role{d_in[0], d_in[3], d_ws, d_in[6], d_in[9], d_in[9], d_in[10], (char*)d_out + 1152};
  fused_decode<<<256, 512, 0, stream>>>(p);
}
```

```cpp
#define STAMP 1
#define REP_IDX 10
#define REP_ROLE 0
#define REP_STAT 1
#define SMASK 3073
#include <hip/hip_runtime.h>
#include <hip/hip_bf16.h>
#include <stdint.h>
#include <type_traits>

typedef unsigned u32;
typedef unsigned long long u64;
typedef u32 v4u __attribute__((ext_vector_type(4)));
typedef u32 v2u __attribute__((ext_vector_type(2)));
typedef float f2 __attribute__((ext_vector_type(2)));
typedef float f4 __attribute__((ext_vector_type(4)));
typedef float f16v __attribute__((ext_vector_type(16)));
typedef __bf16 bf8 __attribute__((ext_vector_type(8)));
typedef __bf16 bf2 __attribute__((ext_vector_type(2)));
typedef short s4v __attribute__((ext_vector_type(4)));
typedef __amdgpu_buffer_rsrc_t rsrc_t;
#define LAS __attribute__((address_space(3)))
#define DI __device__ __forceinline__

constexpr int AUX_NT = 2, AUX_SC1 = 16;
constexpr int W_EPOCH = 0;
constexpr int SLOT = 16896, W_SLOT = 4096;
constexpr int O_HD = 0;
constexpr int O_H5 = 2048;
constexpr int S_H1 = W_SLOT + 2048;
constexpr int S_H2 = W_SLOT + 5120;
constexpr int S_HY = W_SLOT + 5120;
constexpr int S_HX = W_SLOT + 6144;
constexpr int INB = 4352, PLANE = 128 * INB;
constexpr int W_H3D = 5 << 20;
constexpr int W_END = W_H3D + 8 * PLANE;
constexpr int W_STAMP = 16 << 20;
constexpr float SC = 0.08838834764831845f * 1.4426950408889634f;
#define KNOB(n, v) constexpr int n = v
#define K_D_H1 20
#define K_WX 2
#define K_NX 12
#define K_WY 2
#define K_NY 0
#define K_DXA 20
#define K_P_H3 6
#define P_HD 2
#define K_D_A3 22
#define K_D_H3 30
#define K_D_H4 16
#define K_D_H5 14
#define EARLYQ 1
#define K_D_H2 0
#define WS_AT_END 0
#define SPX 1
#define SPY 1
#define SPREAD 1
#define WE_EARLY 0
#define NK 0
#define T_WO 0
#define T_WOY T_WO
#define D_WO 4
#define D_WOY 12
#define D_G3 0
#define UVL 0
#define RDY 2
#define P_H5 0
#define OUT_AUX 0
#define GATE 8
#define WFLAG 0
#define NOB5 1
#define PSPLIT 0
#define WOP 12
#define KFIRST 1
#define PWX 2
#define T_H3 0
#define QA 4
#define QSYNC 0
#define PWT 1
#define WEY 1
#define PWL 1
#define PREW 2
#define HX 0
#define KSYNC 1
#define WSYNC 1
#define T_2 0
#define T_3 0
#define T_4 0
#define T_5 0
#define T_GO 0
#define T_A1 0
#define T_Y0 0
#define D_H1A 16
#define WO_LATE 5
#define K_DX0 0
#define ABL 0
#define CODE_BYTES 22528
constexpr int D_H1 = K_D_H1, WX = K_WX, NX = K_NX, WY = K_WY, NY = K_NY, DXA = K_DXA, P_H3 = K_P_H3, D_A3 = K_D_A3, D_H3 = K_D_H3, D_H4 = K_D_H4,
              D_H5 = K_D_H5, D_H2 = K_D_H2;
constexpr int SPIN_CAP = 1 << 17;

struct Role {
  const void *x, *wq;
  void* ws;
  const void *p0, *p1, *p2, *wo;
  void* out;
};
struct Params { Role role[2]; };

DI rsrc_t mk(const void* p, u32 bytes) { return __builtin_amdgcn_make_buffer_rsrc((void*)p, 0, bytes, 0x00020000); }
template <int AUX> DI v4u ld4(rsrc_t r, int voff, int soff) { return __builtin_amdgcn_raw_buffer_load_b128(r, voff, soff, AUX); }
template <int AUX> DI v2u ld2(rsrc_t r, int voff, int soff) { return __builtin_amdgcn_raw_buffer_load_b64(r, voff, soff, AUX); }
DI void st4(v4u v, rsrc_t r, int voff, int soff) { __builtin_amdgcn_raw_buffer_store_b128(v, r, voff + soff, 0, AUX_SC1); }
DI u32 fu(float f) { return __builtin_bit_cast(u32, f); }
DI float uf(u32 u) { return __builtin_bit_cast(float, u); }
DI u32 pk(float lo, float hi) { f2 f = {lo, hi}; return __builtin_bit_cast(u32, __builtin_convertvector(f, bf2)); }
DI float blo(u32 u) { return uf(u << 16); }
DI float bhi(u32 u) { return uf(u & 0xffff0000u); }
template <int C> DI u32 dppu(u32 x) { return __builtin_amdgcn_mov_dpp(x, C, 0xf, 0xf, true); }
template <int C> DI float dppf(float x) { return uf(dppu<C>(fu(x))); }
constexpr int XOR1 = 0xB1, XOR2 = 0x4E, HMIR = 0x141, ROR4 = 0x124, ROR8 = 0x128, BCAST = 0x150;
DI f4 mfma16(v4u a, v4u b, f4 c) { return __builtin_amdgcn_mfma_f32_16x16x32_bf16(__builtin_bit_cast(bf8, a), __builtin_bit_cast(bf8, b), c, 0, 0, 0); }
DI f16v mfma32(v4u a, v4u b, f16v c) { return __builtin_amdgcn_mfma_f32_32x32x16_bf16(__builtin_bit_cast(bf8, a), __builtin_bit_cast(bf8, b), c, 0, 0, 0); }
DI float mx(float a, float b) { return __builtin_elementwise_maximum(a, b); }
DI float mx(float a, float b, float c) { return mx(mx(a, b), c); }
DI float mx4(f4 v) { return mx(mx(v[0], v[1], v[2]), v[3]); }
DI float ex2(float x) { return __builtin_amdgcn_exp2f(x); }
DI float swap32_add(float a, float b) { auto s = __builtin_amdgcn_permlane32_swap(fu(a), fu(b), false, false); return uf(s[0]) + uf(s[1]); }
DI float swap16_add(float a, float b) { auto s = __builtin_amdgcn_permlane16_swap(fu(a), fu(b), false, false); return uf(s[0]) + uf(s[1]); }
DI float swap32_max(float a) { auto s = __builtin_amdgcn_permlane32_swap(fu(a), fu(a), false, false); return mx(uf(s[0]), uf(s[1])); }
DI float swap16_max(float a) { auto s = __builtin_amdgcn_permlane16_swap(fu(a), fu(a), false, false); return mx(uf(s[0]), uf(s[1])); }
DI void bar() {
  __builtin_amdgcn_sched_barrier(0);
  asm volatile("s_waitcnt lgkmcnt(0)\n\ts_barrier" ::: "memory");
  __builtin_amdgcn_sched_barrier(0);
}
DI void pin() { asm volatile("" ::: "memory"); __builtin_amdgcn_sched_barrier(0); }
DI u32 bad4(u32 acc, v4u v, u32 tag) {
  return __builtin_amdgcn_bitop3_b32(__builtin_amdgcn_bitop3_b32(acc, v.y, tag, 0xF6), v.w, tag, 0xF6);
}
DI bool none(u32 bad) { return __builtin_amdgcn_ballot_w64(bad != 0) == 0; }
DI v4u diag(u32 src, const u32 (&sel)[4]) {
  v4u a;
#pragma unroll
  for (int k = 0; k < 4; ++k) a[k] = __builtin_amdgcn_perm(src, src, sel[k]);
  return a;
}

#define ST(i) do { if ((SMASK >> (i)) & 1) asm volatile("s_memrealtime %0\n\ts_waitcnt lgkmcnt(0)" : "=s"(ts[i]) :: "memory"); } while (0)
#define CNT(i, n) do { cnts[i] += (n); } while (0)

DI void spin_flag(const char* p, u32 want = 1) {
  _Pragma("nounroll") for (int spin = 0; spin < SPIN_CAP * 8; ++spin) {
    if ((u32)__builtin_amdgcn_readfirstlane(*(volatile const LAS u32*)p) >= want) break;
    __builtin_amdgcn_s_sleep(1);
  }
  pin();
}
DI u32 arrive(u32 addr, int lane) {
  u32 old = 0;
  if (lane == 0) asm volatile("ds_add_rtn_u32 %0, %1, %2\n\ts_waitcnt lgkmcnt(0)" : "=v"(old) : "v"(addr), "v"(1u) : "memory");
  old = __builtin_amdgcn_readfirstlane(old);
  pin();
  return old;
}
DI void post(u32 addr, int lane) {
  if (lane == 0) asm volatile("ds_add_u32 %0, %1" ::"v"(addr), "v"(1u) : "memory");
  pin();
}
DI void spin_flag_asm(u32 addr, u32 want) {
  _Pragma("nounroll") for (int spin = 0; spin < SPIN_CAP * 8; ++spin) {
    u32 v;
    asm volatile("ds_read_b32 %0, %1\n\ts_waitcnt lgkmcnt(0)" : "=v"(v) : "v"(addr) : "memory");
    if ((u32)__builtin_amdgcn_readfirstlane(v) >= want) break;
    __builtin_amdgcn_s_sleep(1);
  }
  pin();
}
DI float row4_sum(float v) { v += dppf<ROR4>(v); return v + dppf<ROR8>(v); }
DI float quad_sum(float v) { v += dppf<XOR1>(v); return v + dppf<XOR2>(v); }
DI f2 sum8(const char* p) {
  f2 s = *(const f2*)p;
#pragma unroll
  for (int w = 1; w < 8; ++w) s += *(const f2*)(p + w * 1024);
  return s;
}
DI f4 rows16(u32 c01, u32 c23, const v4u (&w)[4], const u32 (&sel0)[4], const u32 (&sel1)[4]) {
  f4 acc = {0.f, 0.f, 0.f, 0.f};
  acc = mfma16(diag(c01, sel0), w[0], acc); acc = mfma16(diag(c01, sel1), w[1], acc);
  acc = mfma16(diag(c23, sel0), w[2], acc); acc = mfma16(diag(c23, sel1), w[3], acc);
  return acc;
}

__global__ void __launch_bounds__(512, 2) fused_decode(Params P) {
  constexpr int L_FLAG = 131072;
  __shared__ __attribute__((aligned(16))) char smem[L_FLAG + 64 + 4096];
  u64 ts[12];
  int cnts[8] = {0, 0, 0, 0, 0, 0, 0, 0};
#pragma unroll
  for (int i = 0; i < 12; ++i) ts[i] = 0;
  ts[0] = __builtin_amdgcn_s_memrealtime();
  const Role R = P.role[blockIdx.x >> 7];
  const u32 t_in = (T_GO | T_A1 | T_Y0 | T_WO | T_2 | T_3 | T_4 | T_5 | T_H3) ? (u32)__builtin_amdgcn_s_memrealtime() : 0u;
  auto pace = [&](u32 t) { while ((u32)__builtin_amdgcn_s_memrealtime() - t_in < t) __builtin_amdgcn_s_sleep(1); pin(); };
  const int tid = threadIdx.x, lane = tid & 63, wave = __builtin_amdgcn_readfirstlane(tid >> 6);
  const int bid = blockIdx.x, x = bid & 7, jj = bid >> 3, j = jj & 15;
  const bool isX = jj < 16;
  const int g = lane >> 4, c16 = lane & 15, bsel = c16 >> 3;
  const int h = 4 * x + (j >> 2), r = j & 3, id = x * 16 + j;
  u32 pfc = 0;
  if (tid < 16) {
    u64 pc;
    asm volatile("s_getpc_b64 %0" : "=s"(pc));
    pfc = __builtin_amdgcn_raw_buffer_load_b32(mk((const void*)(pc & ~255ull), CODE_BYTES), tid * 64, jj * 1024, 0);
  }
  if (tid < 16) *(u32*)(smem + L_FLAG + tid * 4) = 0u;
  const int w4 = wave & 3;
  int xv = bsel * 1152 + r * 288 + w4 * 72 + g * 16, xv2 = xv + 64 - g * 14, wv8 = (r * 144 + w4 * 36 + g * 8) * 8192 + h * 256 + c16 * 16;
  int wv9 = wv8 + (32 - 7 * g) * 8192;
  asm volatile("" : "+v"(xv), "+v"(xv2), "+v"(wv8), "+v"(wv9));
  pin();
  const int kq_voff = (c16 & 1) * 64 + g * 16;
  const int gk = 2 * x + (j >> 3), bx = (j >> 2) & 1;
  const int ro = isX ? 0 : 128 * SLOT;
  const rsrc_t ws = mk(R.ws, W_END);
  u32 tag, sel0[4], sel1[4];
  auto make_sel = [&]() {
#pragma unroll
    for (int k = 0; k < 4; ++k) {
      const bool on = ((c16 & 7) >> 1) == k;
      sel0[k] = on ? ((c16 & 1) ? 0x01000c0cu : 0x0c0c0100u) : 0x0c0c0c0cu;
      sel1[k] = on ? ((c16 & 1) ? 0x03020c0cu : 0x0c0c0302u) : 0x0c0c0c0cu;
    }
  };
  constexpr int L_QV = 1024, L_R1 = L_FLAG + 64;
  auto gather_q = [&](int gs, int f) {
    const int gv = (lane >> 5) * SLOT + (w4 & 1) * 1024 + (lane & 31) * 16;
    v4u a, c;
    _Pragma("nounroll") for (int spin = 0; spin < SPIN_CAP; ++spin) {
      pin();
      a = ld4<AUX_SC1>(ws, gv, gs); c = ld4<AUX_SC1>(ws, gv, gs + 2 * SLOT);
      if (none(bad4(bad4(0u, a, tag), c, tag))) break;
      if (f) CNT(1, 1); else CNT(0, 1);
      __builtin_amdgcn_s_sleep(2);
    }
    const float s0 = uf(a.x) + uf(c.x), s1 = uf(a.z) + uf(c.z);
    const u32 pq = pk(swap32_add(s0, s0), swap32_add(s1, s1));
    if (lane < 32) *(u32*)(smem + L_QV + f * 512 + (w4 >> 1) * 256 + 16 * c16 + 8 * g + 4 * (w4 & 1)) = pq;
    post(L_FLAG + 12 + 4 * f, lane);
  };
  auto late = [](int v) { asm volatile("" : "+s"(v)); return v; };
  v4u kk[16];
  size_t kvoff;
  int xo;
  auto kv_setup = [&]() {
    const int hsh = (late(bid) * 2654435761u) >> 20;
    kvoff = ((size_t)(bx * 16 + late(gk)) * 8192 + 6144 + r * (SPX ? 64 : 512) + (2 * w4 + (((wave >> 2) ^ (hsh >> (2 + w4))) & 1)) * (SPX ? 256 : 64)) * 256;
    xo = ((hsh + wave) & 3) * 4096;
  };
  auto load_k = [&](auto lo, auto hi) {
    u64 base = (u64)R.p1 + kvoff;
    asm volatile("" : "+s"(base));
    const rsrc_t rk = mk((const void*)base, 16384);
    const int k_voff = ((c16 & 2) * 2 + (c16 >> 2)) * 256 + kq_voff;
#pragma unroll
    for (int i = decltype(lo)::value; i < decltype(hi)::value; ++i) { kk[i] = ld4<AUX_NT>(rk, k_voff + (i & 1) * 128 + ((i >> 1) & 1) * 2048, ((i >> 2) * 4096) ^ xo); pin(); }
  };
  using I0 = std::integral_constant<int, 0>; using INK = std::integral_constant<int, NK>; using I16 = std::integral_constant<int, 16>;
  const v4u zero4 = {0u, 0u, 0u, 0u};
  auto prewrite = [&]() {
    if (PREW & 2) {
      if (isX ? (w4 < 2 || (w4 == 2 ? r == 3 : lane < 16)) : w4 == 0)
        st4(zero4, ws, lane * 16, W_SLOT + ro + id * SLOT + (isX ? (w4 < 2 ? w4 * 1024 : w4 * 2048) : 5120));
      pin();
    }
  };
  if (__builtin_expect(wave >= 4, 1)) {
    __builtin_amdgcn_s_setprio(2);
    if (T_Y0 && !isX) pace(T_Y0);
    if ((PREW & 1) && (isX || (PREW & 4)) && (w4 < 2 || (w4 == 3 && isX && (PREW & 8)))) st4(zero4, ws, lane * 16, S_H1 + ro + id * SLOT + w4 * 1024);
    v4u w1[9];
    const rsrc_t rx = mk(R.x, 2304), rw = mk(R.wq, 576 * 8192);
    const v4u xc = ld4<0>(rx, xv, 0);
    const u32 xe = __builtin_amdgcn_raw_buffer_load_b16(rx, xv2, 0, 0);
    pin();
#pragma unroll
    for (int i = 0; i < 8; ++i) { w1[i] = ld4<AUX_NT>(rw, wv8, i * 8192); pin(); }
    w1[8] = ld4<AUX_NT>(rw, wv9, 0);
    const u32 ep = __builtin_amdgcn_raw_buffer_load_b32(ws, 0, W_EPOCH, AUX_SC1);
    ST(8);
    bar();
    ST(9);
    make_sel();
    pin();
    f4 acc = {0.f, 0.f, 0.f, 0.f};
#pragma unroll
    for (int i = 0; i < 4; ++i) { acc = mfma16(diag(xc[i], sel0), w1[2 * i], acc); acc = mfma16(diag(xc[i], sel1), w1[2 * i + 1], acc); }
    acc = mfma16(diag(xe, sel0), w1[8], acc);
    char* rp = smem + L_R1 + lane * 16;
    *(f4*)(rp + w4 * 1024) = acc;
    pin();
    tag = (u32)__builtin_amdgcn_readfirstlane(ep) + 0x9E3779B9u;
    if (tag + 1 < 2) tag += 0x9E3779B9u;
    if (arrive(L_FLAG + 48, lane) == 3) {
      acc = (*(const f4*)rp + *(const f4*)(rp + 1024)) + (*(const f4*)(rp + 2048) + *(const f4*)(rp + 3072));
      st4(v4u{fu(acc[0]), tag, fu(acc[1]), tag}, ws, lane * 16, S_H1 + ro + id * SLOT);
      st4(v4u{fu(acc[2]), tag, fu(acc[3]), tag}, ws, lane * 16 + 1024, S_H1 + ro + id * SLOT);
      *(u32*)(smem + L_FLAG + 28) = tag;
      pin();
      *(volatile LAS u32*)(smem + L_FLAG + 52) = 1;
    }
    if (__builtin_expect(isX, 1)) {
      spin_flag(smem + L_FLAG + 52);
      ST(1);
      __builtin_amdgcn_s_sleep(D_H1);
      gather_q(S_H1 + (h ^ HX) * 4 * SLOT + (w4 >> 1) * 512, 0);
      ST(2);
      if (T_A1) pace(T_A1);
      if (PWL) spin_flag(smem + L_FLAG + 20, 4);
      if (PWX == 0) prewrite();
      gather_q(S_H1 + (128 + (2 * gk + (w4 >> 1)) * 4) * SLOT + bx * 512, 1);
      ST(3);
    } else if (!PWT) {
      prewrite();
    }
  } else {
    bar();
    if (__builtin_expect(isX, 1)) {
      __builtin_amdgcn_s_setprio(3);
      const int h2v = bsel * 512 + (c16 & 2) * 32 + g * 16 + S_H2 + id * SLOT + w4 * 128;
      if ((PREW & 16) && (c16 & 5) == 0) st4(zero4, ws, h2v, 0);
      ST(10);
      v4u w2[8];
      const rsrc_t ru = mk((const char*)R.p0 + (r * 128 + w4 * 32) * 8192 + (h ^ HX) * 256, 31 * 8192 + 256);
      const int voff = (4 * (c16 >> 2) + ((c16 >> 1) & 1)) * 8192 + kq_voff;
#pragma unroll
      for (int i = 0; i < 8; ++i) { w2[i] = ld4<AUX_NT>(ru, voff + (i & 1) * 128, ((i >> 1) & 1) * 16384 + (i >> 2) * 131072); pin(); }
      make_sel();
      spin_flag(smem + L_FLAG + 12, 4);
      tag = __builtin_amdgcn_readfirstlane(*(const u32*)(smem + L_FLAG + 28));
      const v4u q0 = *(const v4u*)(smem + L_QV + bsel * 256 + kq_voff), q1 = *(const v4u*)(smem + L_QV + bsel * 256 + 128 + kq_voff);
      u32 o[4];
#pragma unroll
      for (int be = 0; be < 4; ++be) {
        f4 d = {0.f, 0.f, 0.f, 0.f};
        d = mfma16(w2[2 * be], q0, d); d = mfma16(w2[2 * be + 1], q1, d);
        o[be] = pk(d[0] + dppf<XOR1>(d[1]), d[2] + dppf<XOR1>(d[3]));
      }
      const bool up = c16 & 2;
      if ((c16 & 5) == 0) st4(v4u{up ? o[2] : o[0], tag, up ? o[3] : o[1], tag}, ws, h2v, 0);
      post(L_FLAG + 20, lane);
      ST(3);
      if (T_GO) pace(T_GO);
    } else {
      make_sel();
    }
  }
  __builtin_amdgcn_s_setprio(1);
  pin();
  constexpr int L_RED = 16384, L_REDE = L_RED + 8192;
  v4u wo[4], we[2];
  auto mk_wo = [&](auto role) {
    u64 base = (u64)R.wo + (h * 128 + wave * 16) * 1152;
    asm volatile("" : "+s"(base));
    return mk((const void*)base, 16 * 1152);
  };
  auto load_wo = [&](auto role) {
    const rsrc_t rwo = mk_wo(role);
    const int voff = g * 4608 + c16 * 16;
#pragma unroll
    for (int i = 0; i < 4; ++i) { wo[i] = ld4<AUX_NT>(rwo, voff + i * 1152, r * 256); pin(); }
  };
  auto load_we = [&](auto role) {
    if (r == 3) {
      const rsrc_t rwo = mk_wo(role);
      const int voff = g * 4608 + bsel * 2304 + (c16 & 7) * 16;
#pragma unroll
      for (int i = 0; i < 2; ++i) { we[i] = ld4<AUX_NT>(rwo, voff + i * 1152, 1024); pin(); }
    }
  };
  constexpr int L_R2 = 53248;

  int hdv, h5v, gsv, dv, redv;
  u32 tl0[4], tl1[4];
  auto tail_setup = [&](auto role) {
    constexpr bool isX = decltype(role)::value;
    constexpr int TS = isX ? SLOT : INB;
    const int bd = isX ? W_SLOT + O_HD : W_H3D + (h & 7) * PLANE, b5 = isX ? W_SLOT + O_H5 : W_H3D + ((h + 4) & 7) * PLANE;
    hdv = 64 * ((lane >> 1) & 15) + 32 * (lane >> 5) + 16 * (lane & 1) + bd + id * TS + wave * 1024;
    h5v = (wave < 2 ? lane * 16 + wave * 1024 : (lane >> 5) * 256 + (lane & 15) * 16 + 1024 + wave * 512) + b5 + id * TS;
    gsv = ((c16 >> 2) + (id & (isX ? ~7 : ~3))) * TS + (c16 & 2) * (isX ? 2 * SLOT : 512);
    dv = gsv + g * 32 + (c16 & 1) * 16 + bd + (isX ? (h & 1) * 1024 : 0) + wave * 128;
    gsv += S_HX + (h & 1) * 128;
    redv = wave * 1024 + lane * 16;
    asm volatile("" : "+v"(hdv), "+v"(h5v), "+v"(dv), "+v"(redv));
    if (isX) asm volatile("" : "+v"(gsv));
#pragma unroll
    for (int k = 0; k < 4; ++k) {
      const bool on = (c16 >> 2) == k;
      tl0[k] = on ? ((c16 & 1) ? 0x01000c0cu : 0x0c0c0100u) : 0x0c0c0c0cu;
      tl1[k] = on ? ((c16 & 1) ? 0x03020c0cu : 0x0c0c0302u) : 0x0c0c0c0cu;
      asm volatile("" : "+v"(tl0[k]), "+v"(tl1[k]));
    }
    pin();
  };
  auto tail = [&](auto role) {
  constexpr bool isX = decltype(role)::value;
  constexpr int TS = isX ? SLOT : INB;
  bar();
  if (isX) ST(7);
  if (wave < 2) {
    const f2 sv = sum8(smem + L_R2 + tid * 8);
    st4(v4u{fu(sv.x), tag, fu(sv.y), tag}, ws, hdv, 0);
  }
  pin();
  if (isX ? WSYNC == 3 : WO_LATE == 5) {
    if (wave >= 2) __builtin_amdgcn_s_sleep(D_WO);
    if (T_WO) pace(isX ? T_WO : T_WOY);
    if (D_WOY) __builtin_amdgcn_s_sleep(D_WOY);
    load_wo(role);
  }
  if (isX && r == 3 && wave >= 2) __builtin_amdgcn_s_sleep(D_WO);
  if (isX || !WEY) load_we(role);
  {
    float w = 1.f;
    __builtin_amdgcn_s_sleep(isX ? D_A3 : D_H4);
    v4u d0 = ld4<AUX_SC1>(ws, dv, 0);
    if (isX) {
      v4u st;
      int spin = 0;
      _Pragma("nounroll") for (; spin < SPIN_CAP; ++spin) {
        pin();
        st = ld4<AUX_SC1>(ws, gsv, 0);
        if (none(bad4(0u, st, tag))) break;
        __builtin_amdgcn_s_sleep(3);
      }
      float M = uf(st.x);
      M = mx(M, dppf<ROR4>(M)); M = mx(M, dppf<ROR8>(M));
      w = ex2(uf(st.x) - M);
      w *= __builtin_amdgcn_rcpf(row4_sum(w * uf(st.z)));
      asm volatile("" : "+v"(w));
      if (GATE && spin > 1) __builtin_amdgcn_s_sleep(GATE);
    }
    _Pragma("nounroll") for (int spin = 0; spin < SPIN_CAP; ++spin) {
      if (none(bad4(0u, d0, tag))) break;
      if (isX) CNT(5, 1); else CNT(6, 1);
      if (P_HD && spin) __builtin_amdgcn_s_sleep(P_HD);
      pin();
      d0 = ld4<AUX_SC1>(ws, dv, 0);
    }
    const u32 cm = pk(row4_sum(w * uf(d0.x)), row4_sum(w * uf(d0.z)));
    const u32 c01 = dppu<0xA0>(cm), c23 = dppu<0xF5>(cm);
    *(f4*)(smem + L_RED + redv) = rows16(c01, c23, wo, tl0, tl1);
    if (r == 3) {
      const f4 z = {0.f, 0.f, 0.f, 0.f};
      const f4 lo = mfma16(diag(c01, tl1), we[1], mfma16(diag(c01, tl0), we[0], z)), hi = mfma16(diag(c23, tl1), we[1], mfma16(diag(c23, tl0), we[0], z));
      *(f4*)(smem + L_REDE + redv) = f4{lo[0] + dppf<ROR8>(hi[0]), lo[1] + dppf<ROR8>(hi[1]), lo[2] + dppf<ROR8>(hi[2]), lo[3] + dppf<ROR8>(hi[3])};
    }
  }
  bar();
  if (wave < 4) {
    if (wave < 2 || (r == 3 && (lane & 16) == 0)) {
      const f2 sv = sum8(smem + L_RED + (wave >> 1) * 8192 + (tid & 127) * 8);
      st4(v4u{fu(sv.x), tag + 1, fu(sv.y), tag + 1}, ws, h5v, 0);
    }
  } else if (wave == 4 && id < 72) {
    const int e = id - 64, L = id < 64 ? id : 64 + (e & 1) + 4 * (e >> 1), al = L & 15;
    int rv = (isX ? W_SLOT + O_H5 : W_H3D + ((lane >> 3) ^ 4) * PLANE) + (id < 64 ? (L >> 4) * TS + al * 128 : 3 * TS + 2048 + e * 128) +
             (lane >> 3) * 4 * TS + (lane & 7) * 16;
    int ov = lane < 8 ? 2 * ((lane & 1) * 1152 + 128 * (L >> 4) + 32 * (al & 3) + 8 * (lane >> 1) + 2 * (al >> 2)) : 0x40000000;
    asm volatile("" : "+v"(rv), "+v"(ov));
    const rsrc_t ro_ = mk(R.out, 3456);
    v4u v[4];
    __builtin_amdgcn_s_sleep(D_H5);
    _Pragma("nounroll") for (int spin = 0; spin < SPIN_CAP; ++spin) {
      pin();
      u32 bad = 0;
#pragma unroll
      for (int i = 0; i < 4; ++i) v[i] = ld4<AUX_SC1>(ws, rv, i * 32 * TS);
#pragma unroll
      for (int i = 0; i < 4; ++i) bad = bad4(bad, v[i], tag + 1);
      if (none(bad)) break;
      CNT(7, 1);
      if (P_H5) __builtin_amdgcn_s_sleep(P_H5);
    }
    float a = swap32_add((uf(v[0].x) + uf(v[1].x)) + (uf(v[2].x) + uf(v[3].x)), (uf(v[0].z) + uf(v[1].z)) + (uf(v[2].z) + uf(v[3].z)));
    a += dppf<ROR8>(a);
    a = swap16_add(a, a);
    const auto pr = __builtin_amdgcn_permlane32_swap(fu(a), fu(a), false, false);
    __builtin_amdgcn_raw_buffer_store_b32(pk(uf(pr[0]), uf(pr[1])), ro_, ov, 0, OUT_AUX);
    if (!isX && id == 0 && lane == 0) __builtin_amdgcn_raw_buffer_store_b32(tag, ws, 0, W_EPOCH, AUX_SC1);
  }
  };

  if (isX) {
    constexpr int L_ML = 0;
    v4u vv[16];
    kv_setup();
    if (wave >= 4) tail_setup(std::true_type{});
    auto load_v = [&]() {
      u64 base = (u64)R.p2 + kvoff;
      asm volatile("" : "+s"(base));
      const rsrc_t rv = mk((const void*)base, 16384);
#pragma unroll
      for (int i = 0; i < 16; ++i) { vv[i] = ld4<AUX_NT>(rv, lane * 16 + (i & 3) * 1024, ((i >> 2) * 4096) ^ xo); pin(); }
      post(L_FLAG + 44, lane);
      ST(4);
    };
    if (KFIRST) __builtin_amdgcn_s_setprio(2);
    load_k(I0{}, I16{});
    if (KFIRST) __builtin_amdgcn_s_setprio(1);
    if (KFIRST == 2) { if (wave >= 4) post(L_FLAG + 40, lane); else spin_flag(smem + L_FLAG + 40, 4); }
    if (!KFIRST && wave >= 4) { post(L_FLAG + 40, lane); spin_flag(smem + L_FLAG + 40, 4); }
    if (PWX == 2 && wave < 4) prewrite();
    if (!KFIRST || wave < 4) load_v();
    if (wave < 4) {
      if (PWX == 1) prewrite();
      tail_setup(std::true_type{});
      if (PWX) asm volatile("s_waitcnt vmcnt(0)" ::: "memory");
    }
    asm volatile("" ::"v"(pfc));
    spin_flag(smem + L_FLAG + 16, 4);
    ST(5);
    float sc[16];
    {
      const v4u q0 = *(const v4u*)(smem + L_QV + 512 + bsel * 256 + kq_voff), q1 = *(const v4u*)(smem + L_QV + 512 + bsel * 256 + 128 + kq_voff);
      const bool odd = c16 & 1;
#pragma unroll
      for (int be = 0; be < 8; ++be) {
        f4 d = {0.f, 0.f, 0.f, 0.f};
        d = mfma16(kk[2 * be], q0, d); d = mfma16(kk[2 * be + 1], q1, d);
        const float u0 = odd ? d[1] : d[0], u1 = odd ? d[3] : d[2];
        sc[2 * be] = u0 + dppf<XOR1>(u0); sc[2 * be + 1] = u1 + dppf<XOR1>(u1);
      }
    }
    float m = mx(mx(mx(sc[0], sc[1], sc[2]), mx(sc[3], sc[4], sc[5]), mx(sc[6], sc[7], sc[8])), mx(mx(sc[9], sc[10], sc[11]), mx(sc[12], sc[13], sc[14]), sc[15]));
    m = swap32_max(swap16_max(m)) * SC;
    float l = 0.f;
#pragma unroll
    for (int be = 0; be < 8; ++be) {
      const float p0 = ex2(fmaf(sc[2 * be], SC, -m)), p1 = ex2(fmaf(sc[2 * be + 1], SC, -m));
      l += p0 + p1;
      const u32 pp = pk(p0, p1);
      kk[2 * be] = diag(pp, sel0); kk[2 * be + 1] = diag(pp, sel1);
    }
    l = swap16_add(l, l); l = swap32_add(l, l);
    if ((lane & 0x37) == 0) { *(float*)(smem + L_ML + bsel * 64 + wave * 4) = m; *(float*)(smem + L_ML + bsel * 64 + 32 + wave * 4) = l; }
    post(L_FLAG + 36, lane);
#pragma unroll
    for (int i = 0; i < 16; i += 4) asm volatile("" : "+v"(kk[i]), "+v"(kk[i + 1]), "+v"(kk[i + 2]), "+v"(kk[i + 3]));
    const char* p = smem + L_ML + (lane >> 5) * 64;
    float M, fo;
    auto scale = [&]() {
      spin_flag(smem + L_FLAG + 36, 8);
      M = mx(mx4(*(const f4*)p), mx4(*(const f4*)(p + 16)));
      fo = ex2(*(const float*)(p + wave * 4) - M);
      asm volatile("" : "+v"(fo));
      pin();
    };
    f4 acc = {0.f, 0.f, 0.f, 0.f};
    if (wave < 4) {
#pragma unroll
      for (int i = 0; i < 16; ++i) acc = mfma16(kk[i], vv[i], acc);
      asm volatile("" : "+v"(acc));
      if (!KFIRST) { spin_flag(smem + L_FLAG + 44, 8); load_wo(std::true_type{}); }
      scale();
      if (KFIRST) { spin_flag(smem + L_FLAG + 44, 8); load_wo(std::true_type{}); }
      if (wave == 2) {
        float L = ex2(*(const float*)(p + (lane & 7) * 4) - M) * *(const float*)(p + 32 + (lane & 7) * 4);
        L = quad_sum(L); L += dppf<HMIR>(L);
        if ((lane & 24) == 0) st4(v4u{fu(M), tag, fu(L), tag}, ws, (lane >> 5) * 128 + (lane & 7) * 16, S_HX + id * SLOT);
      }
    } else {
      if (KFIRST) load_v();
      scale();
#pragma unroll
      for (int i = 0; i < 16; ++i) {
        if (i == WOP) { asm volatile("" : "+v"(acc)); pin(); load_wo(std::true_type{}); }
        acc = mfma16(kk[i], vv[i], acc);
      }
    }
    *(f4*)(smem + L_R2 + redv) = acc * fo;
    if (WOP == 16 && wave >= 4) load_wo(std::true_type{});
    ST(6);
    tail(std::true_type{});
  } else {
    constexpr int L_Q = 0, L_P = 32768, L_MX = 40960, L_LS = 41472, L_LAT = 45056, L_ST = 49152, L_CNT = L_FLAG + 32;
    const int b = id >> 6;
    __builtin_amdgcn_s_waitcnt(0x0070);
    v4u fr[32], wv[4];
    const int r32 = lane & 31, hh = lane >> 5;
    const int row_base = r32 * 1024 + 16 * (hh ^ (((r32 & 3) << 2) | ((r32 >> 2) & 3)));
    const rsrc_t ruv = mk((const char*)R.p1 + (r * 128 + 64 * (wave >> 2) + 8 * w4) * 8192 + h * 256, 39 * 8192 + 256);
#define LDUV(i) wv[i] = ld4<AUX_NT>(ruv, g * 16384 + c16 * 16, (((i) >> 1) + 32 * ((i) & 1)) * 8192)
#define QCNT(rq) (L_FLAG + ((rq) < 3 ? 36 + 4 * (rq) : 56))
    f16v s;
    int voff[4];
    if (wave < 4) {
      const rsrc_t rc = SPREAD ? mk((const char*)R.p0 + (SPY ? (id >> 3) * 1048576 + wave * 262144 + (id & 7) * 32768 : id * 131072 + wave * 32768), 32768) : mk((const char*)R.p0 + (size_t)id * 131072 + wave * 8192, 131072 - wave * 8192);
      __builtin_amdgcn_s_waitcnt(0xc07f);
      const int hsh = (late(bid) * 2654435761u) >> 20;
      const int kz = ((hsh >> 3) + wave) & 3;
      const int xi = hsh & 4, lx = lane ^ ((2 * wave + (xi >> 2)) & 3);
      ST(10);
#pragma unroll
      for (int i = 0; i < 32; ++i) {
        const int ro = (i >> 3) * 32768 + ((i & 7) ^ xi) * 1024;
        __builtin_amdgcn_raw_ptr_buffer_load_lds(rc, (LAS void*)(smem + wave * 8192 + ro), 16, 16 * (lx ^ (((i & 3) << 2) | ((i >> 2) & 1))), SPREAD ? ((i >> 3) ^ kz) * 8192 + ((i & 7) ^ xi) * 1024 : ro, 0, AUX_NT);
        pin();
        if (i >= WY - 1 && i < NY) asm volatile("s_waitcnt vmcnt(%0)" ::"i"(WY - 1) : "memory");
        if (i == 23 || i == 31) {
          asm volatile("s_waitcnt vmcnt(16)" ::: "memory");
          if (lane == 0) asm volatile("ds_add_u32 %0, %1" ::"v"(L_FLAG + 4 * ((i >> 3) - 2)), "v"(1u) : "memory");
          pin();
        }
      }
      int qa[8], fa[8];
#pragma unroll
      for (int t = 0; t < 8; ++t) { qa[t] = row_base ^ (32 * t); fa[t] = qa[t] + wave * 32768; asm volatile("" : "+v"(qa[t]), "+v"(fa[t])); }
      asm volatile("s_waitcnt vmcnt(8)" ::: "memory");
      post(L_FLAG + 8, lane);
      auto landed = [&]() {
        asm volatile("s_waitcnt vmcnt(0)" ::: "memory");
        post(L_FLAG + 12, lane);
        ST(1);
      };
      if (wave == 3) landed();
      spin_flag_asm(L_FLAG + 4 * wave, 4);
#pragma unroll
      for (int t = 0; t < 32; ++t)
        asm volatile("ds_read_b128 %0, %1 offset:%2" : "=v"(fr[t]) : "v"(fa[t & 7]), "i"(256 * (t >> 3)) : "memory");
      pin();
      if (wave < 2) post(L_FLAG + 16, lane);
      if (wave != 3) landed();
      __builtin_amdgcn_s_waitcnt(0x0f70);
      asm volatile("" ::"v"(pfc));
      v4u qb[4];
#define QRD(t) (*(const v4u*)(smem + L_Q + qa[(t) & 7] + 256 * ((t) >> 3)))
      spin_flag(smem + L_FLAG + 36, 4);
      ST(2);
      u32 rdy = 0;
#pragma unroll
      for (int t = 0; t < 4; ++t) qb[t] = QRD(t);
#pragma unroll
      for (int i = 0; i < 16; ++i) s[i] = 0.f;
      pin();
#pragma unroll
      for (int t = 0; t < 32; ++t) {
        s = mfma32(fr[t], qb[t & 3], s);
        __builtin_amdgcn_sched_barrier(0);
        if ((t & 7) == 4 && t < 24) {
          if (__builtin_expect((u32)__builtin_amdgcn_readfirstlane(rdy) < 4, 0)) spin_flag(smem + QCNT((t >> 3) + 1), 4);
          pin();
        }
        if ((t & 7) == RDY && t < 24) rdy = *(volatile const LAS u32*)(smem + QCNT((t >> 3) + 1));
        if (t + 4 < 32) qb[t & 3] = QRD(t + 4);
        if (!UVL && t >= 21 && t < 25) LDUV(t - 21);
        __builtin_amdgcn_sched_barrier(0);
      }
      ST(3);
      float m = mx(mx(mx(s[0], s[1], s[2]), mx(s[3], s[4], s[5]), mx(s[6], s[7], s[8])), mx(mx(s[9], s[10], s[11]), mx(s[12], s[13], s[14]), s[15]));
      m = swap32_max(m);
      if (lane < 32) *(float*)(smem + L_MX + lane * 16 + wave * 4) = m;
    } else {
      const int q = c16 >> 2, p = lane & 3;
      const int base = (4 * hh + q) * 1024 + w4 * 256 + 16 * ((2 * (g & 1) + (p >> 1)) ^ (4 * q + hh)) + 8 * (p & 1);
      auto frags = [&](auto blk) {
        constexpr int kb = decltype(blk)::value;
        spin_flag(smem + L_FLAG + 4 * kb, 4);
#pragma unroll
        for (int ta = 0; ta < 4; ++ta)
#pragma unroll
          for (int sh = 0; sh < 2; ++sh) {
            const char* pa = smem + kb * 32768 + sh * 16384;
            const v2u a = __builtin_bit_cast(v2u, __builtin_amdgcn_ds_read_tr16_b64_v4i16((LAS s4v*)(pa + (base ^ (64 * ta)))));
            const v2u c = __builtin_bit_cast(v2u, __builtin_amdgcn_ds_read_tr16_b64_v4i16((LAS s4v*)(pa + (base ^ (64 * ta) ^ 32) + 8192)));
            fr[ta * 8 + 2 * kb + sh] = v4u{a.x, a.y, c.x, c.y};
          }
        pin();
      };
      v4u qg[16];
      int qvo = S_H2 + (32 * w4 + 4 * hh) * SLOT + b * 512 + r32 * 16;
      asm volatile("" : "+v"(qvo));
#define LDQ(rq, i) qg[4 * (rq) + (i)] = ld4<AUX_SC1>(ws, qvo, (8 * (i) + (rq)) * SLOT)
      int qw[4];
#pragma unroll
      for (int i = 0; i < 4; ++i) {
        const int hd = (8 * w4 + 2 * i + hh) ^ HX;
        qw[i] = hd * 1024 + 16 * ((r32 >> 1) ^ (((hd & 3) << 2) | ((hd >> 2) & 3))) + 8 * (lane & 1);
        asm volatile("" : "+v"(qw[i]));
      }
      frags(std::integral_constant<int, 0>{});
      frags(std::integral_constant<int, 1>{});
      post(L_FLAG + 16, lane);
      if (EARLYQ < 2) frags(std::integral_constant<int, 2>{});
      if (!EARLYQ) spin_flag(smem + L_FLAG + 12, 4);
#pragma unroll
      for (int i = 0; i < 4 * QA; ++i) {
        LDQ(i >> 2, i & 3);
        pin();
        if (QSYNC && (i & 3) == 3 && i < 4 * QA - 1) { post(L_FLAG + 20, lane); spin_flag(smem + L_FLAG + 20, i + 1); }
      }
      if (EARLYQ == 2) frags(std::integral_constant<int, 2>{});
      spin_flag(smem + L_FLAG + 16, 6);
#pragma unroll
      for (int rq = 0; rq < 4; ++rq) {
        u32 bad = 0;
#pragma unroll
        for (int i = 0; i < 4; ++i) bad = bad4(bad, qg[4 * rq + i], tag);
        if (__builtin_expect(!none(bad), 0)) {
          _Pragma("nounroll") for (int spin = 0; spin < SPIN_CAP; ++spin) {
            pin();
            CNT(2, 1);
            bad = 0;
#pragma unroll
            for (int i = 0; i < 4; ++i) LDQ(rq, i);
#pragma unroll
            for (int i = 0; i < 4; ++i) bad = bad4(bad, qg[4 * rq + i], tag);
            if (none(bad)) break;
            __builtin_amdgcn_s_sleep(4);
          }
        }
#pragma unroll
        for (int i = 0; i < 4; ++i) *(v2u*)(smem + L_Q + qw[i] + 256 * rq) = v2u{qg[4 * rq + i].x, qg[4 * rq + i].z};
        post(QCNT(rq), lane);
        if (rq + QA < 4) {
#pragma unroll
          for (int i = 0; i < 4; ++i) { LDQ(rq + QA, i); pin(); }
        }
      }
      ST(3);
#pragma unroll
      for (int i = 0; i < 4; ++i) { if (!UVL) LDUV(i); pin(); }
      if (EARLYQ == 3) frags(std::integral_constant<int, 2>{});
      frags(std::integral_constant<int, 3>{});
#pragma unroll
      for (int k = 0; k < 4; ++k) { voff[k] = ((x + k + 4 * hh) & 7) * PLANE + (4 * hh + k) * 4 * INB + r32 * 4; asm volatile("" : "+v"(voff[k])); }
    }
    bar();
    if (wave < 4) {
      const f4 mm = *(const f4*)(smem + L_MX + r32 * 16);
      const float m = mx(mx(mm[0], mm[1]), mx(mm[2], mm[3])) * SC;
      u32 pp[8];
#pragma unroll
      for (int i = 0; i < 8; ++i) {
        s[2 * i] = ex2(fmaf(s[2 * i], SC, -m)); s[2 * i + 1] = ex2(fmaf(s[2 * i + 1], SC, -m));
        pp[i] = pk(s[2 * i], s[2 * i + 1]);
      }
      *(v4u*)(smem + L_P + wave * 2048 + lane * 16) = v4u{pp[0], pp[1], pp[2], pp[3]};
      *(v4u*)(smem + L_P + wave * 2048 + 1024 + lane * 16) = v4u{pp[4], pp[5], pp[6], pp[7]};
    }
    bar();
    pin();
    if (wave < 4) {
      float l = 0.f;
#pragma unroll
      for (int i = 0; i < 8; ++i) l += s[2 * i] + s[2 * i + 1];
      l = swap32_add(l, l);
      if (lane < 32) *(float*)(smem + L_LS + lane * 16 + wave * 4) = l;
      tag = __builtin_amdgcn_readfirstlane(*(const u32*)(smem + L_FLAG + 28));
      if (PWT) { prewrite(); if (!NOB5 || wave == 0) asm volatile("s_waitcnt vmcnt(0)" ::: "memory"); }
      if (NOB5) post(L_FLAG + 24, lane);
    }
    ST(4);
    if (wave >= 4) {
      v4u pf[8];
#pragma unroll
      for (int ks = 0; ks < 8; ++ks) pf[ks] = *(const v4u*)(smem + L_P + ks * 1024 + lane * 16);
      f16v o0, o1, o2, o3;
#pragma unroll
      for (int i = 0; i < 16; ++i) { o0[i] = 0.f; o1[i] = 0.f; o2[i] = 0.f; o3[i] = 0.f; }
      const int soff = W_H3D + w4 * INB + j * 256;
      pin();
#pragma unroll
      for (int ks = 0; ks < 8; ++ks) { o0 = mfma32(pf[ks], fr[ks], o0); o1 = mfma32(pf[ks], fr[8 + ks], o1); }
      __builtin_amdgcn_sched_barrier(0);
#define OST(a, c, i, pi) __builtin_amdgcn_raw_buffer_store_b32(pk(a[i], c[i]), ws, voff[(i) & 3] + (pi) * 128, soff + ((i) >> 2) * 32 * INB, AUX_SC1)
#pragma unroll
      for (int ks = 0; ks < 8; ++ks) {
        o2 = mfma32(pf[ks], fr[16 + ks], o2);
        __builtin_amdgcn_sched_barrier(0);
        if (ks) OST(o0, o1, 2 * ks - 2, 0);
        __builtin_amdgcn_sched_barrier(0);
        o3 = mfma32(pf[ks], fr[24 + ks], o3);
        __builtin_amdgcn_sched_barrier(0);
        if (ks) OST(o0, o1, 2 * ks - 1, 0);
        __builtin_amdgcn_sched_barrier(0);
      }
      OST(o0, o1, 14, 0); OST(o0, o1, 15, 0);
#pragma unroll
      for (int i = 0; i < 16; ++i) OST(o2, o3, i, 1);
    }
    if (!NOB5) bar();
    ST(5);
    if (wave >= 4) {
      if (NOB5) spin_flag(smem + L_FLAG + 24, 4);
      __builtin_amdgcn_s_setprio(3);
      const int hd = (lane >> 3) * 4 + (lane & 3);
      const f4 mm = *(const f4*)(smem + L_MX + hd * 16), ll = *(const f4*)(smem + L_LS + hd * 16);
      v4u sv = {fu(mx(mx(mm[0], mm[1]), mx(mm[2], mm[3])) * SC), tag, fu((ll[0] + ll[1]) + (ll[2] + ll[3])), tag};
      asm volatile("s_waitcnt vmcnt(0)" : "+v"(sv)::"memory");
      if (arrive(L_CNT, lane) == 3) st4(sv, ws, lane * 16, S_HY + (128 + id) * SLOT);
      __builtin_amdgcn_s_setprio(0);
      ST(6);
    }
    pin();
    if (!WO_LATE) load_wo(std::false_type{});
    if (WO_LATE == 4) { spin_flag(smem + L_CNT, 4); if (wave < 4) __builtin_amdgcn_s_sleep(2); load_wo(std::false_type{}); }
    tail_setup(std::false_type{});
    if (UVL) {
      spin_flag(smem + L_CNT, 4);
      if (wave < 4) __builtin_amdgcn_s_sleep(2);
#pragma unroll
      for (int i = 0; i < 4; ++i) { LDUV(i); pin(); }
    }
    if (NOB5) { spin_flag(smem + L_CNT, 4); __builtin_amdgcn_s_sleep(D_H3); if (T_H3) pace(T_H3); }
    else if (wave < 4 && WO_LATE != 4 && !UVL) __builtin_amdgcn_s_sleep(D_H3 + 14); else __builtin_amdgcn_s_sleep(D_H3);
    {
      v4u st;
      _Pragma("nounroll") for (int spin = 0; spin < SPIN_CAP; ++spin) {
        pin();
        st = ld4<AUX_SC1>(ws, (lane >> 2) * SLOT, S_HY + (128 + wave * 16) * SLOT + x * 128 + (h & 3) * 16);
        if (none(bad4(0u, st, tag))) break;
        CNT(3, 1);
        __builtin_amdgcn_s_sleep(P_H3);
      }
      pin();
      ST(7);
      if (D_G3) __builtin_amdgcn_s_sleep(D_G3);
      v4u d[4];
#pragma unroll
      for (int i = 0; i < 4; ++i) d[i] = ld4<AUX_SC1>(ws, g * 1024 + c16 * 16 + i * 256, W_H3D + ((wave + h) & 7) * PLANE + id * INB);
      pin();
      float M = uf(st.x);
      M = mx(M, dppf<ROR4>(M)); M = mx(M, dppf<ROR8>(M)); M = swap32_max(swap16_max(M));
      float wg = ex2(uf(st.x) - M);
      float L = wg * uf(st.z);
      L += dppf<ROR4>(L); L += dppf<ROR8>(L); L = swap16_add(L, L); L = swap32_add(L, L);
      if (lane == 0) *(f2*)(smem + L_ST + wave * 8) = f2{M, L};
      bar();
      const f4 s0 = *(const f4*)(smem + L_ST + (wave >> 2) * 32), s1 = *(const f4*)(smem + L_ST + (wave >> 2) * 32 + 16);
      const float MM = mx(mx(s0[0], s0[2], s1[0]), s1[2]);
      const float LL = fmaf(ex2(s0[0] - MM), s0[1], fmaf(ex2(s0[2] - MM), s0[3], fmaf(ex2(s1[0] - MM), s1[1], ex2(s1[2] - MM) * s1[3])));
      wg *= ex2(M - MM) * __builtin_amdgcn_rcpf(LL);
      const u32 w01 = pk(dppf<BCAST>(wg), dppf<BCAST + 4>(wg)), w23 = pk(dppf<BCAST + 8>(wg), dppf<BCAST + 12>(wg));
      v4u af[4] = {diag(w01, sel0), diag(w01, sel1), diag(w23, sel0), diag(w23, sel1)};
      asm volatile("" : "+v"(af[0]), "+v"(af[1]), "+v"(af[2]), "+v"(af[3]));
      pin();
      if (WO_LATE == 3) load_wo(std::false_type{});
      f4 lat = {0.f, 0.f, 0.f, 0.f};
#pragma unroll
      for (int i = 0; i < 4; ++i) lat = mfma16(af[i], d[i], lat);
      if (lane < 32) *(f4*)(smem + L_LAT + wave * 512 + c16 * 32 + g * 16) = lat;
      pin();
      if (WEY) load_we(std::false_type{});
    }
    bar();
    if (WO_LATE == 2) load_wo(std::false_type{});
    {
      const char* p = smem + L_LAT + bsel * 2048 + wave * 64 + g * 16;
      const f4 c = (*(const f4*)p + *(const f4*)(p + 512)) + (*(const f4*)(p + 1024) + *(const f4*)(p + 1536));
      *(f4*)(smem + L_R2 + redv) = rows16(pk(c[0], c[1]), pk(c[2], c[3]), wv, sel0, sel1);
    }
    tail(std::false_type{});
  }
  ST(11);
  if (lane == 0) {
    u64* sp = (u64*)((char*)R.ws + W_STAMP) + (bid * 8 + wave) * 12;
#pragma unroll
    for (int i = 0; i < 12; ++i) __hip_atomic_store(sp + i, ts[i], __ATOMIC_RELAXED, __HIP_MEMORY_SCOPE_AGENT);
  }
  if (bid == 0 && wave == 7) {
    while (__builtin_amdgcn_s_memrealtime() - ts[0] < 7000) __builtin_amdgcn_s_sleep(8);
    u64* sp = (u64*)((char*)R.ws + W_STAMP);
    u64 mn = ~0ull;
    for (int e = lane; e < 2048; e += 64) { u64 v = __hip_atomic_load(sp + e * 12, __ATOMIC_RELAXED, __HIP_MEMORY_SCOPE_AGENT); mn = v < mn ? v : mn; }
    for (int o = 32; o; o >>= 1) { u64 t = __shfl_xor(mn, o); mn = t < mn ? t : mn; }
    float sum = 0.f, cnt = 0.f, mx = 0.f;
    for (int e = lane; e < 2048; e += 64) {
      u64 v = __hip_atomic_load(sp + e * 12 + REP_IDX, __ATOMIC_RELAXED, __HIP_MEMORY_SCOPE_AGENT);
      const int role = (e >> 6) >= 16;
      if (v && (REP_ROLE == 2 || role == REP_ROLE)) { float d = (float)(v - mn); sum += d; cnt += 1.f; mx = fmaxf(mx, d); }
    }
    for (int o = 32; o; o >>= 1) { sum += __shfl_xor(sum, o); cnt += __shfl_xor(cnt, o); mx = fmaxf(mx, __shfl_xor(mx, o)); }
    float q = REP_STAT == 0 ? 0.f : REP_STAT == 1 ? sum / cnt : mx;
    q = fminf(q, 6000.f);
    const u64 until = mn + 10000 + (u64)(4.f * q);
    while (__builtin_amdgcn_s_memrealtime() < until) __builtin_amdgcn_s_sleep(1);
  }
}

extern "C" void kernel_launch(void* const* d_in, const int* in_sizes, int n_in, void* d_out, int out_size, void* d_ws, size_t ws_size,
                              hipStream_t stream) {
  Params p;
  char* w = (char*)d_ws + (WS_AT_END ? ((ws_size - (18 << 20)) & ~(size_t)0x1fffff) : 0);
  p.role[0] = Role{d_in[5], d_in[7], w, d_in[8], d_in[1], d_in[2], d_in[4], d_out};
  p.role[1] = Role{d_in[0], d_in[3], w, d_in[6], d_in[9], d_in[9], d_in[10], (char*)d_out + 1152};
  fused_decode<<<256, 512, 0, stream>>>(p);
}
```

```cpp
#include <hip/hip_runtime.h>
#include <hip/hip_bf16.h>
#include <stdint.h>
#include <type_traits>

typedef unsigned u32;
typedef unsigned long long u64;
typedef u32 v4u __attribute__((ext_vector_type(4)));
typedef u32 v2u __attribute__((ext_vector_type(2)));
typedef float f2 __attribute__((ext_vector_type(2)));
typedef float f4 __attribute__((ext_vector_type(4)));
typedef float f16v __attribute__((ext_vector_type(16)));
typedef __bf16 bf8 __attribute__((ext_vector_type(8)));
typedef __bf16 bf2 __attribute__((ext_vector_type(2)));
typedef short s4v __attribute__((ext_vector_type(4)));
typedef __amdgpu_buffer_rsrc_t rsrc_t;
#define LAS __attribute__((address_space(3)))
#define DI __device__ __forceinline__
#define NOUNROLL _Pragma("nounroll")
template <int N> using IC = std::integral_constant<int, N>;

constexpr int AUX_NT = 2, AUX_SC1 = 16;
constexpr int W_EPOCH = 0;
constexpr int SLOT = 16896, W_SLOT = 4096;
constexpr int S_HD = W_SLOT;
constexpr int S_H1 = W_SLOT + 2048;
constexpr int S_H5 = S_H1;
constexpr int S_H2 = W_SLOT + 5120;
constexpr int S_HY = W_SLOT + 5120;
constexpr int S_HX = W_SLOT + 6144;
constexpr int INB = 4352, PLANE = 128 * INB, W_H3 = 5 << 20;
constexpr int W_END = W_H3 + 8 * PLANE;
constexpr float SC = 0.08838834764831845f * 1.4426950408889634f;
constexpr int CODE_BYTES = 19904;
constexpr int SPIN_GLOBAL = 1 << 20, SPIN_LDS = 1 << 24;
constexpr int D_H1 = 20, D_H3 = 30, P_H3 = 6, D_HX = 26, D_HY = 16, D_H5 = 14, D_WO = 4, D_WOY = 12;

struct Role {
  const void *x, *wq;
  void* ws;
  const void *p0, *p1, *p2, *wo;
  void* out;
};
struct Params { Role role[2]; };

DI rsrc_t mk(const void* p, u32 bytes) { return __builtin_amdgcn_make_buffer_rsrc((void*)p, 0, bytes, 0x00020000); }
DI rsrc_t mk_here(const void* p, size_t off, u32 bytes) {
  u64 base = (u64)p + off;
  asm volatile("" : "+s"(base));
  return mk((const void*)base, bytes);
}
template <int AUX> DI v4u ld4(rsrc_t r, int voff, int soff) { return __builtin_amdgcn_raw_buffer_load_b128(r, voff, soff, AUX); }
DI void st4(v4u v, rsrc_t r, int voff) { __builtin_amdgcn_raw_buffer_store_b128(v, r, voff, 0, AUX_SC1); }
DI u32 fu(float f) { return __builtin_bit_cast(u32, f); }
DI float uf(u32 u) { return __builtin_bit_cast(float, u); }
DI v4u gran(float a, float b, u32 tag) { return v4u{fu(a), tag, fu(b), tag}; }
DI u32 pk(float lo, float hi) { f2 f = {lo, hi}; return __builtin_bit_cast(u32, __builtin_convertvector(f, bf2)); }
template <int C> DI u32 dppu(u32 x) { return __builtin_amdgcn_mov_dpp(x, C, 0xf, 0xf, true); }
template <int C> DI float dppf(float x) { return uf(dppu<C>(fu(x))); }
constexpr int XOR1 = 0xB1, XOR2 = 0x4E, EVEN = 0xA0, ODD = 0xF5, HMIR = 0x141, ROR4 = 0x124, ROR8 = 0x128, BCAST = 0x150;
DI f4 mfma16(v4u a, v4u b, f4 c) { return __builtin_amdgcn_mfma_f32_16x16x32_bf16(__builtin_bit_cast(bf8, a), __builtin_bit_cast(bf8, b), c, 0, 0, 0); }
DI f16v mfma32(v4u a, v4u b, f16v c) { return __builtin_amdgcn_mfma_f32_32x32x16_bf16(__builtin_bit_cast(bf8, a), __builtin_bit_cast(bf8, b), c, 0, 0, 0); }
DI float mx(float a, float b) { return __builtin_elementwise_maximum(a, b); }
DI float mx(float a, float b, float c) { return mx(mx(a, b), c); }
DI float mx4(f4 v) { return mx(mx(v[0], v[1], v[2]), v[3]); }
template <class T> DI float mx16(const T& s) {
  return mx(mx(mx(s[0], s[1], s[2]), mx(s[3], s[4], s[5]), mx(s[6], s[7], s[8])), mx(mx(s[9], s[10], s[11]), mx(s[12], s[13], s[14]), s[15]));
}
DI float ex2(float x) { return __builtin_amdgcn_exp2f(x); }
DI float swap32_add(float a, float b) { auto s = __builtin_amdgcn_permlane32_swap(fu(a), fu(b), false, false); return uf(s[0]) + uf(s[1]); }
DI float swap16_add(float a, float b) { auto s = __builtin_amdgcn_permlane16_swap(fu(a), fu(b), false, false); return uf(s[0]) + uf(s[1]); }
DI float swap32_max(float a) { auto s = __builtin_amdgcn_permlane32_swap(fu(a), fu(a), false, false); return mx(uf(s[0]), uf(s[1])); }
DI float swap16_max(float a) { auto s = __builtin_amdgcn_permlane16_swap(fu(a), fu(a), false, false); return mx(uf(s[0]), uf(s[1])); }
DI float row4_sum(float v) { v += dppf<ROR4>(v); return v + dppf<ROR8>(v); }
DI float row4_max(float v) { v = mx(v, dppf<ROR4>(v)); return mx(v, dppf<ROR8>(v)); }
DI void pin() { asm volatile("" ::: "memory"); __builtin_amdgcn_sched_barrier(0); }
DI void bar() {
  __builtin_amdgcn_sched_barrier(0);
  asm volatile("s_waitcnt lgkmcnt(0)\n\ts_barrier" ::: "memory");
  __builtin_amdgcn_sched_barrier(0);
}
template <int N> DI void wait_vm() { asm volatile("s_waitcnt vmcnt(%0)" ::"i"(N) : "memory"); }
DI u32 bad4(u32 acc, v4u v, u32 tag) {
  return __builtin_amdgcn_bitop3_b32(__builtin_amdgcn_bitop3_b32(acc, v.y, tag, 0xF6), v.w, tag, 0xF6);
}
DI bool none(u32 bad) { return __builtin_amdgcn_ballot_w64(bad != 0) == 0; }

DI void post(u32 addr) {
  __builtin_amdgcn_sched_barrier(0);
  asm volatile("s_mov_b64 exec, 1\n\tds_add_u32 %0, %1\n\ts_mov_b64 exec, -1" ::"v"(addr), "v"(1u) : "memory");
  __builtin_amdgcn_sched_barrier(0);
}
DI u32 arrive(u32 addr) {
  u32 v, old;
  __builtin_amdgcn_sched_barrier(0);
  asm volatile("s_mov_b64 exec, 1\n\tds_add_rtn_u32 %0, %2, %3\n\ts_mov_b64 exec, -1\n\ts_waitcnt lgkmcnt(0)\n\tv_readfirstlane_b32 %1, %0"
               : "=&v"(v), "=s"(old) : "v"(addr), "v"(1u) : "memory");
  __builtin_amdgcn_sched_barrier(0);
  return old;
}
template <int WANT> DI void await(u32 addr) {
  u32 v, t, n;
  __builtin_amdgcn_sched_barrier(0);
  asm volatile(
      "s_mov_b32 %2, %5\n"
      "1:\n\tds_read_b32 %0, %3\n\ts_waitcnt lgkmcnt(0)\n\tv_readfirstlane_b32 %1, %0\n\ts_cmp_ge_u32 %1, %4\n\ts_cbranch_scc1 2f\n\t"
      "s_sleep 1\n\ts_sub_u32 %2, %2, 1\n\ts_cbranch_scc0 1b\n"
      "2:"
      : "=&v"(v), "=&s"(t), "=&s"(n) : "v"(addr), "i"(WANT), "i"(SPIN_LDS) : "memory", "scc");
  __builtin_amdgcn_sched_barrier(0);
}

DI v4u diag(u32 pair, const u32 (&sel)[4]) {
  v4u a;
#pragma unroll
  for (int k = 0; k < 4; ++k) a[k] = __builtin_amdgcn_perm(pair, pair, sel[k]);
  return a;
}
DI void make_sel(int dw, bool odd, u32 (&lo)[4], u32 (&hi)[4]) {
  const u32 l = odd ? 0x01000c0cu : 0x0c0c0100u, u = odd ? 0x03020c0cu : 0x0c0c0302u;
#pragma unroll
  for (int k = 0; k < 4; ++k) { lo[k] = dw == k ? l : 0x0c0c0c0cu; hi[k] = dw == k ? u : 0x0c0c0c0cu; }
}
DI f4 rows16(u32 c01, u32 c23, const v4u (&w)[4], const u32 (&lo)[4], const u32 (&hi)[4]) {
  f4 acc = {0.f, 0.f, 0.f, 0.f};
  acc = mfma16(diag(c01, lo), w[0], acc); acc = mfma16(diag(c01, hi), w[1], acc);
  acc = mfma16(diag(c23, lo), w[2], acc); acc = mfma16(diag(c23, hi), w[3], acc);
  return acc;
}
DI f2 sum8(const char* p) {
  f2 s[8];
#pragma unroll
  for (int w = 0; w < 8; ++w) s[w] = *(const f2*)(p + w * 1024);
  __builtin_amdgcn_sched_barrier(0);
  return ((s[0] + s[1]) + (s[2] + s[3])) + ((s[4] + s[5]) + (s[6] + s[7]));
}

__global__ void __launch_bounds__(512, 2) fused_decode(Params P) {
  constexpr int L_C = 131072, L_R1 = L_C + 64;
  constexpr int C_TAG = L_C + 28, C_PROJ = L_C + 48, C_PUB = L_C + 52;
  constexpr int C_QB = L_C + 12, C_QA = L_C + 16, C_ABS = L_C + 20, C_SCORE = L_C + 36, C_VAL = L_C + 44;
  constexpr int C_BLK = L_C, C_FREE = L_C + 16, C_SUMS = L_C + 24, C_DRAIN = L_C + 32;
#define C_QUART(q) (L_C + ((q) < 3 ? 36 + 4 * (q) : 56))
  constexpr int L_QV = 1024, L_RED = 16384, L_REDE = L_RED + 8192, L_R2 = 53248;
  __shared__ __attribute__((aligned(16))) char smem[L_R1 + 4096];

  const Role R = P.role[blockIdx.x >> 7];
  const int tid = threadIdx.x, lane = tid & 63, wave = __builtin_amdgcn_readfirstlane(tid >> 6);
  const int bid = blockIdx.x, x = bid & 7, jj = bid >> 3, j = jj & 15;
  const bool isX = jj < 16;
  const int g = lane >> 4, c16 = lane & 15, bsel = c16 >> 3;
  const int h = 4 * x + (j >> 2), r = j & 3, id = x * 16 + j;
  u32 pfc = 0;
  if (tid < 16) {
    u64 pc;
    asm volatile("s_getpc_b64 %0" : "=s"(pc));
    pfc = __builtin_amdgcn_raw_buffer_load_b32(mk((const void*)(pc & ~255ull), CODE_BYTES), tid * 64, jj * 1024, 0);
  }
  if (tid < 16) *(u32*)(smem + L_C + tid * 4) = 0u;
  const int w4 = wave & 3;
  int xv = bsel * 1152 + r * 288 + w4 * 72 + g * 16, xv2 = xv + 64 - g * 14;
  int wv8 = (r * 144 + w4 * 36 + g * 8) * 8192 + h * 256 + c16 * 16, wv9 = wv8 + (32 - 7 * g) * 8192;
  asm volatile("" : "+v"(xv), "+v"(xv2), "+v"(wv8), "+v"(wv9));
  pin();
  auto late = [](int v) { asm volatile("" : "+s"(v)); return v; };
  const int kq_voff = (c16 & 1) * 64 + g * 16;
  const int gk = 2 * x + (j >> 3), bx = (j >> 2) & 1;
  const rsrc_t ws = mk(R.ws, W_END);
  const v4u zero4 = {0u, 0u, 0u, 0u};
  u32 tag, sel0[4], sel1[4];

  if (__builtin_expect(wave >= 4, 1)) {
    __builtin_amdgcn_s_setprio(2);
    v4u w1[9];
    const rsrc_t rx = mk(R.x, 2304), rw = mk(R.wq, 576 * 8192);
    const v4u xc = ld4<0>(rx, xv, 0);
    const u32 xe = __builtin_amdgcn_raw_buffer_load_b16(rx, xv2, 0, 0);
    pin();
#pragma unroll
    for (int i = 0; i < 8; ++i) { w1[i] = ld4<AUX_NT>(rw, wv8, i * 8192); pin(); }
    w1[8] = ld4<AUX_NT>(rw, wv9, 0);
    const u32 ep = __builtin_amdgcn_raw_buffer_load_b32(ws, 0, W_EPOCH, AUX_SC1);
    bar();
    make_sel((c16 & 7) >> 1, c16 & 1, sel0, sel1);
    pin();
    f4 acc = {0.f, 0.f, 0.f, 0.f};
#pragma unroll
    for (int i = 0; i < 4; ++i) { acc = mfma16(diag(xc[i], sel0), w1[2 * i], acc); acc = mfma16(diag(xc[i], sel1), w1[2 * i + 1], acc); }
    acc = mfma16(diag(xe, sel0), w1[8], acc);
    char* rp = smem + L_R1 + lane * 16;
    *(f4*)(rp + w4 * 1024) = acc;
    pin();
    tag = (u32)__builtin_amdgcn_readfirstlane(ep) + 0x9E3779B9u;
    if (tag + 1 < 2) tag += 0x9E3779B9u;
    if (arrive(C_PROJ) == 3) {
      acc = (*(const f4*)rp + *(const f4*)(rp + 1024)) + (*(const f4*)(rp + 2048) + *(const f4*)(rp + 3072));
      const int so = S_H1 + ((isX ? 0 : 128) + id) * SLOT + lane * 16;
      st4(gran(acc[0], acc[1], tag), ws, so);
      st4(gran(acc[2], acc[3], tag), ws, so + 1024);
      *(u32*)(smem + C_TAG) = tag;
      post(C_PUB);
    }
    if (__builtin_expect(isX, 1)) {
      auto gather_q = [&](int gs, int f) {
        const int gv = (lane >> 5) * SLOT + (w4 & 1) * 1024 + (lane & 31) * 16;
        v4u a, c;
        NOUNROLL for (int spin = 0; spin < SPIN_GLOBAL; ++spin) {
          pin();
          a = ld4<AUX_SC1>(ws, gv, gs); c = ld4<AUX_SC1>(ws, gv, gs + 2 * SLOT);
          if (none(bad4(bad4(0u, a, tag), c, tag))) break;
          __builtin_amdgcn_s_sleep(2);
        }
        const float s0 = uf(a.x) + uf(c.x), s1 = uf(a.z) + uf(c.z);
        const u32 pq = pk(swap32_add(s0, s0), swap32_add(s1, s1));
        if (lane < 32) *(u32*)(smem + L_QV + f * 512 + (w4 >> 1) * 256 + 16 * c16 + 8 * g + 4 * (w4 & 1)) = pq;
        post(f ? C_QA : C_QB);
      };
      await<1>(C_PUB);
      __builtin_amdgcn_s_sleep(D_H1);
      gather_q(S_H1 + h * 4 * SLOT + (w4 >> 1) * 512, 0);
      await<4>(C_ABS);
      gather_q(S_H1 + (128 + (2 * gk + (w4 >> 1)) * 4) * SLOT + bx * 512, 1);
    }
  } else {
    bar();
    if (__builtin_expect(isX, 1)) {
      __builtin_amdgcn_s_setprio(3);
      v4u w2[8];
      const rsrc_t ru = mk((const char*)R.p0 + (r * 128 + w4 * 32) * 8192 + h * 256, 31 * 8192 + 256);
      const int voff = (4 * (c16 >> 2) + ((c16 >> 1) & 1)) * 8192 + kq_voff;
#pragma unroll
      for (int i = 0; i < 8; ++i) { w2[i] = ld4<AUX_NT>(ru, voff + (i & 1) * 128, ((i >> 1) & 1) * 16384 + (i >> 2) * 131072); pin(); }
      make_sel((c16 & 7) >> 1, c16 & 1, sel0, sel1);
      int h2v = (c16 & 5) ? 0x40000000 : bsel * 512 + (c16 & 2) * 32 + g * 16 + S_H2 + id * SLOT + w4 * 128, qv = bsel * 256 + kq_voff;
      asm volatile("" : "+v"(h2v), "+v"(qv));
#pragma unroll
      for (int k = 0; k < 4; ++k) asm volatile("" : "+v"(sel0[k]), "+v"(sel1[k]));
      await<1>(C_PUB);
      tag = __builtin_amdgcn_readfirstlane(*(const u32*)(smem + C_TAG));
      asm volatile("" : "+s"(tag));
      await<4>(C_QB);
      const v4u q0 = *(const v4u*)(smem + L_QV + qv), q1 = *(const v4u*)(smem + L_QV + 128 + qv);
      u32 o[4];
#pragma unroll
      for (int be = 0; be < 4; ++be) {
        f4 d = {0.f, 0.f, 0.f, 0.f};
        d = mfma16(w2[2 * be], q0, d); d = mfma16(w2[2 * be + 1], q1, d);
        o[be] = pk(d[0] + dppf<XOR1>(d[1]), d[2] + dppf<XOR1>(d[3]));
      }
      const bool up = c16 & 2;
      st4(v4u{up ? o[2] : o[0], tag, up ? o[3] : o[1], tag}, ws, h2v);
      post(C_ABS);
    } else {
      make_sel((c16 & 7) >> 1, c16 & 1, sel0, sel1);
    }
  }
  __builtin_amdgcn_s_setprio(1);
  pin();

  v4u wo[4], we[2];
  auto load_wo = [&]() {
    const rsrc_t rwo = mk_here(R.wo, (h * 128 + wave * 16) * 1152, 16 * 1152);
    const int voff = g * 4608 + c16 * 16;
#pragma unroll
    for (int i = 0; i < 4; ++i) { wo[i] = ld4<AUX_NT>(rwo, voff + i * 1152, r * 256); pin(); }
  };
  auto load_we = [&]() {
    const rsrc_t rwo = mk_here(R.wo, (h * 128 + wave * 16) * 1152, 16 * 1152);
    const int voff = g * 4608 + bsel * 2304 + (c16 & 7) * 16;
#pragma unroll
    for (int i = 0; i < 2; ++i) { we[i] = ld4<AUX_NT>(rwo, voff + i * 1152, 1024); pin(); }
  };
  int hdv, h5v, gsv, dv, redv, sumv;
  u32 tl0[4], tl1[4];
  auto tail_setup = [&](auto role) {
    constexpr bool X = decltype(role)::value;
    constexpr int TS = X ? SLOT : INB;
    const int bd = X ? S_HD : W_H3 + (h & 7) * PLANE, b5 = X ? S_H5 : W_H3 + ((h + 4) & 7) * PLANE;
    hdv = 64 * ((lane >> 1) & 15) + 32 * (lane >> 5) + 16 * (lane & 1) + bd + id * TS + wave * 1024;
    h5v = (wave < 2 ? lane * 16 + wave * 1024 : (lane & 16) * 0x4000000 + (lane >> 5) * 256 + c16 * 16 + 1024 + wave * 512) + b5 + id * TS;
    sumv = (wave >> 1) * 8192 + (tid & 127) * 8;
    gsv = ((c16 >> 2) + (id & (X ? ~7 : ~3))) * TS + (c16 & 2) * (X ? 2 * SLOT : 512);
    dv = gsv + g * 32 + (c16 & 1) * 16 + bd + (X ? (h & 1) * 1024 : 0) + wave * 128;
    gsv += S_HX + (h & 1) * 128;
    redv = wave * 1024 + lane * 16;
    asm volatile("" : "+v"(hdv), "+v"(h5v), "+v"(dv), "+v"(redv), "+v"(sumv));
    if (X) asm volatile("" : "+v"(gsv));
    make_sel(c16 >> 2, c16 & 1, tl0, tl1);
#pragma unroll
    for (int k = 0; k < 4; ++k) asm volatile("" : "+v"(tl0[k]), "+v"(tl1[k]));
    pin();
  };
  auto tail = [&](auto role) {
    constexpr bool X = decltype(role)::value;
    constexpr int TS = X ? SLOT : INB;
    bar();
    if (wave < 2) {
      const f2 sv = sum8(smem + L_R2 + sumv);
      st4(gran(sv.x, sv.y, tag), ws, hdv);
    }
    pin();
    if (!X || r == 3) {
      if (wave >= 2) __builtin_amdgcn_s_sleep(D_WO);
      if (X) load_we();
      else { __builtin_amdgcn_s_sleep(D_WOY); load_wo(); }
    }
    __builtin_amdgcn_s_sleep(X ? D_HX : D_HY);
    v4u d0 = ld4<AUX_SC1>(ws, dv, 0);
    float w = 1.f;
    if (X) {
      v4u st;
      NOUNROLL for (int spin = 0; spin < SPIN_GLOBAL; ++spin) {
        pin();
        st = ld4<AUX_SC1>(ws, gsv, 0);
        if (none(bad4(0u, st, tag))) break;
        __builtin_amdgcn_s_sleep(3);
      }
      w = ex2(uf(st.x) - row4_max(uf(st.x)));
      w *= __builtin_amdgcn_rcpf(row4_sum(w * uf(st.z)));
      asm volatile("" : "+v"(w));
    }
    NOUNROLL for (int spin = 0; spin < SPIN_GLOBAL; ++spin) {
      if (none(bad4(0u, d0, tag))) break;
      if (spin) __builtin_amdgcn_s_sleep(2);
      pin();
      d0 = ld4<AUX_SC1>(ws, dv, 0);
    }
    const u32 cm = pk(row4_sum(w * uf(d0.x)), row4_sum(w * uf(d0.z)));
    const u32 c01 = dppu<EVEN>(cm), c23 = dppu<ODD>(cm);
    *(f4*)(smem + L_RED + redv) = rows16(c01, c23, wo, tl0, tl1);
    if (r == 3) {
      const f4 z = {0.f, 0.f, 0.f, 0.f};
      const f4 lo = mfma16(diag(c01, tl1), we[1], mfma16(diag(c01, tl0), we[0], z));
      const f4 hi = mfma16(diag(c23, tl1), we[1], mfma16(diag(c23, tl0), we[0], z));
      *(f4*)(smem + L_REDE + redv) = f4{lo[0] + dppf<ROR8>(hi[0]), lo[1] + dppf<ROR8>(hi[1]), lo[2] + dppf<ROR8>(hi[2]), lo[3] + dppf<ROR8>(hi[3])};
    }
    bar();
    if (wave < (r == 3 ? 4 : 2)) {
      const f2 sv = sum8(smem + L_RED + sumv);
      st4(gran(sv.x, sv.y, tag + 1), ws, h5v);
    }
    pin();
    if (wave == 4 && id < 72) {
      const int e = id - 64, L = id < 64 ? id : 64 + (e & 1) + 4 * (e >> 1), al = L & 15;
      int rv = (X ? S_H5 : W_H3 + ((lane >> 3) ^ 4) * PLANE) + (id < 64 ? (L >> 4) * TS + al * 128 : 3 * TS + 2048 + e * 128) +
               (lane >> 3) * 4 * TS + (lane & 7) * 16;
      int ov = lane < 8 ? 2 * ((lane & 1) * 1152 + 128 * (L >> 4) + 32 * (al & 3) + 8 * (lane >> 1) + 2 * (al >> 2)) : 0x40000000;
      asm volatile("" : "+v"(rv), "+v"(ov));
      v4u v[4];
      __builtin_amdgcn_s_sleep(D_H5);
      NOUNROLL for (int spin = 0; spin < SPIN_GLOBAL; ++spin) {
        pin();
        u32 bad = 0;
#pragma unroll
        for (int i = 0; i < 4; ++i) v[i] = ld4<AUX_SC1>(ws, rv, i * 32 * TS);
#pragma unroll
        for (int i = 0; i < 4; ++i) bad = bad4(bad, v[i], tag + 1);
        if (none(bad)) break;
      }
      float a = swap32_add((uf(v[0].x) + uf(v[1].x)) + (uf(v[2].x) + uf(v[3].x)), (uf(v[0].z) + uf(v[1].z)) + (uf(v[2].z) + uf(v[3].z)));
      a += dppf<ROR8>(a);
      a = swap16_add(a, a);
      const auto pr = __builtin_amdgcn_permlane32_swap(fu(a), fu(a), false, false);
      __builtin_amdgcn_raw_buffer_store_b32(pk(uf(pr[0]), uf(pr[1])), mk(R.out, 3456), ov, 0, 0);
      if (!X && id == 0 && lane == 0) __builtin_amdgcn_raw_buffer_store_b32(tag, ws, 0, W_EPOCH, AUX_SC1);
    }
  };

  if (isX) {
    constexpr int L_ML = 0;
    v4u kk[16], vv[16];
    const int hsh = (late(bid) * 2654435761u) >> 20;
    const size_t kvoff = ((size_t)(bx * 16 + late(gk)) * 8192 + 6144 + r * 64 + (2 * w4 + (((wave >> 2) ^ (hsh >> (2 + w4))) & 1)) * 256) * 256;
    const int xo = ((hsh + wave) & 3) * 4096;
    auto load_v = [&]() {
      const rsrc_t rv = mk_here(R.p2, kvoff, 16384);
#pragma unroll
      for (int i = 0; i < 16; ++i) { vv[i] = ld4<AUX_NT>(rv, lane * 16 + (i & 3) * 1024, ((i >> 2) * 4096) ^ xo); pin(); }
      post(C_VAL);
    };
    __builtin_amdgcn_s_setprio(2);
    {
      const rsrc_t rk = mk_here(R.p1, kvoff, 16384);
      const int voff = ((c16 & 2) * 2 + (c16 >> 2)) * 256 + kq_voff;
#pragma unroll
      for (int i = 0; i < 16; ++i) { kk[i] = ld4<AUX_NT>(rk, voff + (i & 1) * 128 + ((i >> 1) & 1) * 2048, ((i >> 2) * 4096) ^ xo); pin(); }
    }
    __builtin_amdgcn_s_setprio(1);
    tail_setup(std::true_type{});
    __builtin_amdgcn_s_waitcnt(0x0f70);
    pin();
    if (wave < 2 || (wave == 2 ? r == 3 : wave == 3 && lane < 16)) st4(zero4, ws, lane * 16 + W_SLOT + id * SLOT + (w4 < 2 ? w4 * 1024 : w4 * 2048));
    pin();
    if (wave < 5) load_v();
    pin();
    asm volatile("" ::"v"(pfc));
    await<4>(C_QA);
    float sc[16];
    {
      const v4u q0 = *(const v4u*)(smem + L_QV + 512 + bsel * 256 + kq_voff), q1 = *(const v4u*)(smem + L_QV + 512 + bsel * 256 + 128 + kq_voff);
      const bool odd = c16 & 1;
#pragma unroll
      for (int be = 0; be < 8; ++be) {
        f4 d = {0.f, 0.f, 0.f, 0.f};
        d = mfma16(kk[2 * be], q0, d); d = mfma16(kk[2 * be + 1], q1, d);
        const float u0 = odd ? d[1] : d[0], u1 = odd ? d[3] : d[2];
        sc[2 * be] = u0 + dppf<XOR1>(u0); sc[2 * be + 1] = u1 + dppf<XOR1>(u1);
      }
    }
    const float m = swap32_max(swap16_max(mx16(sc))) * SC;
    float l = 0.f;
#pragma unroll
    for (int be = 0; be < 8; ++be) {
      const float p0 = ex2(fmaf(sc[2 * be], SC, -m)), p1 = ex2(fmaf(sc[2 * be + 1], SC, -m));
      l += p0 + p1;
      const u32 pp = pk(p0, p1);
      kk[2 * be] = diag(pp, sel0); kk[2 * be + 1] = diag(pp, sel1);
    }
    l = swap16_add(l, l); l = swap32_add(l, l);
    if ((lane & 0x37) == 0) { *(float*)(smem + L_ML + bsel * 64 + wave * 4) = m; *(float*)(smem + L_ML + bsel * 64 + 32 + wave * 4) = l; }
    post(C_SCORE);
#pragma unroll
    for (int i = 0; i < 16; i += 4) asm volatile("" : "+v"(kk[i]), "+v"(kk[i + 1]), "+v"(kk[i + 2]), "+v"(kk[i + 3]));
    const char* p = smem + L_ML + (lane >> 5) * 64;
    float M, fo;
    auto scale = [&]() {
      await<8>(C_SCORE);
      M = mx(mx4(*(const f4*)p), mx4(*(const f4*)(p + 16)));
      fo = ex2(*(const float*)(p + wave * 4) - M);
      asm volatile("" : "+v"(fo));
      pin();
    };
    if (wave >= 5) load_v();
    pin();
    f4 acc = {0.f, 0.f, 0.f, 0.f};
    if (wave < 4) {
#pragma unroll
      for (int i = 0; i < 16; ++i) acc = mfma16(kk[i], vv[i], acc);
      asm volatile("" : "+v"(acc));
      wait_vm<0>();
      post(C_VAL);
      scale();
      await<12>(C_VAL);
      load_wo();
      if (wave == 2) {
        float L = ex2(*(const float*)(p + (lane & 7) * 4) - M) * *(const float*)(p + 32 + (lane & 7) * 4);
        L += dppf<XOR1>(L); L += dppf<XOR2>(L); L += dppf<HMIR>(L);
        if ((lane & 24) == 0) st4(gran(M, L, tag), ws, (lane >> 5) * 128 + (lane & 7) * 16 + S_HX + id * SLOT);
      }
    } else {
      scale();
#pragma unroll
      for (int i = 0; i < 16; ++i) {
        if (i == 12) { asm volatile("" : "+v"(acc)); pin(); load_wo(); }
        acc = mfma16(kk[i], vv[i], acc);
      }
    }
    *(f4*)(smem + L_R2 + redv) = acc * fo;
    tail(std::true_type{});
  } else {
    constexpr int L_Q = 0, L_P = 32768, L_MX = 40960, L_LS = 41472, L_LAT = 45056, L_ST = 49152;
    const int b = id >> 6;
    __builtin_amdgcn_s_waitcnt(0x0070);
    v4u fr[32], wv[4];
    const int r32 = lane & 31, hh = lane >> 5;
    const rsrc_t ruv = mk((const char*)R.p1 + (r * 128 + 64 * (wave >> 2) + 8 * w4) * 8192 + h * 256, 39 * 8192 + 256);
#define LDUV(i) wv[i] = ld4<AUX_NT>(ruv, g * 16384 + c16 * 16, (((i) >> 1) + 32 * ((i) & 1)) * 8192)
    f16v s;
    int voff[4];
    if (wave < 4) {
      const rsrc_t rc = mk((const char*)R.p0 + (id >> 3) * 1048576 + wave * 262144 + (id & 7) * 32768, 32768);
      __builtin_amdgcn_s_waitcnt(0xc07f);
      const int hsh = (late(bid) * 2654435761u) >> 20;
      const int kz = ((hsh >> 3) + wave) & 3, xi = hsh & 4, lx = lane ^ ((2 * wave + (xi >> 2)) & 3);
#pragma unroll
      for (int i = 0; i < 32; ++i) {
        const int row = ((i & 7) ^ xi) * 1024;
        __builtin_amdgcn_raw_ptr_buffer_load_lds(rc, (LAS void*)(smem + (i >> 3) * 32768 + wave * 8192 + row), 16,
                                                 16 * (lx ^ (((i & 3) << 2) | ((i >> 2) & 1))), ((i >> 3) ^ kz) * 8192 + row, 0, AUX_NT);
        pin();
        if (i == 23 || i == 31) { wait_vm<16>(); post(C_BLK + 4 * ((i >> 3) - 2)); }
      }
      int qa[8], fa[8];
#pragma unroll
      for (int t = 0; t < 8; ++t) {
        qa[t] = (r32 * 1024 + 16 * (hh ^ (((r32 & 3) << 2) | ((r32 >> 2) & 3)))) ^ (32 * t);
        fa[t] = qa[t] + wave * 32768;
        asm volatile("" : "+v"(qa[t]), "+v"(fa[t]));
      }
      wait_vm<8>();
      post(C_BLK + 8);
      if (wave == 3) { wait_vm<0>(); post(C_BLK + 12); }
      await<4>(C_BLK + 4 * wave);
#pragma unroll
      for (int t = 0; t < 32; ++t) asm volatile("ds_read_b128 %0, %1 offset:%2" : "=v"(fr[t]) : "v"(fa[t & 7]), "i"(256 * (t >> 3)) : "memory");
      pin();
      if (wave < 2) post(C_FREE);
      if (wave != 3) { wait_vm<0>(); post(C_BLK + 12); }
      __builtin_amdgcn_s_waitcnt(0x0f70);
      asm volatile("" ::"v"(pfc));
      v4u qb[4];
#define QRD(t) (*(const v4u*)(smem + L_Q + qa[(t) & 7] + 256 * ((t) >> 3)))
      await<4>(C_QUART(0));
      u32 rdy = 0;
#pragma unroll
      for (int t = 0; t < 4; ++t) qb[t] = QRD(t);
#pragma unroll
      for (int i = 0; i < 16; ++i) s[i] = 0.f;
      pin();
#pragma unroll
      for (int t = 0; t < 32; ++t) {
        s = mfma32(fr[t], qb[t & 3], s);
        __builtin_amdgcn_sched_barrier(0);
        if ((t & 7) == 4 && t < 24) {
          if (__builtin_expect((u32)__builtin_amdgcn_readfirstlane(rdy) < 4, 0)) await<4>(C_QUART((t >> 3) + 1));
          pin();
        }
        if ((t & 7) == 2 && t < 24) rdy = *(volatile const LAS u32*)(smem + C_QUART((t >> 3) + 1));
        if (t + 4 < 32) qb[t & 3] = QRD(t + 4);
        if (t >= 21 && t < 25) LDUV(t - 21);
        __builtin_amdgcn_sched_barrier(0);
      }
      const float m = swap32_max(mx16(s));
      if (lane < 32) *(float*)(smem + L_MX + lane * 16 + wave * 4) = m;
    } else {
      const int q = c16 >> 2, p = lane & 3;
      const int base = (4 * hh + q) * 1024 + w4 * 256 + 16 * ((2 * (g & 1) + (p >> 1)) ^ (4 * q + hh)) + 8 * (p & 1);
      auto frags = [&](auto blk) {
        constexpr int kb = decltype(blk)::value;
        await<4>(C_BLK + 4 * kb);
#pragma unroll
        for (int ta = 0; ta < 4; ++ta)
#pragma unroll
          for (int sh = 0; sh < 2; ++sh) {
            const char* pa = smem + kb * 32768 + sh * 16384;
            const v2u a = __builtin_bit_cast(v2u, __builtin_amdgcn_ds_read_tr16_b64_v4i16((LAS s4v*)(pa + (base ^ (64 * ta)))));
            const v2u c = __builtin_bit_cast(v2u, __builtin_amdgcn_ds_read_tr16_b64_v4i16((LAS s4v*)(pa + (base ^ (64 * ta) ^ 32) + 8192)));
            fr[ta * 8 + 2 * kb + sh] = v4u{a.x, a.y, c.x, c.y};
          }
        pin();
      };
      v4u qg[16];
      int qvo = S_H2 + (32 * w4 + 4 * hh) * SLOT + b * 512 + r32 * 16;
      asm volatile("" : "+v"(qvo));
#define LDQ(rq, i) qg[4 * (rq) + (i)] = ld4<AUX_SC1>(ws, qvo, (8 * (i) + (rq)) * SLOT)
      int qw[4];
#pragma unroll
      for (int i = 0; i < 4; ++i) {
        const int hd = 8 * w4 + 2 * i + hh;
        qw[i] = hd * 1024 + 16 * ((r32 >> 1) ^ (((hd & 3) << 2) | ((hd >> 2) & 3))) + 8 * (lane & 1);
        asm volatile("" : "+v"(qw[i]));
      }
      frags(IC<0>{});
      frags(IC<1>{});
      post(C_FREE);
      frags(IC<2>{});
#pragma unroll
      for (int i = 0; i < 16; ++i) { LDQ(i >> 2, i & 3); pin(); }
      await<6>(C_FREE);
#pragma unroll
      for (int rq = 0; rq < 4; ++rq) {
        u32 bad = 0;
#pragma unroll
        for (int i = 0; i < 4; ++i) bad = bad4(bad, qg[4 * rq + i], tag);
        if (__builtin_expect(!none(bad), 0)) {
          NOUNROLL for (int spin = 0; spin < SPIN_GLOBAL; ++spin) {
            pin();
            bad = 0;
#pragma unroll
            for (int i = 0; i < 4; ++i) LDQ(rq, i);
#pragma unroll
            for (int i = 0; i < 4; ++i) bad = bad4(bad, qg[4 * rq + i], tag);
            if (none(bad)) break;
            __builtin_amdgcn_s_sleep(4);
          }
        }
#pragma unroll
        for (int i = 0; i < 4; ++i) *(v2u*)(smem + L_Q + qw[i] + 256 * rq) = v2u{qg[4 * rq + i].x, qg[4 * rq + i].z};
        post(C_QUART(rq));
      }
#pragma unroll
      for (int i = 0; i < 4; ++i) { LDUV(i); pin(); }
      frags(IC<3>{});
#pragma unroll
      for (int k = 0; k < 4; ++k) { voff[k] = ((x + k + 4 * hh) & 7) * PLANE + (4 * hh + k) * 4 * INB + r32 * 4; asm volatile("" : "+v"(voff[k])); }
    }
    bar();
    if (wave < 4) {
      const float m = mx4(*(const f4*)(smem + L_MX + r32 * 16)) * SC;
      u32 pp[8];
#pragma unroll
      for (int i = 0; i < 8; ++i) {
        s[2 * i] = ex2(fmaf(s[2 * i], SC, -m)); s[2 * i + 1] = ex2(fmaf(s[2 * i + 1], SC, -m));
        pp[i] = pk(s[2 * i], s[2 * i + 1]);
      }
      *(v4u*)(smem + L_P + wave * 2048 + lane * 16) = v4u{pp[0], pp[1], pp[2], pp[3]};
      *(v4u*)(smem + L_P + wave * 2048 + 1024 + lane * 16) = v4u{pp[4], pp[5], pp[6], pp[7]};
    }
    bar();
    if (wave < 4) {
      float l = 0.f;
#pragma unroll
      for (int i = 0; i < 8; ++i) l += s[2 * i] + s[2 * i + 1];
      l = swap32_add(l, l);
      if (lane < 32) *(float*)(smem + L_LS + lane * 16 + wave * 4) = l;
      tag = __builtin_amdgcn_readfirstlane(*(const u32*)(smem + C_TAG));
      if (wave == 0) { st4(zero4, ws, lane * 16 + S_HY + (128 + id) * SLOT); wait_vm<0>(); }
      post(C_SUMS);
    }
    pin();
    if (wave >= 4) {
      v4u pf[8];
#pragma unroll
      for (int ks = 0; ks < 8; ++ks) pf[ks] = *(const v4u*)(smem + L_P + ks * 1024 + lane * 16);
      f16v o0, o1, o2, o3;
#pragma unroll
      for (int i = 0; i < 16; ++i) { o0[i] = 0.f; o1[i] = 0.f; o2[i] = 0.f; o3[i] = 0.f; }
      const int soff = W_H3 + w4 * INB + j * 256;
      pin();
#pragma unroll
      for (int ks = 0; ks < 8; ++ks) { o0 = mfma32(pf[ks], fr[ks], o0); o1 = mfma32(pf[ks], fr[8 + ks], o1); }
      __builtin_amdgcn_sched_barrier(0);
#define OST(a, c, i, pi) __builtin_amdgcn_raw_buffer_store_b32(pk(a[i], c[i]), ws, voff[(i) & 3] + (pi) * 128, soff + ((i) >> 2) * 32 * INB, AUX_SC1)
#pragma unroll
      for (int ks = 0; ks < 8; ++ks) {
        o2 = mfma32(pf[ks], fr[16 + ks], o2);
        __builtin_amdgcn_sched_barrier(0);
        if (ks) OST(o0, o1, 2 * ks - 2, 0);
        __builtin_amdgcn_sched_barrier(0);
        o3 = mfma32(pf[ks], fr[24 + ks], o3);
        __builtin_amdgcn_sched_barrier(0);
        if (ks) OST(o0, o1, 2 * ks - 1, 0);
        __builtin_amdgcn_sched_barrier(0);
      }
      OST(o0, o1, 14, 0); OST(o0, o1, 15, 0);
#pragma unroll
      for (int i = 0; i < 16; ++i) OST(o2, o3, i, 1);
      await<4>(C_SUMS);
      const int hd = (lane >> 3) * 4 + (lane & 3);
      const f4 ll = *(const f4*)(smem + L_LS + hd * 16);
      v4u sv = gran(mx4(*(const f4*)(smem + L_MX + hd * 16)) * SC, (ll[0] + ll[1]) + (ll[2] + ll[3]), tag);
      int fv = lane * 16 + S_HY + (128 + id) * SLOT;
      asm volatile("s_waitcnt vmcnt(0)" : "+v"(sv), "+v"(fv)::"memory");
      if (arrive(C_DRAIN) == 3) st4(sv, ws, fv);
    }
    pin();
    tail_setup(std::false_type{});
    await<4>(C_DRAIN);
    __builtin_amdgcn_s_sleep(D_H3);
    {
      v4u st;
      NOUNROLL for (int spin = 0; spin < SPIN_GLOBAL; ++spin) {
        pin();
        st = ld4<AUX_SC1>(ws, (lane >> 2) * SLOT, S_HY + (128 + wave * 16) * SLOT + x * 128 + (h & 3) * 16);
        if (none(bad4(0u, st, tag))) break;
        __builtin_amdgcn_s_sleep(P_H3);
      }
      pin();
      v4u d[4];
#pragma unroll
      for (int i = 0; i < 4; ++i) d[i] = ld4<AUX_SC1>(ws, g * 1024 + c16 * 16 + i * 256, W_H3 + ((wave + h) & 7) * PLANE + id * INB);
      pin();
      const float M = swap32_max(swap16_max(row4_max(uf(st.x))));
      float wg = ex2(uf(st.x) - M);
      float L = row4_sum(wg * uf(st.z));
      L = swap16_add(L, L); L = swap32_add(L, L);
      if (lane == 0) *(f2*)(smem + L_ST + wave * 8) = f2{M, L};
      bar();
      const f4 s0 = *(const f4*)(smem + L_ST + (wave >> 2) * 32), s1 = *(const f4*)(smem + L_ST + (wave >> 2) * 32 + 16);
      const float MM = mx(mx(s0[0], s0[2], s1[0]), s1[2]);
      const float LL = fmaf(ex2(s0[0] - MM), s0[1], fmaf(ex2(s0[2] - MM), s0[3], fmaf(ex2(s1[0] - MM), s1[1], ex2(s1[2] - MM) * s1[3])));
      wg *= ex2(M - MM) * __builtin_amdgcn_rcpf(LL);
      const u32 w01 = pk(dppf<BCAST>(wg), dppf<BCAST + 4>(wg)), w23 = pk(dppf<BCAST + 8>(wg), dppf<BCAST + 12>(wg));
      v4u af[4] = {diag(w01, sel0), diag(w01, sel1), diag(w23, sel0), diag(w23, sel1)};
      asm volatile("" : "+v"(af[0]), "+v"(af[1]), "+v"(af[2]), "+v"(af[3]));
      pin();
      f4 lat = {0.f, 0.f, 0.f, 0.f};
#pragma unroll
      for (int i = 0; i < 4; ++i) lat = mfma16(af[i], d[i], lat);
      if (lane < 32) *(f4*)(smem + L_LAT + wave * 512 + c16 * 32 + g * 16) = lat;
      pin();
      if (r == 3) load_we();
    }
    bar();
    {
      const char* p = smem + L_LAT + bsel * 2048 + wave * 64 + g * 16;
      const f4 c = (*(const f4*)p + *(const f4*)(p + 512)) + (*(const f4*)(p + 1024) + *(const f4*)(p + 1536));
      *(f4*)(smem + L_R2 + redv) = rows16(pk(c[0], c[1]), pk(c[2], c[3]), wv, sel0, sel1);
    }
    tail(std::false_type{});
  }
}

extern "C" void kernel_launch(void* const* d_in, const int* in_sizes, int n_in, void* d_out, int out_size, void* d_ws, size_t ws_size,
                              hipStream_t stream) {
  Params p;
  p.role[0] = Role{d_in[5], d_in[7], d_ws, d_in[8], d_in[1], d_in[2], d_in[4], d_out};
  p.role[1] = Role{d_in[0], d_in[3], d_ws, d_in[6], d_in[9], d_in[9], d_in[10], (char*)d_out + 1152};
  fused_decode<<<256, 512, 0, stream>>>(p);
}
```
